```python
import torch

def _softmax(x, axis=-1):
    return torch.softmax(x, dim=axis)
def _gelu(x):
    return torch.nn.functional.gelu(x, approximate="tanh")
def _silu(x):
    return torch.nn.functional.silu(x)
def _sigmoid(x):
    return torch.sigmoid(x)
def _layernorm(x, axis=-1, eps=1e-5):
    m = x.mean(dim=axis, keepdim=True)
    v = x.var(dim=axis, keepdim=True, unbiased=False)
    return (x - m) / torch.sqrt(v + eps)
def _scatter_add(tgt, idx, src, axis=0):
    return tgt.clone().index_add_(axis, idx, src)
def _segment_sum(src, seg, n):
    out = torch.zeros((n,) + src.shape[1:], dtype=src.dtype)
    return out.index_add_(0, seg, src)
def _pad(x, pw):
    flat = tuple(v for pair in reversed(pw) for v in pair)
    return torch.nn.functional.pad(x, flat)

kernel_name = 'gated_mix__linear_kernel_hedgehog__glu_gated_geglu__prompt'

def setup_inputs():

    _w = (lambda a: (lambda t: t.to(torch.bfloat16) if t.is_floating_point() else t)(torch.as_tensor(a)))
    def _A_setup():
        B, S, D, H = 1, 2048, 448, 16
        Dh = D // H
        return {
            "x": _w((lambda *s: torch.randn(*s, dtype=torch.bfloat16) * 0.05)(B, S, D)),
            "wqkv": _w((lambda *s: torch.randn(*s, dtype=torch.bfloat16) * 0.8)(D, 3 * D)),
            "w_phi": _w((lambda *s: torch.randn(*s, dtype=torch.bfloat16) * 0.8)(H, Dh, Dh) * (Dh ** -0.5)),
            "wo": _w((lambda *s: torch.randn(*s, dtype=torch.bfloat16) * 0.05)(D, D)),
        }
    _A_in = _A_setup()

    def _B_setup():
        B, S, Dm, Df = 1, 2048, 448, 1792
        return {
            "x": _w((lambda *s: torch.randn(*s, dtype=torch.bfloat16) * 0.05)(B, S, Dm)),
            "w_gate": _w((lambda *s: torch.randn(*s, dtype=torch.bfloat16) * 1.6)(Dm, Df) * (Dm ** -0.5)),
            "w_up": _w((lambda *s: torch.randn(*s, dtype=torch.bfloat16) * 1.6)(Dm, Df) * (Dm ** -0.5)),
            "w_down": _w((lambda *s: torch.randn(*s, dtype=torch.bfloat16) * 1.6)(Df, Dm) * (Df ** -0.5)),
        }
    _B_in = _B_setup()

    return {
        "A_x": _A_in["x"],
        "A_wqkv": _A_in["wqkv"],
        "A_w_phi": _A_in["w_phi"],
        "A_wo": _A_in["wo"],
        "B_x": _B_in["x"],
        "B_w_gate": _B_in["w_gate"],
        "B_w_up": _B_in["w_up"],
        "B_w_down": _B_in["w_down"],
        "gate_w": _w((lambda *s: torch.randn(*s, dtype=torch.bfloat16) * 0.05)(448) * 16.0),
    }

def reference(A_x, A_wqkv, A_w_phi, A_wo, B_x, B_w_gate, B_w_up, B_w_down, gate_w):
    def _A_ref(x, wqkv, w_phi, wo):
        B, S, D = x.shape
        H = 16; Dh = D // H
        qkv = (lambda x, *d: x.permute(*d))((x @ wqkv).reshape(B, S, 3, H, Dh), 2, 0, 3, 1, 4)
        q, k, v = qkv[0], qkv[1], qkv[2]
        pq = torch.einsum("bhsd,hdf->bhsf", q, w_phi)
        pk = torch.einsum("bhsd,hdf->bhsf", k, w_phi)
        phi_q = torch.concatenate([_softmax(pq, axis=-1), _softmax(-pq, axis=-1)], axis=-1)
        phi_k = torch.concatenate([_softmax(pk, axis=-1), _softmax(-pk, axis=-1)], axis=-1)
        kv = torch.einsum("bhsf,bhsd->bhfd", phi_k, v)
        z  = torch.einsum("bhsf->bhf", phi_k)
        num = torch.einsum("bhqf,bhfd->bhqd", phi_q, kv)
        den = torch.einsum("bhqf,bhf->bhq", phi_q, z)[..., None] + 1e-6
        o = num / den
        return (lambda x, *d: x.permute(*d))(o, 0, 2, 1, 3).reshape(B, S, D) @ wo

    def _B_ref(x, w_gate, w_up, w_down):
        g = torch.einsum("bsd,df->bsf", x, w_gate)
        u = torch.einsum("bsd,df->bsf", x, w_up)
        h = _gelu(g) * u
        return torch.einsum("bsf,fd->bsd", h, w_down)

    x = A_x
    g = _sigmoid((x * gate_w).sum(-1, keepdims=True))
    a = _A_ref(x, A_wqkv, A_w_phi, A_wo)
    b = _B_ref(x, B_w_gate, B_w_up, B_w_down)
    return g * a + (1 - g) * b
```

```cpp
#include <hip/hip_runtime.h>
#include <hip/hip_bf16.h>
#include <stdint.h>

typedef unsigned short u16;
static __device__ __forceinline__ float bf2f(u16 b){ return __uint_as_float(((unsigned)b)<<16); }
static __device__ __forceinline__ u16 f2bf(float f){ __hip_bfloat16 h = __float2bfloat16(f); return *(u16*)&h; }
static __device__ __forceinline__ float rbf(float f){ return bf2f(f2bf(f)); }

constexpr int S=2048, D=448, H=16, DH=28, DF=1792;

__global__ void gemm_naive(const u16* A, const u16* B, u16* C, int M, int N, int K){
  int n = blockIdx.x*blockDim.x+threadIdx.x; int m = blockIdx.y;
  if(n>=N) return;
  float acc=0.f;
  for(int k=0;k<K;++k) acc += bf2f(A[m*K+k])*bf2f(B[k*N+n]);
  C[m*N+n]=f2bf(acc);
}
__global__ void phi_kernel(const u16* qkv, const u16* wphi, u16* phiq, u16* phik){
  int idx = blockIdx.x*blockDim.x+threadIdx.x;
  if(idx>=S*32) return;
  int h = idx&15, which=(idx>>4)&1, s=idx>>5;
  const u16* src = qkv + s*3*D + which*D + h*DH;
  u16* dst = (which? phik: phiq) + (s*H+h)*56;
  float mx=-1e30f, mn=1e30f;
  for(int f=0;f<DH;++f){ float a=0.f; for(int d=0;d<DH;++d) a+=bf2f(src[d])*bf2f(wphi[(h*DH+d)*DH+f]); a=rbf(a); dst[f]=f2bf(a); mx=fmaxf(mx,a); mn=fminf(mn,a);}
  float s1=0.f,s2=0.f;
  for(int f=0;f<DH;++f){ float a=bf2f(dst[f]); s1+=__expf(a-mx); s2+=__expf(mn-a);}
  for(int f=0;f<DH;++f){ float a=bf2f(dst[f]); dst[f]=f2bf(__expf(a-mx)/s1); dst[DH+f]=f2bf(__expf(mn-a)/s2);}
}
__global__ void kv_kernel(const u16* qkv, const u16* phik, float* kv){
  int idx = blockIdx.x*blockDim.x+threadIdx.x;
  if(idx>=H*56*29) return;
  int d = idx%29, f=(idx/29)%56, h=idx/(29*56);
  float a=0.f;
  for(int s=0;s<S;++s){ float v = d<28? bf2f(qkv[s*3*D+2*D+h*DH+d]) : 1.f; a += bf2f(phik[(s*H+h)*56+f])*v; }
  kv[idx]=rbf(a);
}
__global__ void o_kernel(const u16* phiq, const float* kv, u16* o){
  int idx = blockIdx.x*blockDim.x+threadIdx.x;
  if(idx>=S*D) return;
  int c = idx%D, s=idx/D, h=c/DH, d=c%DH;
  float num=0.f, den=0.f;
  for(int f=0;f<56;++f){ float p=bf2f(phiq[(s*H+h)*56+f]); num+=p*kv[(h*56+f)*29+d]; den+=p*kv[(h*56+f)*29+28]; }
  o[idx]=f2bf(rbf(num)/rbf(rbf(den)+1e-6f));
}
__global__ void h_kernel(const u16* g, const u16* u, u16* hh){
  int idx = blockIdx.x*blockDim.x+threadIdx.x;
  if(idx>=S*DF) return;
  float x=bf2f(g[idx]); float t=tanhf(0.7978845608028654f*(x+0.044715f*x*x*x));
  hh[idx]=f2bf(rbf(0.5f*x*(1.f+t))*bf2f(u[idx]));
}
__global__ void fin_kernel(const u16* x, const u16* gw, const u16* a, const u16* b, u16* out){
  int s=blockIdx.x; __shared__ float red[64]; float p=0.f;
  for(int k=threadIdx.x;k<D;k+=64) p+=rbf(bf2f(x[s*D+k])*bf2f(gw[k]));
  red[threadIdx.x]=p; __syncthreads();
  float t=0.f; for(int i=0;i<64;++i) t+=red[i];
  t=rbf(t); float g=rbf(1.f/(1.f+__expf(-t))); float og=rbf(1.f-g);
  for(int k=threadIdx.x;k<D;k+=64) out[s*D+k]=f2bf(rbf(g*bf2f(a[s*D+k]))+rbf(og*bf2f(b[s*D+k])));
}

extern "C" void kernel_launch(void* const* d_in, const int* in_sizes, int n_in,
                              void* d_out, int out_size, void* d_ws, size_t ws_size,
                              hipStream_t stream) {
  const u16* x=(const u16*)d_in[0]; const u16* wqkv=(const u16*)d_in[1]; const u16* wphi=(const u16*)d_in[2];
  const u16* wo=(const u16*)d_in[3]; const u16* wg=(const u16*)d_in[5]; const u16* wu=(const u16*)d_in[6];
  const u16* wd=(const u16*)d_in[7]; const u16* gw=(const u16*)d_in[8];
  char* ws=(char*)d_ws; size_t MB=1<<20;
  u16* qkv=(u16*)(ws); u16* phiq=(u16*)(ws+8*MB); u16* phik=(u16*)(ws+16*MB); float* kv=(float*)(ws+24*MB);
  u16* o=(u16*)(ws+32*MB); u16* a=(u16*)(ws+40*MB); u16* g=(u16*)(ws+48*MB); u16* u=(u16*)(ws+56*MB); u16* hh=(u16*)(ws+64*MB); u16* b=(u16*)(ws+72*MB);
  gemm_naive<<<dim3((3*D+63)/64,S),64,0,stream>>>(x,wqkv,qkv,S,3*D,D);
  phi_kernel<<<S*32/64,64,0,stream>>>(qkv,wphi,phiq,phik);
  kv_kernel<<<(H*56*29+63)/64,64,0,stream>>>(qkv,phik,kv);
  o_kernel<<<S*D/64,64,0,stream>>>(phiq,kv,o);
  gemm_naive<<<dim3(D/64,S),64,0,stream>>>(o,wo,a,S,D,D);
  gemm_naive<<<dim3(DF/64,S),64,0,stream>>>(x,wg,g,S,DF,D);
  gemm_naive<<<dim3(DF/64,S),64,0,stream>>>(x,wu,u,S,DF,D);
  h_kernel<<<S*DF/64,64,0,stream>>>(g,u,hh);
  gemm_naive<<<dim3(D/64,S),64,0,stream>>>(hh,wd,b,S,D,DF);
  fin_kernel<<<S,64,0,stream>>>(x,gw,a,b,(u16*)d_out);
}
```

```cpp
#include <hip/hip_runtime.h>
#include <hip/hip_bf16.h>
#include <stdint.h>

typedef unsigned short u16;
static __device__ __forceinline__ float bf2f(u16 b){ return __uint_as_float(((unsigned)b)<<16); }
static __device__ __forceinline__ u16 f2bf(float f){ __hip_bfloat16 h = __float2bfloat16(f); return *(u16*)&h; }
static __device__ __forceinline__ float rbf(float f){ return bf2f(f2bf(f)); }

constexpr int S=2048, D=448, H=16, DH=28, DF=1792;

__global__ void gemm_naive(const u16* A, const u16* B, u16* C, int M, int N, int K){
  int n = blockIdx.x*blockDim.x+threadIdx.x; int m = blockIdx.y;
  if(n>=N) return;
  float acc=0.f;
  for(int k=0;k<K;++k) acc += bf2f(A[m*K+k])*bf2f(B[k*N+n]);
  C[m*N+n]=f2bf(acc);
}
__global__ void phi_kernel(const u16* qkv, const u16* wphi, u16* phiq, u16* phik){
  int idx = blockIdx.x*blockDim.x+threadIdx.x;
  if(idx>=S*32) return;
  int h = idx&15, which=(idx>>4)&1, s=idx>>5;
  const u16* src = qkv + s*3*D + which*D + h*DH;
  u16* dst = (which? phik: phiq) + (s*H+h)*56;
  float mx=-1e30f, mn=1e30f;
  for(int f=0;f<DH;++f){ float a=0.f; for(int d=0;d<DH;++d) a+=bf2f(src[d])*bf2f(wphi[(h*DH+d)*DH+f]); a=rbf(a); dst[f]=f2bf(a); mx=fmaxf(mx,a); mn=fminf(mn,a);}
  float s1=0.f,s2=0.f;
  for(int f=0;f<DH;++f){ float a=bf2f(dst[f]); s1+=__expf(a-mx); s2+=__expf(mn-a);}
  for(int f=0;f<DH;++f){ float a=bf2f(dst[f]); dst[f]=f2bf(__expf(a-mx)/s1); dst[DH+f]=f2bf(__expf(mn-a)/s2);}
}
__global__ void kv_kernel(const u16* qkv, const u16* phik, float* kv){
  int idx = blockIdx.x*blockDim.x+threadIdx.x;
  if(idx>=H*56*29) return;
  int d = idx%29, f=(idx/29)%56, h=idx/(29*56);
  float a=0.f;
  for(int s=0;s<S;++s){ float v = d<28? bf2f(qkv[s*3*D+2*D+h*DH+d]) : 1.f; a += bf2f(phik[(s*H+h)*56+f])*v; }
  kv[idx]=rbf(a);
}
__global__ void o_kernel(const u16* phiq, const float* kv, u16* o){
  int idx = blockIdx.x*blockDim.x+threadIdx.x;
  if(idx>=S*D) return;
  int c = idx%D, s=idx/D, h=c/DH, d=c%DH;
  float num=0.f, den=0.f;
  for(int f=0;f<56;++f){ float p=bf2f(phiq[(s*H+h)*56+f]); num+=p*kv[(h*56+f)*29+d]; den+=p*kv[(h*56+f)*29+28]; }
  o[idx]=f2bf(rbf(num)/rbf(rbf(den)+1e-6f));
}
__global__ void h_kernel(const u16* g, const u16* u, u16* hh){
  int idx = blockIdx.x*blockDim.x+threadIdx.x;
  if(idx>=S*DF) return;
  float x=bf2f(g[idx]); float t=tanhf(0.7978845608028654f*(x+0.044715f*x*x*x));
  hh[idx]=f2bf(rbf(0.5f*x*(1.f+t))*bf2f(u[idx]));
}
__global__ void fin_kernel(const u16* x, const u16* gw, const u16* a, const u16* b, u16* out){
  int s=blockIdx.x; __shared__ float red[64]; float p=0.f;
  for(int k=threadIdx.x;k<D;k+=64) p+=rbf(bf2f(x[s*D+k])*bf2f(gw[k]));
  red[threadIdx.x]=p; __syncthreads();
  float t=0.f; for(int i=0;i<64;++i) t+=red[i];
  t=rbf(t); float g=rbf(1.f/(1.f+__expf(-t))); float og=rbf(1.f-g);
  for(int k=threadIdx.x;k<D;k+=64) out[s*D+k]=f2bf(rbf(g*bf2f(a[s*D+k]))+rbf(og*bf2f(b[s*D+k])));
}


typedef unsigned v4u __attribute__((ext_vector_type(4)));
template<int NT> __global__ void probe(const v4u* p, long n, unsigned* sink){
  unsigned acc=0; long i=blockIdx.x*(long)blockDim.x+threadIdx.x; long st=(long)gridDim.x*blockDim.x;
  for(;i+3*st<n;i+=4*st){ v4u a,b,c,d; if(NT){a=__builtin_nontemporal_load(p+i); b=__builtin_nontemporal_load(p+i+st); c=__builtin_nontemporal_load(p+i+2*st); d=__builtin_nontemporal_load(p+i+3*st);} else {a=p[i];b=p[i+st];c=p[i+2*st];d=p[i+3*st];} acc+=a.x^b.y^c.z^d.w; }
  if(acc==0x12345678u) *sink=acc;
}

extern "C" void kernel_launch(void* const* d_in, const int* in_sizes, int n_in,
                              void* d_out, int out_size, void* d_ws, size_t ws_size,
                              hipStream_t stream) {
  const u16* x=(const u16*)d_in[0]; const u16* wqkv=(const u16*)d_in[1]; const u16* wphi=(const u16*)d_in[2];
  const u16* wo=(const u16*)d_in[3]; const u16* wg=(const u16*)d_in[5]; const u16* wu=(const u16*)d_in[6];
  const u16* wd=(const u16*)d_in[7]; const u16* gw=(const u16*)d_in[8];
  char* ws=(char*)d_ws; size_t MB=1<<20;
  u16* qkv=(u16*)(ws); u16* phiq=(u16*)(ws+8*MB); u16* phik=(u16*)(ws+16*MB); float* kv=(float*)(ws+24*MB);
  u16* o=(u16*)(ws+32*MB); u16* a=(u16*)(ws+40*MB); u16* g=(u16*)(ws+48*MB); u16* u=(u16*)(ws+56*MB); u16* hh=(u16*)(ws+64*MB); u16* b=(u16*)(ws+72*MB);
  probe<0><<<2048,256,0,stream>>>((const v4u*)(ws),120l*MB/16,(unsigned*)(ws+250*MB));
  gemm_naive<<<dim3((3*D+63)/64,S),64,0,stream>>>(x,wqkv,qkv,S,3*D,D);
  phi_kernel<<<S*32/64,64,0,stream>>>(qkv,wphi,phiq,phik);
  kv_kernel<<<(H*56*29+63)/64,64,0,stream>>>(qkv,phik,kv);
  o_kernel<<<S*D/64,64,0,stream>>>(phiq,kv,o);
  gemm_naive<<<dim3(D/64,S),64,0,stream>>>(o,wo,a,S,D,D);
  gemm_naive<<<dim3(DF/64,S),64,0,stream>>>(x,wg,g,S,DF,D);
  gemm_naive<<<dim3(DF/64,S),64,0,stream>>>(x,wu,u,S,DF,D);
  h_kernel<<<S*DF/64,64,0,stream>>>(g,u,hh);
  gemm_naive<<<dim3(D/64,S),64,0,stream>>>(hh,wd,b,S,D,DF);
  fin_kernel<<<S,64,0,stream>>>(x,gw,a,b,(u16*)d_out);
}
```

```cpp
#include <hip/hip_runtime.h>
#include <stdint.h>

#define NOWAIT 0
#define PRIO2 3
#define STAMPS 0

namespace hk {
typedef unsigned u32;
typedef unsigned long long u64;
typedef __bf16 bf16x8 __attribute__((ext_vector_type(8)));
typedef __bf16 bf16x2 __attribute__((ext_vector_type(2)));
typedef short s16x4 __attribute__((ext_vector_type(4)));
typedef short s16x8 __attribute__((ext_vector_type(8)));
typedef u32 u32x4 __attribute__((ext_vector_type(4)));
typedef u32 u32x2 __attribute__((ext_vector_type(2)));
typedef float f32x16 __attribute__((ext_vector_type(16)));
typedef float f32x4 __attribute__((ext_vector_type(4)));
typedef float f32x2 __attribute__((ext_vector_type(2)));
typedef __amdgpu_buffer_rsrc_t srd_t;
#define LAS __attribute__((address_space(3)))
#define DI __device__ __forceinline__

constexpr int S = 2048, D = 448, DF = 1792;
constexpr u32 WS_E = 0;
constexpr u32 WS_FH = 4096;
constexpr u32 WS_FP = WS_FH + 2 * 64 * 128;
constexpr u32 WS_FF = WS_FP + 16 * 4 * 128;
constexpr u32 WS_FO = WS_FF + 16 * 64 + 1024;
constexpr u32 WS_OMG = 65536;
constexpr u32 WS_KVF = 131072;
constexpr u32 WS_KVP = 262144;
constexpr u32 WS_O = WS_KVP + 16 * 16 * 8192;
constexpr u32 WS_H = WS_O + 64 * 32 * 1024;
constexpr u32 WS_END = WS_H + 64 * 112 * 1024;
constexpr u32 WS_STAMP = 16u << 20;
constexpr u32 L_SCR = 7 * 16384;
constexpr u32 L_WPHI = L_SCR + 32768;
constexpr u32 L_GW = L_WPHI + 13312;
constexpr u32 L_G = L_GW + 1024;
constexpr u32 L_FULL = L_G + 512;
constexpr u32 L_PROG = L_FULL + 16;
constexpr u32 L_SYNC = L_PROG + 16;
constexpr u32 L_FULK = L_SYNC + 16;
constexpr u32 L_TOTAL = 163840;

struct Params {
  const char *x, *wqkv, *wphi, *wo, *wg, *wu, *wd, *gw;
  char *out, *ws;
};

DI u32 rfl(u32 v) { return __builtin_amdgcn_readfirstlane(v); }
DI srd_t mksrd(const void* p, u32 bytes) { return __builtin_amdgcn_make_buffer_rsrc((void*)p, 0, bytes, 0x00020000); }
DI u32 cvtpk(float lo, float hi) { f32x2 v = {lo, hi}; return __builtin_bit_cast(u32, __builtin_convertvector(v, bf16x2)); }
DI f32x16 mfma(u32x4 a, u32x4 b, f32x16 c) {
  return __builtin_amdgcn_mfma_f32_32x32x16_bf16(__builtin_bit_cast(bf16x8, a), __builtin_bit_cast(bf16x8, b), c, 0, 0, 0);
}
DI u32x2 tr64(u32 addr) { return __builtin_bit_cast(u32x2, __builtin_amdgcn_ds_read_tr16_b64_v4i16((LAS s16x4*)(size_t)addr)); }
DI u32x4 trfrag(u32 a0, u32 a1) { u32x2 lo = tr64(a0), hi = tr64(a1); return u32x4{lo.x, lo.y, hi.x, hi.y}; }
DI u32x4 lds128(u32 addr) { return *(LAS u32x4*)(size_t)addr; }
DI void sts128(u32 addr, u32x4 v) { *(LAS u32x4*)(size_t)addr = v; }
DI void sts64(u32 addr, u32x2 v) { *(LAS u32x2*)(size_t)addr = v; }
#define MEMFENCE() asm volatile("" ::: "memory")
#define VMWAIT(n) asm volatile("s_waitcnt vmcnt(%0)" ::"n"(n) : "memory")
DI void dma_buf(u32 ldsaddr, u32 voff, srd_t srd, u32 soff) {
  asm volatile("s_mov_b32 m0, %0\n\ts_nop 0\n\tbuffer_load_dwordx4 %1, %2, %3 offen lds" ::"s"(ldsaddr), "v"(voff), "s"(srd), "s"(soff) : "memory");
}
DI void dma_glb(u32 ldsaddr, const char* p) {
  asm volatile("s_mov_b32 m0, %0\n\ts_nop 0\n\tglobal_load_lds_dwordx4 %1, off" ::"s"(ldsaddr), "v"(p) : "memory");
}
DI void ldg_sc1(u32x4& dst, u32 voff, srd_t srd, u32 soff) {
  asm volatile("buffer_load_dwordx4 %0, %1, %2, %3 offen sc1" : "=v"(dst) : "v"(voff), "s"(srd), "s"(soff) : "memory");
}
DI void stg_sc1(u32x4 v, u32 voff, srd_t srd, u32 soff) {
  asm volatile("buffer_store_dwordx4 %0, %1, %2, %3 offen sc1\n\ts_nop 1" ::"v"(v), "v"(voff), "s"(srd), "s"(soff) : "memory");
}
DI void stg64_sc1(u32x2 v, u32 voff, srd_t srd, u32 soff) {
  asm volatile("buffer_store_dwordx2 %0, %1, %2, %3 offen sc1" ::"v"(v), "v"(voff), "s"(srd), "s"(soff) : "memory");
}
DI void stg32_sc1(u32 v, u32 voff, srd_t srd, u32 soff) {
  asm volatile("buffer_store_dword %0, %1, %2, %3 offen sc1" ::"v"(v), "v"(voff), "s"(srd), "s"(soff) : "memory");
}
DI void lds_post(u32 addr, u32 val) {
  asm volatile("s_mov_b64 exec, 1\n\tds_write_b32 %0, %1\n\ts_mov_b64 exec, -1" ::"v"(addr), "v"(val) : "memory");
}
template <int T> DI void lds_wait(u32 a) {
  u32 v, cnt;
  asm volatile("s_mov_b32 %1, 0x400000\n"
               ".Lsp%=:\n\tds_read_b32 %0, %2\n\ts_waitcnt lgkmcnt(0)\n\tv_cmp_gt_u32 vcc, %3, %0\n\ts_cbranch_vccz .Lse%=\n\t"
               "s_sub_u32 %1, %1, 1\n\ts_cbranch_scc0 .Lsp%=\n"
               ".Lse%=:"
               : "=&v"(v), "=&s"(cnt) : "v"(a), "n"(T) : "vcc", "scc", "memory");
}
template <int T> DI void lds_wait_pre(u32 a, u32 v) {
  u32 cnt;
  asm volatile("v_cmp_gt_u32 vcc, %3, %0\n\ts_cbranch_vccz .Lse%=\n\ts_mov_b32 %1, 0x400000\n"
               ".Lsp%=:\n\tds_read_b32 %0, %2\n\ts_waitcnt lgkmcnt(0)\n\tv_cmp_gt_u32 vcc, %3, %0\n\ts_cbranch_vccz .Lse%=\n\t"
               "s_sub_u32 %1, %1, 1\n\ts_cbranch_scc0 .Lsp%=\n"
               ".Lse%=:"
               : "+v"(v), "=&s"(cnt) : "v"(a), "n"(T) : "vcc", "scc", "memory");
}
DI u32 lds_peek(u32 a) { return *(volatile LAS u32*)(size_t)a; }
DI void flag_peek(u64& v, srd_t ws, u32 voff) {
  asm volatile("buffer_load_dwordx2 %0, %1, %2, 0 offen sc1" : "=v"(v) : "v"(voff), "s"(ws) : "memory");
}
template <int N> DI void flag_check(u64 v, srd_t ws, u32 voff, u64 tag) {
  u32 cnt;
  asm volatile("s_waitcnt vmcnt(%5)\n\t"
               "v_cmp_ne_u64 vcc, %4, %0\n\ts_cbranch_vccz .Lfe%=\n\ts_mov_b32 %1, 0x8000\n"
               ".Lfp%=:\n\tbuffer_load_dwordx2 %0, %2, %3, 0 offen sc1\n\ts_waitcnt vmcnt(0)\n\tv_cmp_ne_u64 vcc, %4, %0\n\ts_cbranch_vccz .Lfe%=\n\t"
               "s_sleep 2\n\ts_sub_u32 %1, %1, 1\n\ts_cbranch_scc0 .Lfp%=\n"
               ".Lfe%=:"
               : "+v"(v), "=&s"(cnt) : "v"(voff), "s"(ws), "s"(tag), "n"(N) : "vcc", "scc", "memory");
}
DI void flag_wait(srd_t ws, u32 voff, u64 tag) {
  u64 v; u32 cnt;
  asm volatile("s_mov_b32 %1, 0x8000\n"
               ".Lfp%=:\n\tbuffer_load_dwordx2 %0, %2, %3, 0 offen sc1\n\ts_waitcnt vmcnt(0)\n\tv_cmp_ne_u64 vcc, %4, %0\n\ts_cbranch_vccz .Lfe%=\n\t"
               "s_sleep 2\n\ts_sub_u32 %1, %1, 1\n\ts_cbranch_scc0 .Lfp%=\n"
               ".Lfe%=:"
               : "=&v"(v), "=&s"(cnt) : "v"(voff), "s"(ws), "s"(tag) : "vcc", "scc", "memory");
}
DI void flag_set(srd_t ws, u32 off, u64 tag) {
  asm volatile("s_mov_b64 exec, 1\n\tbuffer_store_dwordx2 %0, off, %1, %2 sc1\n\ts_mov_b64 exec, -1" ::"v"(tag), "s"(ws), "s"(off) : "memory");
}
#define SB() __builtin_amdgcn_sched_barrier(0)
#define OPAQUE(x) asm volatile("" : "+v"(x))
DI void interleave4() {
#pragma unroll
  for (int i = 0; i < 4; ++i) { __builtin_amdgcn_sched_group_barrier(0x8, 1, 0); __builtin_amdgcn_sched_group_barrier(0x100, 2, 0); }
}
DI float swap_hi(float lo_wants, int h) {
  auto r = __builtin_amdgcn_permlane32_swap(__float_as_uint(lo_wants), __float_as_uint(lo_wants), false, false);
  (void)h;
  return __uint_as_float(r[1]);
}
DI void both_halves(float v, float& lo, float& hi) {
  auto r = __builtin_amdgcn_permlane32_swap(__float_as_uint(v), __float_as_uint(v), false, false);
  lo = __uint_as_float(r[0]); hi = __uint_as_float(r[1]);
}
#define STAMP(i) do {} while (0)
#define STAMP_FLUSH() do {} while (0)
#define STAMP_ARG
#define STAMP_PASS

#define FSTAMP(i) do {} while (0)
constexpr float LOG2E = 1.4426950408889634f;

template <int I> struct IC { static constexpr int v = I; };
template <int N, class F, int I = 0> DI void sfor(F f) { if constexpr (I < N) { f(IC<I>{}); sfor<N, F, I + 1>(f); } }

template <bool FIRST, bool POST, class Hook>
DI void unit_gemm(u32x4 (&xf)[28], f32x16 (&acc)[4], int lane, int w, Hook hook STAMP_ARG) {
  const u32 h = lane >> 5, mat = (lane >> 4) & 1, q = (lane & 15) >> 2, pp = lane & 3;
  const u32 vb0 = (8 * h + q) * 256 + mat * 128 + q * 32 + 8 * pp;
  const u32 r = lane & 31;
  const u32 xb = w * 4096 + r * 128 + ((h ^ ((r >> 1) & 7)) * 16);
  const u32 fa = L_FULL + (lane & 3) * 4, pa = L_PROG + w * 4;
  u32 vb[4], pre = 0;
  u32x4 fr[2][4];
  auto stage_open = [&](auto sc) {
    constexpr int s = decltype(sc)::v, item = FIRST ? 2 * s + 1 : 14 + s;
    FSTAMP((FIRST ? 16 : 32) + 2 * s);
    lds_wait_pre<item + 1>(fa, pre);
    FSTAMP((FIRST ? 16 : 32) + 2 * s + 1);
#pragma unroll
    for (int c = 0; c < 4; ++c) { vb[c] = (vb0 ^ (c * 32)) + (item % 7) * 16384; OPAQUE(vb[c]); }
    if (FIRST) {
#pragma unroll
      for (int kk = 0; kk < 4; ++kk) xf[4 * s + kk] = lds128(((2 * s) % 7) * 16384 + (xb ^ (kk * 32)));
      lds_post(pa, 2 * s + 1);
    }
    SB();
  };
  auto reads = [&](int buf, int kk) {
#pragma unroll
    for (int c = 0; c < 4; ++c) fr[buf][c] = trfrag(vb[c] + kk * 4096, vb[c] + kk * 4096 + 1024);
  };
#pragma unroll
  for (int c = 0; c < 4; ++c)
#pragma unroll
    for (int e = 0; e < 16; ++e) acc[c][e] = 0.f;
  stage_open(IC<0>{});
  reads(0, 0);
  SB();
  sfor<7>([&](auto sc) {
    constexpr int s = decltype(sc)::v;
#pragma unroll
    for (int kk = 0; kk < 4; ++kk) {
      if (kk == 2 && s < 6) { pre = lds_peek(fa); SB(); }
      if (kk == 3) { if constexpr (s < 6) stage_open(IC<s + 1>{}); }
      if (kk < 3) reads((kk + 1) & 1, kk + 1); else if (s < 6) reads(0, 0);
#pragma unroll
      for (int c = 0; c < 4; ++c) acc[c] = mfma(fr[kk & 1][c], xf[4 * s + kk], acc[c]);
      interleave4();
      SB();
      asm volatile("" : "+v"(acc[0]), "+v"(acc[1]), "+v"(acc[2]), "+v"(acc[3]));
      if (kk == 2) { if (POST) lds_post(pa, FIRST ? 2 * s + 2 : 15 + s); hook(s); SB(); }
    }
  });
}

struct Item { int kind, idx; };
constexpr int NITEM = 26;
constexpr Item ITEMS[NITEM] = {
  {0,0},{1,0},{4,0},{0,1},{1,1},{0,2},{1,2},{0,3},{2,0},{1,3},{0,4},{2,1},{1,4},{0,5},{2,2},{1,5},{0,6},{2,3},{1,6},
  {3,0},{3,1},{3,2},{3,3},{3,4},{3,5},{3,6}};
constexpr int item_n(int it) { return ITEMS[it].kind == 4 ? 9 : ITEMS[it].kind == 2 && ITEMS[it].idx == 3 ? 2 : 4; }
constexpr int item_first(int it) { int n = 0; for (int i = 0; i < it; ++i) n += item_n(i); return n; }
constexpr int item_of(int I) { int it = 0; while (it < NITEM && item_first(it + 1) <= I) ++it; return it; }
constexpr int ring_idx(int it) { return ITEMS[it].kind == 0 ? 2 * ITEMS[it].idx : ITEMS[it].kind == 1 ? 2 * ITEMS[it].idx + 1 : 14 + ITEMS[it].idx; }
constexpr bool needs_prog(int it) { return it < NITEM && ITEMS[it].kind != 2 && ITEMS[it].kind != 4 && ring_idx(it) >= 7; }
constexpr int NLD1 = item_first(NITEM), LAG = 12;
#define F(it) item_first(it)

struct Op2 { int kind, job, i, pre; };
constexpr bool b_in_ring(int job) { return (job >= 14 && job < 28) || job >= 33; }
constexpr int b_off(int job) { return (job < 14 ? job % 5 : job < 28 ? job - 14 : job < 33 ? (job - 28 + 4) % 5 : job - 33) * 2048; }
struct Tab2 { Op2 v[160]; int pos[40]; int n; };
constexpr Tab2 make_ops2() {
  Tab2 t{}; int n = 0;
  auto B = [&](int job, int pre = 0) { t.v[n++] = Op2{0, job, 0, pre}; t.v[n++] = Op2{0, job, 1, 0}; };
  auto A = [&](int job, int pre = 0) { t.v[n++] = Op2{1, job, 0, pre}; t.v[n++] = Op2{1, job, 1, 0}; };
  t.v[n++] = Op2{5, 0, 0, 0};
  for (int j = 0; j < 5; ++j) B(j);
  t.v[n++] = Op2{6, 0, 0, 5};
  for (int j = 0; j < 4; ++j) A(j, j == 0 ? 1 : 0);
  for (int j = 0; j < 36; ++j) {
    t.pos[j] = n;
    if (j < 10) { A(j + 4); if (j < 9) B(j + 5); else B(14, 4); }
    else if (j < 13) { B(15 + 2 * (j - 10)); B(16 + 2 * (j - 10)); }
    else if (j == 13) { for (int k = 21; k < 28; ++k) B(k); t.v[n++] = Op2{5, 1, 0, 0}; for (int k = 28; k < 33; ++k) B(k); for (int k = 14; k < 20; ++k) A(k, k == 14 ? 2 : 0); }
    else if (j < 22) A(j + 6);
    else if (j < 25) { if (j == 22) t.v[n++] = Op2{5, 2, 0, 0}; B(j + 11); }
    else if (j == 25) { t.v[n++] = Op2{7, 0, 0, 3}; t.v[n++] = Op2{7, 0, 1, 0}; A(28); A(29); }
    else if (j < 28) { A(30 + 3 * (j - 26)); A(31 + 3 * (j - 26)); A(32 + 3 * (j - 26)); }
  }
  t.pos[36] = n; t.n = n;
  return t;
}
constexpr Tab2 OPS2 = make_ops2();
constexpr int NOP2 = OPS2.n;
constexpr int job_pos(int j) { return OPS2.pos[j]; }
constexpr int find_op(int kind, int job, int i) { for (int k = 0; k < NOP2; ++k) if (OPS2.v[k].kind == kind && OPS2.v[k].job == job && OPS2.v[k].i == i) return k; return 1000; }
constexpr int cmax(int a, int b) { return a > b ? a : b; }

DI void load_wphi(u32x4 (&wp)[2], u32 base, int lane) {
  const u32 h = lane >> 5, fb = (lane >> 4) & 1, q = (lane & 15) >> 2, pp = lane & 3;
  u32 col = 16 * fb + 4 * pp; if (col == 28) col = 24;
  const u32 a = base + (4 * h + q) * 56 + col * 2;
  wp[0] = trfrag(a, a + 8 * 56);
  u32x2 lo = tr64(a + 16 * 56), hi = tr64(base + (24 + q) * 56 + col * 2);
  wp[1] = u32x4{lo.x, lo.y, h ? 0u : hi.x, h ? 0u : hi.y};
}
DI u32x4 pack8(const f32x16& v, int s) {
  return u32x4{cvtpk(v[8 * s], v[8 * s + 1]), cvtpk(v[8 * s + 2], v[8 * s + 3]), cvtpk(v[8 * s + 4], v[8 * s + 5]), cvtpk(v[8 * s + 6], v[8 * s + 7])};
}
DI float max3(float a, float b, float c) { return fmaxf(fmaxf(a, b), c); }
DI float min3(float a, float b, float c) { return fminf(fminf(a, b), c); }
DI void feature_exp(const f32x16& t, const u32x4 (&wp)[2], f32x16& ep, f32x16& en) {
  f32x16 pz;
#pragma unroll
  for (int e = 0; e < 16; ++e) pz[e] = 0.f;
  pz = mfma(wp[0], pack8(t, 0), pz);
  pz = mfma(wp[1], pack8(t, 1), pz);
  float mx = max3(max3(max3(pz[0], pz[1], pz[2]), max3(pz[3], pz[4], pz[5]), max3(pz[6], pz[7], pz[8])),
                  max3(max3(pz[9], pz[10], pz[11]), max3(pz[12], pz[13], pz[14]), pz[15]), pz[15]);
  float mn = min3(min3(min3(pz[0], pz[1], pz[2]), min3(pz[3], pz[4], pz[5]), min3(pz[6], pz[7], pz[8])),
                  min3(min3(pz[9], pz[10], pz[11]), min3(pz[12], pz[13], pz[14]), pz[15]), pz[15]);
  float a, b;
  both_halves(mx, a, b); mx = fmaxf(a, b) * LOG2E;
  both_halves(mn, a, b); mn = fminf(a, b) * LOG2E;
#pragma unroll
  for (int e = 0; e < 16; ++e) {
    ep[e] = __builtin_amdgcn_exp2f(__builtin_fmaf(pz[e], LOG2E, -mx));
    en[e] = __builtin_amdgcn_exp2f(__builtin_fmaf(pz[e], -LOG2E, mn));
  }
}
DI void normalize(f32x16& v, int h) {
#pragma unroll
  for (int e = 12; e < 16; ++e) v[e] = h ? 0.f : v[e];
  const float s = (((v[0] + v[1]) + (v[2] + v[3])) + ((v[4] + v[5]) + (v[6] + v[7]))) + (((v[8] + v[9]) + (v[10] + v[11])) + ((v[12] + v[13]) + (v[14] + v[15])));
  float a, b; both_halves(s, a, b);
  const float r = __builtin_amdgcn_rcpf(a + b);
#pragma unroll
  for (int e = 0; e < 16; ++e) v[e] *= r;
}

DI void epi_q(const f32x16 (&acc)[4], u32x4 (&ef)[4][2][2], u32 wphi_base, int lane) {
#pragma unroll
  for (int c = 0; c < 4; ++c) {
    u32x4 wp[2]; load_wphi(wp, wphi_base + c * 1568, lane);
    f32x16 ep, en; feature_exp(acc[c], wp, ep, en);
    ef[c][0][0] = pack8(ep, 0); ef[c][0][1] = pack8(ep, 1);
    ef[c][1][0] = pack8(en, 0); ef[c][1][1] = pack8(en, 1);
    asm volatile("" : "+v"(ef[c][0][0]), "+v"(ef[c][0][1]), "+v"(ef[c][1][0]), "+v"(ef[c][1][1]));
  }
}
DI void stage_o(const u32x4 (&ef)[4][2][2], int head0, int rt32, float g, srd_t ws, u64 tag, int lane) {
  const int h = lane >> 5;
  flag_wait(ws, WS_FF + head0 * 64 + (lane & 31) * 8, tag);
  u32x4 kf[4][2][2];
#pragma unroll
  for (int c = 0; c < 4; ++c)
#pragma unroll
    for (int t = 0; t < 2; ++t)
#pragma unroll
      for (int s = 0; s < 2; ++s) ldg_sc1(kf[c][t][s], lane * 16, ws, WS_KVF + ((head0 + c) * 4 + t * 2 + s) * 1024);
  asm volatile("s_waitcnt vmcnt(0)"
               : "+v"(kf[0][0][0]), "+v"(kf[0][0][1]), "+v"(kf[0][1][0]), "+v"(kf[0][1][1]), "+v"(kf[1][0][0]), "+v"(kf[1][0][1]), "+v"(kf[1][1][0]), "+v"(kf[1][1][1]),
                 "+v"(kf[2][0][0]), "+v"(kf[2][0][1]), "+v"(kf[2][1][0]), "+v"(kf[2][1][1]), "+v"(kf[3][0][0]), "+v"(kf[3][0][1]), "+v"(kf[3][1][0]), "+v"(kf[3][1][1])
               :: "memory");
#pragma unroll
  for (int c = 0; c < 4; ++c) {
    f32x16 np, nn;
#pragma unroll
    for (int e = 0; e < 16; ++e) { np[e] = 0.f; nn[e] = 0.f; }
    np = mfma(kf[c][0][0], ef[c][0][0], np); nn = mfma(kf[c][1][0], ef[c][1][0], nn);
    np = mfma(kf[c][0][1], ef[c][0][1], np); nn = mfma(kf[c][1][1], ef[c][1][1], nn);
    const float rp = __builtin_amdgcn_rcpf(np[13]), rn = __builtin_amdgcn_rcpf(nn[13]);
    const float inv = __builtin_amdgcn_rcpf(np[12] * rp + nn[12] * rn + 1e-6f);
    const float cp = swap_hi(rp * inv, h) * g, cn = swap_hi(rn * inv, h) * g;
    f32x16 o;
#pragma unroll
    for (int e = 0; e < 16; ++e) o[e] = np[e] * cp + nn[e] * cn;
    u32x4 o0 = pack8(o, 0), o1 = pack8(o, 1);
    o1.z = h ? 0u : o1.z; o1.w = h ? 0u : o1.w;
    stg_sc1(o0, lane * 16, ws, WS_O + (rt32 * 32 + (head0 + c) * 2) * 1024);
    stg_sc1(o1, lane * 16, ws, WS_O + (rt32 * 32 + (head0 + c) * 2 + 1) * 1024);
  }
  stg32_sc1(__float_as_uint(1.f - g), (lane & 31) * 4, ws, WS_OMG + ((head0 >> 2) * 2048 + rt32 * 32) * 4);
  VMWAIT(0);
  flag_set(ws, WS_FO + rt32 * 32 + (head0 >> 2) * 8, tag);
}

DI void epi_h(const f32x16 (&acc)[4], srd_t ws, u32 soff, int lane) {
#pragma unroll
  for (int c = 0; c < 4; ++c) {
    float hv[8];
#pragma unroll
    for (int e = 0; e < 8; ++e) {
      const float x = acc[c][e];
      const float t = __builtin_amdgcn_exp2f(x * __builtin_fmaf(x * x, -0.10294323970f, -2.30220819814f));
      hv[e] = x * __builtin_amdgcn_rcpf(1.f + t) * acc[c][e + 8];
    }
    stg_sc1(u32x4{cvtpk(hv[0], hv[1]), cvtpk(hv[2], hv[3]), cvtpk(hv[4], hv[5]), cvtpk(hv[6], hv[7])}, lane * 16, ws, soff + c * 1024);
  }
}

template <bool P1, bool isH> DI void body(const Params& p, const int wave) {
  const int tid = threadIdx.x, lane = tid & 63;
  const int w = wave & 3;
  const int b = blockIdx.x, xcd = b & 7, bi = b >> 3;
  const int m = 4 * (xcd >> 1) + (bi & 3), sl = bi >> 2, half = xcd & 1;
  const int j = 7 * half + sl;
  const int kvhead = isH ? 8 * half + sl + (sl >= 4 - half ? 1 : 0) : 4 + 7 * half;
  const int h = lane >> 5;
  const srd_t ws = mksrd(p.ws, WS_END);
  STAMP(0);

  if (P1) *(LAS u32*)(size_t)(L_FULL + (lane & 15) * 4) = 0u;
  u32x2 ev;
  asm volatile("buffer_load_dwordx2 %0, off, %1, 0 sc1" : "=v"(ev) : "s"(ws) : "memory");

  asm volatile("s_waitcnt lgkmcnt(0)\n\ts_barrier" ::: "memory");
  if (P1) {
    asm volatile("s_waitcnt vmcnt(0)" : "+v"(ev)::"memory");
    const u64 tag = (((u64)rfl(ev.y) << 32) | rfl(ev.x)) + 1;
    STAMP(1);
    u32x4 xf[28];
    f32x16 acc[4];
    unit_gemm<true, true>(xf, acc, lane, w, [](int) {} STAMP_PASS);
    STAMP(2);
    const int rt32 = m * 4 + w;
    if (isH) {
      epi_h(acc, ws, WS_H + (rt32 * 112 + j * 4) * 1024, lane);
      STAMP(3);
      unit_gemm<false, true>(xf, acc, lane, w, [&](int s) {
        if (s == 1) { VMWAIT(0); flag_set(ws, WS_FH + rt32 * 128 + j * 8, tag); }
      } STAMP_PASS);
      STAMP(4);
      epi_h(acc, ws, WS_H + (rt32 * 112 + (14 + j) * 4) * 1024, lane);
      VMWAIT(0);
      flag_set(ws, WS_FH + (64 + rt32) * 128 + j * 8, tag);
      STAMP(5);
    } else {
      u32x4 ef[4][2][2];
      epi_q(acc, ef, L_WPHI, lane);
      STAMP(3);
      {
        const int qw = (m * 2 + half) * 4 + w, hd = qw >> 3, pc = qw & 7;
        flag_wait(ws, WS_FP + (hd * 4 + (pc >> 1)) * 128 + (lane & 15) * 8, tag);
        STAMP(4);
        u32x4 pv[16];
#pragma unroll
        for (int i = 0; i < 16; ++i) ldg_sc1(pv[i], lane * 16, ws, WS_KVP + ((hd * 16 + i) * 8 + pc) * 1024);
        asm volatile("s_waitcnt vmcnt(0)"
                     : "+v"(pv[0]), "+v"(pv[1]), "+v"(pv[2]), "+v"(pv[3]), "+v"(pv[4]), "+v"(pv[5]), "+v"(pv[6]), "+v"(pv[7]), "+v"(pv[8]), "+v"(pv[9]),
                       "+v"(pv[10]), "+v"(pv[11]), "+v"(pv[12]), "+v"(pv[13]), "+v"(pv[14]), "+v"(pv[15])::"memory");
        f32x4 sm = __builtin_bit_cast(f32x4, pv[0]);
#pragma unroll
        for (int i = 1; i < 16; ++i) sm += __builtin_bit_cast(f32x4, pv[i]);
        u32x2 o = {cvtpk(sm.x, sm.y), cvtpk(sm.z, sm.w)};
        if ((lane & 31) == 29) { const u32 one2 = ((pc & 3) == 3 && h) ? 0u : 0x3f803f80u; o = u32x2{one2, one2}; }
        stg64_sc1(o, lane * 16 + (pc & 1) * 8, ws, WS_KVF + (hd * 4 + (pc >> 1)) * 1024);
        VMWAIT(0);
        flag_set(ws, WS_FF + hd * 64 + pc * 8, tag);
      }
      STAMP(5);
      lds_wait<5>(L_SYNC + (lane & 3) * 4);
      const float g = *(LAS float*)(size_t)(L_G + (w * 32 + (lane & 31)) * 4);
      stage_o(ef, 8 * half, rt32, g, ws, tag, lane);
      STAMP(6);
      STAMP_FLUSH();
    }
  } else {
    __builtin_amdgcn_s_setprio(PRIO2);
    const srd_t sx = mksrd(p.x, S * D * 2), sqkv = mksrd(p.wqkv, D * 3 * D * 2);
    {
      const srd_t sp = mksrd(p.wphi, 16 * 1568);
      if (isH) { if (w < 2) dma_buf(L_WPHI + w * 1024, lane * 16, sp, kvhead * 1568 + w * 1024); }
      else {
#pragma unroll
        for (int i = 0; i < 4; ++i) if (i * 4 + w < 13) dma_buf(L_WPHI + (i * 4 + w) * 1024, lane * 16, sp, half * 8 * 1568 + (i * 4 + w) * 1024);
        dma_buf(L_GW, lane * 16, mksrd(p.gw, D * 2), 0);
      }
    }
    u32 xvo[2];
#pragma unroll
    for (int i = 0; i < 2; ++i) xvo[i] = (lane >> 3) * 896 + (((lane & 7) ^ ((4 * i + (lane >> 4)) & 7)) * 16);
    const u32 xso = (m * 128 + w * 32) * 896;
    const char* wptr; u32 wrs, wu2;
    {
      const u32 row = lane >> 4, mat = (lane >> 3) & 1, c = ((lane >> 1) & 3) ^ row, pc = lane & 1;
      if (isH) { wrs = 3584; wptr = (mat ? p.wu : p.wg) + (w * 16 + row) * 3584 + j * 128 + c * 32 + pc * 16; wu2 = 14 * 128; }
      else     { wrs = 2688; wptr = p.wqkv + (w * 16 + row) * 2688 + (8 * half + c) * 56 + mat * 32 + pc * 16; wu2 = 4 * 56; }
    }
    const u32 kvo = (lane >> 2) * 2688 + (lane & 3) * 16;
    const u32 kso = (1 + (w >> 1)) * 896 + kvhead * 56;
    u32x4 xf[28];
    const u32 r = lane & 31;
    const u32 xb = w * 4096 + r * 128 + ((h ^ ((r >> 1) & 7)) * 16);
    const u32 l4 = (lane & 3) * 4, l16 = lane * 16;
    const srd_t spa = mksrd(w == 0 ? p.x : w == 1 ? p.wg : w == 2 ? p.wu : p.wd, w == 0 ? S * D * 2 : D * DF * 2);
    const srd_t spb = mksrd(w == 3 ? p.wo : p.wqkv, w == 3 ? D * D * 2 : D * 3 * D * 2);
    f32x16 ga;
#pragma unroll
    for (int e = 0; e < 16; ++e) ga[e] = 0.f;
    u32 pre = 0;
    auto landed = [&](auto ic) {
      constexpr int it = decltype(ic)::v, kind = ITEMS[it].kind, idx = ITEMS[it].idx;
      if constexpr (kind == 0) {
#pragma unroll
        for (int kk = 0; kk < 4; ++kk) xf[4 * idx + kk] = lds128(((2 * idx) % 7) * 16384 + (xb ^ (kk * 32)));
      } else if constexpr (kind == 2) lds_post(L_FULK + w * 4, idx + 1);
      else if constexpr (kind != 4) {
        lds_post(L_FULL + w * 4, ring_idx(it) + 1);
        if constexpr (!isH && kind == 1) {
#pragma unroll
          for (int kk = 0; kk < 4; ++kk) ga = mfma(lds128(L_GW + (4 * idx + kk) * 32 + h * 16), xf[4 * idx + kk], ga);
          asm volatile("" : "+v"(ga));
        }
      }
    };
    auto step = [&](auto ic) {
      constexpr int I = decltype(ic)::v;
      if constexpr (I < NLD1) {
        constexpr int it = item_of(I), i = I - item_first(it), kind = ITEMS[it].kind, idx = ITEMS[it].idx;
        if constexpr (i == 0) {
          if constexpr (kind == 2) { if constexpr (idx >= 2) lds_wait<idx - 1>(L_SYNC + l4); }
          else if constexpr (needs_prog(it)) lds_wait_pre<ring_idx(it) - 6>(L_PROG + l4, pre);
        }
        constexpr u32 sb = kind == 2 ? L_SCR + (idx & 1) * 16384 : (ring_idx(it) % 7) * 16384;
        if constexpr (kind == 0) dma_buf(sb + w * 4096 + i * 1024, xvo[i & 1], sx, xso + i * 8 * 896 + idx * 128);
        else if constexpr (kind == 1 || kind == 3) dma_glb(sb + w * 4096 + i * 1024, wptr + ((idx * 64 + i * 4) * wrs + (kind == 1 ? 0u : wu2)));
        else if constexpr (kind == 4) {
          if constexpr (i < 7) dma_buf(L_SCR + w * 4096, l16, spa, (i * 256 + b) * 1024);
          else dma_buf(L_SCR + w * 4096, l16, spb, ((w == 3 ? i - 7 : w * 2 + i - 7) * 256 + b) * 1024);
        }
        else if constexpr (idx < 3) dma_buf(sb + w * 4096 + i * 1024, kvo, sqkv, kso + (idx * 128 + (w & 1) * 64 + i * 16) * 2688);
        else dma_buf(sb + (w >> 1) * 8192 + (w & 1) * 2048 + i * 1024, kvo, sqkv, kso + (384 + (w & 1) * 32 + i * 16) * 2688);
        if constexpr (i == item_n(it) - 2 && needs_prog(it + 1)) pre = lds_peek(L_PROG + l4);
        constexpr int J = I - LAG;
        if constexpr (J >= 0) {
          if constexpr (item_first(item_of(J) + 1) == J + 1 && ITEMS[item_of(J)].kind != 4) {
            if constexpr (J == 3) asm volatile("s_waitcnt vmcnt(%1)" : "+v"(ev) : "n"(LAG) : "memory"); else VMWAIT(LAG);
            landed(IC<item_of(J)>{});
            FSTAMP(16 + item_of(J));
          }
        }
        SB();
      }
    };
    auto steps = [&](auto ac, auto bc) { sfor<decltype(bc)::v - decltype(ac)::v>([&](auto kc) { step(IC<decltype(ac)::v + decltype(kc)::v>{}); }); };
#define STEPS(a, b) steps(IC<(a)>{}, IC<(b)>{})
#define TICK(a) step(IC<(a)>{})
    f32x16 ka, va;
#pragma unroll
    for (int e = 0; e < 16; ++e) { ka[e] = 0.f; va[e] = 0.f; }
    auto kv_item = [&](auto tc, auto fc) {
      constexpr int t = decltype(tc)::v, F0 = decltype(fc)::v, NK = t < 3 ? 8 : 4;
      const u32 cb = (lane >> 4) & 1, q = (lane & 15) >> 2, pp = lane & 3;
      lds_wait<t + 1>(L_FULK + l4);
      u32 a = L_SCR + (t & 1) * 16384 + (8 * h + q) * 64 + cb * 32 + 8 * pp; OPAQUE(a);
      u32x4 fk[3], fv[3];
      auto reads = [&](int kk) { fk[kk % 3] = trfrag(a + kk * 1024, a + kk * 1024 + 256); fv[kk % 3] = trfrag(a + kk * 1024 + 8192, a + kk * 1024 + 8192 + 256); };
      reads(0); reads(1);
      SB();
      sfor<NK>([&](auto kc) {
        constexpr int kk = decltype(kc)::v;
        TICK(F0 + kk);
        if (kk + 2 < NK) reads(kk + 2);
        if (kk + 3 == NK) lds_post(L_SYNC + w * 4, t + 1);
        SB();
        ka = mfma(fk[kk % 3], xf[8 * t + kk], ka);
        va = mfma(xf[8 * t + kk], fv[kk % 3], va);
        SB();
        asm volatile("" : "+v"(ka), "+v"(va));
      });
    };
    STEPS(0, F(12));
    STAMP(1);
    kv_item(IC<0>{}, IC<F(12)>{});
    STEPS(F(14), F(15));
    kv_item(IC<1>{}, IC<F(15)>{});
    STEPS(F(17), F(18) + 2);
    kv_item(IC<2>{}, IC<F(18) + 2>{});
    STEPS(F(20) + 2, F(21));
    u32x4 wp[2]; load_wphi(wp, L_WPHI + (isH ? 0 : (kvhead - 8 * half) * 1568), lane);
    kv_item(IC<3>{}, IC<F(21)>{});
    STAMP(3);
    const u64 tag = (((u64)rfl(ev.y) << 32) | rfl(ev.x)) + 1;
    float g = 0.f;
    if (!isH) {
      g = __builtin_amdgcn_rcpf(1.f + __builtin_amdgcn_exp2f(-LOG2E * ga[0]));
      *(LAS float*)(size_t)(L_G + (w * 32 + r) * 4) = g;
    }
    {
      constexpr int E = F(22);
      const u32 scr = L_SCR + w * 8192;
      TICK(E);
      f32x16 pz;
#pragma unroll
      for (int e = 0; e < 16; ++e) pz[e] = 0.f;
      pz = mfma(wp[0], pack8(ka, 0), pz);
      pz = mfma(wp[1], pack8(ka, 1), pz);
      u32x4 vf[2];
#pragma unroll
      for (int s = 0; s < 2; ++s) {
        vf[s] = pack8(va, s);
        const u32 ov = r == 28 ? 0x3f803f80u : 0u;
        if (r >= 28) vf[s] = u32x4{ov, ov, ov, ov};
      }
      asm volatile("" : "+v"(pz), "+v"(vf[0]), "+v"(vf[1]));
      TICK(E + 1);
      float mx = max3(max3(max3(pz[0], pz[1], pz[2]), max3(pz[3], pz[4], pz[5]), max3(pz[6], pz[7], pz[8])),
                      max3(max3(pz[9], pz[10], pz[11]), max3(pz[12], pz[13], pz[14]), pz[15]), pz[15]);
      float mn = min3(min3(min3(pz[0], pz[1], pz[2]), min3(pz[3], pz[4], pz[5]), min3(pz[6], pz[7], pz[8])),
                      min3(min3(pz[9], pz[10], pz[11]), min3(pz[12], pz[13], pz[14]), pz[15]), pz[15]);
      asm volatile("" : "+v"(mx), "+v"(mn));
      TICK(E + 2);
      { float a, c; both_halves(mx, a, c); mx = fmaxf(a, c) * LOG2E; both_halves(mn, a, c); mn = fminf(a, c) * LOG2E; }
      asm volatile("" : "+v"(mx), "+v"(mn));
      TICK(E + 3);
      float ev2[2][16];
      sfor<6>([&](auto gc) {
        constexpr int g6 = decltype(gc)::v, lo = g6 * 32 / 6, hi = (g6 + 1) * 32 / 6;
#pragma unroll
        for (int e = lo; e < hi; ++e) {
          ev2[e >> 4][e & 15] = __builtin_amdgcn_exp2f(e < 16 ? __builtin_fmaf(pz[e & 15], LOG2E, -mx) : __builtin_fmaf(pz[e & 15], -LOG2E, mn));
          asm volatile("" : "+v"(ev2[e >> 4][e & 15]));
        }
        TICK(E + 4 + g6);
      });
      f32x16 ep, en;
#pragma unroll
      for (int e = 0; e < 16; ++e) { ep[e] = ev2[0][e]; en[e] = ev2[1][e]; }
      normalize(ep, h);
      asm volatile("" : "+v"(ep));
      TICK(E + 10);
      pre = lds_peek(L_SYNC + l4);
      normalize(en, h);
      asm volatile("" : "+v"(en));
      lds_wait_pre<4>(L_SYNC + l4, pre);
      pre = 0;
      TICK(E + 11);
      const u32 sw = (r >> 1) & 7;
#pragma unroll
      for (int g4 = 0; g4 < 4; ++g4) {
        const u32 a = scr + r * 64 + (((2 * g4 + h) ^ sw) * 8);
        sts64(a, u32x2{cvtpk(ep[4 * g4], ep[4 * g4 + 1]), cvtpk(ep[4 * g4 + 2], ep[4 * g4 + 3])});
        sts64(a + 2048, u32x2{cvtpk(en[4 * g4], en[4 * g4 + 1]), cvtpk(en[4 * g4 + 2], en[4 * g4 + 3])});
      }
      const u32 fb = (lane >> 4) & 1, q = (lane & 15) >> 2, pp = lane & 3;
      u32x4 pf[2][2];
#pragma unroll
      for (int s = 0; s < 2; ++s) {
        const u32 t0 = 16 * s + 4 * h + q, t1 = t0 + 8;
        const u32 a0 = scr + t0 * 64 + (((4 * fb + pp) ^ ((t0 >> 1) & 7)) * 8), a1 = scr + t1 * 64 + (((4 * fb + pp) ^ ((t1 >> 1) & 7)) * 8);
        pf[s][0] = trfrag(a0, a1); pf[s][1] = trfrag(a0 + 2048, a1 + 2048);
      }
      TICK(E + 12);
      TICK(E + 13);
      f32x16 kp, kn;
#pragma unroll
      for (int e = 0; e < 16; ++e) { kp[e] = 0.f; kn[e] = 0.f; }
#pragma unroll
      for (int s = 0; s < 2; ++s) { kp = mfma(pf[s][0], vf[s], kp); kn = mfma(pf[s][1], vf[s], kn); }
      asm volatile("" : "+v"(kp), "+v"(kn));
      TICK(E + 14);
      TICK(E + 15);
#pragma unroll
      for (int g4 = 0; g4 < 4; ++g4) {
        sts128(scr + g4 * 1024 + l16, __builtin_bit_cast(u32x4, f32x4{kp[4 * g4], kp[4 * g4 + 1], kp[4 * g4 + 2], kp[4 * g4 + 3]}));
        sts128(scr + 4096 + g4 * 1024 + l16, __builtin_bit_cast(u32x4, f32x4{kn[4 * g4], kn[4 * g4 + 1], kn[4 * g4 + 2], kn[4 * g4 + 3]}));
      }
      lds_post(L_SYNC + w * 4, 5);
      VMWAIT(8); landed(IC<23>{});
      lds_wait<5>(L_SYNC + l4);
#pragma unroll
      for (int i = 0; i < 2; ++i) {
        f32x4 sm = __builtin_bit_cast(f32x4, lds128(L_SCR + (w * 2 + i) * 1024 + l16));
#pragma unroll
        for (int u = 1; u < 4; ++u) sm += __builtin_bit_cast(f32x4, lds128(L_SCR + u * 8192 + (w * 2 + i) * 1024 + l16));
        stg_sc1(__builtin_bit_cast(u32x4, sm), l16, ws, WS_KVP + ((kvhead * 16 + m) * 8 + w * 2 + i) * 1024);
      }
      lds_post(L_SYNC + w * 4, 6);
      VMWAIT(2); landed(IC<25>{});
    }
    STAMP(4);
    if (!isH) {
      VMWAIT(0);
      flag_set(ws, WS_FP + (kvhead * 4 + w) * 128 + m * 8, tag);
      f32x16 acc[4];
      unit_gemm<false, false>(xf, acc, lane, w, [](int) {} STAMP_PASS);
      STAMP(5);
      u32x4 ef[4][2][2];
      epi_q(acc, ef, L_WPHI + 4 * 1568, lane);
      STAMP(6);
      stage_o(ef, 8 * half + 4, m * 4 + w, g, ws, tag, lane);
      STAMP(7);
      STAMP_FLUSH();
    } else {
      const int rt64 = 4 * xcd + (bi & 3), ct = sl;
      const srd_t swd = mksrd(p.wd, D * DF * 2), swo = mksrd(p.wo, D * D * 2);
      const u32 stg = L_SCR + w * 10240, rng = w * 28672;
      f32x16 acc[2][2];
#pragma unroll
      for (int i = 0; i < 4; ++i)
#pragma unroll
        for (int e = 0; e < 16; ++e) acc[i >> 1][i & 1][e] = 0.f;
      const u32 brow = lane >> 3;
      const u32 bpc = ((lane & 7) ^ (((brow >> 1) & 1) * 4)) * 16;
      const u32 bvo = brow * 896 + bpc;
      const u32 bvo_pad = (brow < 4 ? brow : 3u) * 896 + bpc;
      const u32 cb = (lane >> 4) & 1, q = (lane & 15) >> 2, pp = lane & 3;
      u32 rbs[2], rbr[2];
#pragma unroll
      for (int c = 0; c < 2; ++c) {
        const u32 o = (4 * h + q) * 128 + ((c ^ ((q >> 1) & 1)) * 64) + cb * 32 + pp * 8;
        rbs[c] = stg + o; rbr[c] = rng + o; OPAQUE(rbs[c]); OPAQUE(rbr[c]);
      }
      u32x4 af[14][2], bf[2][2];
      u32 om[2];
      u64 fv;
      const u32 hso = WS_H + (rt64 * 2 * 112 + w) * 1024, oso = WS_O + (rt64 * 2 * 32 + w * 8) * 1024;
      const u32 dso = w * 16 * 896 + ct * 128, wso = w * 112 * 896 + ct * 128;
      const u32 fl = lane & 31;
      const u32 fho = WS_FH + (rt64 * 2 + (fl >> 4)) * 128 + ((fl & 15) < 14 ? (fl & 15) : 0) * 8;
      const u32 foo = WS_FO + (rt64 * 2 + (lane & 1)) * 32 + w * 8;
      auto op = [&](auto kc) {
        constexpr int K = decltype(kc)::v;
        if constexpr (K < NOP2) {
          constexpr Op2 o = OPS2.v[K];
          if constexpr (o.pre == 1) { flag_check<K - 1 - find_op(5, 0, 0)>(fv, ws, fho, tag); STAMP(5); }
          if constexpr (o.pre == 2) { flag_check<K - 1 - find_op(5, 1, 0)>(fv, ws, fho + 64 * 128, tag); STAMP(7); }
          if constexpr (o.pre == 3) { flag_check<K - 1 - find_op(5, 2, 0)>(fv, ws, foo, tag); STAMP(9); }
          if constexpr (o.pre == 4) lds_wait<21>(L_PROG + l4);
          if constexpr (o.pre == 5) VMWAIT(K);
          if constexpr (o.kind == 0) {
            const u32 dst = (b_in_ring(o.job) ? rng : stg) + b_off(o.job) + o.i * 1024;
            if constexpr (o.job < 28) dma_buf(dst, bvo, swd, dso + (o.job * 64 + o.i * 8) * 896);
            else dma_buf(dst, ((o.job & 1) && o.i) ? bvo_pad : bvo, swo, wso + (((o.job - 28) >> 1) * 28 + (o.job & 1) * 16 + o.i * 8) * 896);
          } else if constexpr (o.kind == 1) {
            if constexpr (o.job < 28) ldg_sc1(af[o.job % 14][o.i], l16, ws, hso + o.job * 4096 + o.i * 112 * 1024);
            else ldg_sc1(af[o.job % 14][o.i], l16, ws, oso + (o.job - 28) * 1024 + o.i * 32 * 1024);
          } else if constexpr (o.kind == 5) flag_peek(fv, ws, o.job == 0 ? fho : o.job == 1 ? fho + 64 * 128 : foo);
          else if constexpr (o.kind == 6) flag_set(ws, WS_FP + (kvhead * 4 + w) * 128 + m * 8, tag);
          else asm volatile("buffer_load_dword %0, %1, %2, %3 offen sc1" : "=v"(om[o.i]) : "v"(r * 4), "s"(ws), "s"(WS_OMG + (w * 2048 + rt64 * 64 + o.i * 32) * 4) : "memory");
          SB();
        }
      };
      auto ops = [&](auto ac, auto bc) { sfor<decltype(bc)::v - decltype(ac)::v>([&](auto kc) { op(IC<decltype(ac)::v + decltype(kc)::v>{}); }); };
      auto bread = [&](int job, int c) {
        const u32 a = (b_in_ring(job) ? rbr[c] : rbs[c]) + b_off(job);
        bf[job & 1][c] = trfrag(a, a + 1024);
      };
      lds_wait<6>(L_SYNC + l4);
      ops(IC<0>{}, IC<job_pos(0)>{});
      VMWAIT(job_pos(0) - 1 - find_op(0, 0, 1));
      bread(0, 0); bread(0, 1);
      sfor<36>([&](auto nc) {
        constexpr int n = decltype(nc)::v, P = job_pos(n), Q = job_pos(n + 1), NL = Q - P;
        constexpr int need = n < 35 ? cmax(find_op(1, n, 1), find_op(0, n + 1, 1)) : find_op(1, n, 1);
        static_assert(need < P, "stream order");
        if constexpr (n == 28) {
          asm volatile("s_waitcnt vmcnt(%2)" : "+v"(om[0]), "+v"(om[1]) : "n"(P - 1 - find_op(7, 0, 1)) : "memory");
#pragma unroll
          for (int i = 0; i < 4; ++i)
#pragma unroll
            for (int e = 0; e < 16; ++e) acc[i >> 1][i & 1][e] *= __uint_as_float(om[i & 1]);
        }
        asm volatile("s_waitcnt vmcnt(%2)" : "+v"(af[n % 14][0]), "+v"(af[n % 14][1]) : "n"(P - 1 - need) : "memory");
        sfor<4>([&](auto uc) {
          constexpr int u = decltype(uc)::v, c = u >> 1, t = u & 1;
          acc[c][t] = mfma(bf[n & 1][c], af[n % 14][t], acc[c][t]);
          if constexpr (n < 35 && u < 2) bread(n + 1, u);
          SB();
          asm volatile("" : "+v"(acc[c][t]));
          ops(IC<P + NL * u / 4>{}, IC<P + NL * (u + 1) / 4>{});
        });
        if constexpr (n == 13) STAMP(6);
        if constexpr (n == 27) STAMP(8);
      });
      STAMP(10);
      if (b == 0 && w == 0) flag_set(ws, WS_E, tag);
      asm volatile("s_barrier" ::: "memory");
#pragma unroll
      for (int c = 0; c < 2; ++c)
#pragma unroll
        for (int t = 0; t < 2; ++t)
#pragma unroll
          for (int g4 = 0; g4 < 4; ++g4)
            sts128(w * 17408 + (t * 32 + r) * 272 + (c * 32 + g4 * 8 + h * 4) * 4,
                   __builtin_bit_cast(u32x4, f32x4{acc[c][t][4 * g4], acc[c][t][4 * g4 + 1], acc[c][t][4 * g4 + 2], acc[c][t][4 * g4 + 3]}));
    }
  }
  if (isH) {
    if (P1) asm volatile("s_barrier" ::: "memory");
    asm volatile("s_waitcnt lgkmcnt(0)\n\ts_barrier" ::: "memory");
    const int tok = tid >> 3, pc = tid & 7;
    f32x4 s0 = {0.f, 0.f, 0.f, 0.f}, s1 = s0;
#pragma unroll
    for (int u = 0; u < 4; ++u) {
      s0 += __builtin_bit_cast(f32x4, lds128(u * 17408 + tok * 272 + pc * 32));
      s1 += __builtin_bit_cast(f32x4, lds128(u * 17408 + tok * 272 + pc * 32 + 16));
    }
    const int rt64 = 4 * xcd + (bi & 3);
    *(u32x4*)(p.out + (rt64 * 64 + tok) * 896 + sl * 128 + pc * 16) = u32x4{cvtpk(s0.x, s0.y), cvtpk(s0.z, s0.w), cvtpk(s1.x, s1.y), cvtpk(s1.z, s1.w)};
    STAMP(12);
    STAMP_FLUSH();
  }
}
__global__ __launch_bounds__(512, 2) void fused(Params p) {
  const int wave = rfl(threadIdx.x >> 6);
  if ((blockIdx.x >> 5) < 7) {
    if (wave < 4) body<true, true>(p, wave); else body<false, true>(p, wave);
  } else {
    if (wave < 4) body<true, false>(p, wave); else body<false, false>(p, wave);
  }
}
}

extern "C" void kernel_launch(void* const* d_in, const int* in_sizes, int n_in, void* d_out, int out_size, void* d_ws, size_t ws_size,
                              hipStream_t stream) {
  hk::Params p;
  p.x = (const char*)d_in[0]; p.wqkv = (const char*)d_in[1]; p.wphi = (const char*)d_in[2]; p.wo = (const char*)d_in[3];
  p.wg = (const char*)d_in[5]; p.wu = (const char*)d_in[6]; p.wd = (const char*)d_in[7]; p.gw = (const char*)d_in[8];
  p.out = (char*)d_out; p.ws = (char*)d_ws;
  (void)hipFuncSetAttribute((const void*)hk::fused, hipFuncAttributeMaxDynamicSharedMemorySize, hk::L_TOTAL);
  hk::fused<<<256, 512, hk::L_TOTAL, stream>>>(p);
}
```

```cpp
#include <hip/hip_runtime.h>
#include <stdint.h>

#define NOWAIT 0
#define STAMPS 0

namespace hk {
typedef unsigned u32;
typedef unsigned long long u64;
typedef __bf16 bf16x8 __attribute__((ext_vector_type(8)));
typedef __bf16 bf16x2 __attribute__((ext_vector_type(2)));
typedef short s16x4 __attribute__((ext_vector_type(4)));
typedef short s16x8 __attribute__((ext_vector_type(8)));
typedef u32 u32x4 __attribute__((ext_vector_type(4)));
typedef u32 u32x2 __attribute__((ext_vector_type(2)));
typedef float f32x16 __attribute__((ext_vector_type(16)));
typedef float f32x4 __attribute__((ext_vector_type(4)));
typedef float f32x2 __attribute__((ext_vector_type(2)));
typedef __amdgpu_buffer_rsrc_t srd_t;
#define LAS __attribute__((address_space(3)))
#define DI __device__ __forceinline__

constexpr int S = 2048, D = 448, DF = 1792;
constexpr u32 WS_E = 0;
constexpr u32 WS_FH = 4096;
constexpr u32 WS_FP = WS_FH + 2 * 64 * 128;
constexpr u32 WS_FF = WS_FP + 16 * 4 * 128;
constexpr u32 WS_FO = WS_FF + 16 * 64 + 1024;
constexpr u32 WS_OMG = 65536;
constexpr u32 WS_KVF = 131072;
constexpr u32 WS_KVP = 262144;
constexpr u32 WS_O = WS_KVP + 16 * 16 * 8192;
constexpr u32 WS_H = WS_O + 64 * 32 * 1024;
constexpr u32 WS_END = WS_H + 64 * 112 * 1024;
constexpr u32 WS_STAMP = 16u << 20;
constexpr u32 L_SCR = 7 * 16384;
constexpr u32 L_WPHI = L_SCR + 32768;
constexpr u32 L_GW = L_WPHI + 13312;
constexpr u32 L_G = L_GW + 1024;
constexpr u32 L_FULL = L_G + 512;
constexpr u32 L_PROG = L_FULL + 16;
constexpr u32 L_SYNC = L_PROG + 16;
constexpr u32 L_TOTAL = 163840;

struct Params {
  const char *x, *wqkv, *wphi, *wo, *wg, *wu, *wd, *gw;
  char *out, *ws;
};

DI u32 rfl(u32 v) { return __builtin_amdgcn_readfirstlane(v); }
DI srd_t mksrd(const void* p, u32 bytes) { return __builtin_amdgcn_make_buffer_rsrc((void*)p, 0, bytes, 0x00020000); }
DI u32 cvtpk(float lo, float hi) { f32x2 v = {lo, hi}; return __builtin_bit_cast(u32, __builtin_convertvector(v, bf16x2)); }
DI f32x16 mfma(u32x4 a, u32x4 b, f32x16 c) {
  return __builtin_amdgcn_mfma_f32_32x32x16_bf16(__builtin_bit_cast(bf16x8, a), __builtin_bit_cast(bf16x8, b), c, 0, 0, 0);
}
DI u32x2 tr64(u32 addr) { return __builtin_bit_cast(u32x2, __builtin_amdgcn_ds_read_tr16_b64_v4i16((LAS s16x4*)(size_t)addr)); }
DI u32x4 trfrag(u32 a0, u32 a1) { u32x2 lo = tr64(a0), hi = tr64(a1); return u32x4{lo.x, lo.y, hi.x, hi.y}; }
DI u32x4 lds128(u32 addr) { return *(LAS u32x4*)(size_t)addr; }
DI void sts128(u32 addr, u32x4 v) { *(LAS u32x4*)(size_t)addr = v; }
DI void sts64(u32 addr, u32x2 v) { *(LAS u32x2*)(size_t)addr = v; }
#define MEMFENCE() asm volatile("" ::: "memory")
#define VMWAIT(n) asm volatile("s_waitcnt vmcnt(%0)" ::"n"(n) : "memory")
DI void dma_buf(u32 ldsaddr, u32 voff, srd_t srd, u32 soff) {
  asm volatile("s_mov_b32 m0, %0\n\ts_nop 0\n\tbuffer_load_dwordx4 %1, %2, %3 offen lds" ::"s"(ldsaddr), "v"(voff), "s"(srd), "s"(soff) : "memory");
}
DI void dma_glb(u32 ldsaddr, const char* p) {
  asm volatile("s_mov_b32 m0, %0\n\ts_nop 0\n\tglobal_load_lds_dwordx4 %1, off" ::"s"(ldsaddr), "v"(p) : "memory");
}
DI void ldg_sc1(u32x4& dst, u32 voff, srd_t srd, u32 soff) {
  asm volatile("buffer_load_dwordx4 %0, %1, %2, %3 offen sc1" : "=v"(dst) : "v"(voff), "s"(srd), "s"(soff) : "memory");
}
DI void stg_sc1(u32x4 v, u32 voff, srd_t srd, u32 soff) {
  asm volatile("buffer_store_dwordx4 %0, %1, %2, %3 offen sc1\n\ts_nop 1" ::"v"(v), "v"(voff), "s"(srd), "s"(soff) : "memory");
}
DI void stg64_sc1(u32x2 v, u32 voff, srd_t srd, u32 soff) {
  asm volatile("buffer_store_dwordx2 %0, %1, %2, %3 offen sc1" ::"v"(v), "v"(voff), "s"(srd), "s"(soff) : "memory");
}
DI void stg32_sc1(u32 v, u32 voff, srd_t srd, u32 soff) {
  asm volatile("buffer_store_dword %0, %1, %2, %3 offen sc1" ::"v"(v), "v"(voff), "s"(srd), "s"(soff) : "memory");
}
DI void lds_post(u32 addr, u32 val) {
  asm volatile("s_mov_b64 exec, 1\n\tds_write_b32 %0, %1\n\ts_mov_b64 exec, -1" ::"v"(addr), "v"(val) : "memory");
}
template <int T> DI void lds_wait(u32 a) {
  u32 v, cnt;
  asm volatile("s_mov_b32 %1, 0x400000\n"
               ".Lsp%=:\n\tds_read_b32 %0, %2\n\ts_waitcnt lgkmcnt(0)\n\tv_cmp_gt_u32 vcc, %3, %0\n\ts_cbranch_vccz .Lse%=\n\t"
               "s_sub_u32 %1, %1, 1\n\ts_cbranch_scc0 .Lsp%=\n"
               ".Lse%=:"
               : "=&v"(v), "=&s"(cnt) : "v"(a), "n"(T) : "vcc", "scc", "memory");
}
DI void flag_wait(srd_t ws, u32 voff, u64 tag) {
  u64 v; u32 cnt;
  asm volatile("s_mov_b32 %1, 0x8000\n"
               ".Lfp%=:\n\tbuffer_load_dwordx2 %0, %2, %3, 0 offen sc1\n\ts_waitcnt vmcnt(0)\n\tv_cmp_ne_u64 vcc, %4, %0\n\ts_cbranch_vccz .Lfe%=\n\t"
               "s_sleep 2\n\ts_sub_u32 %1, %1, 1\n\ts_cbranch_scc0 .Lfp%=\n"
               ".Lfe%=:"
               : "=&v"(v), "=&s"(cnt) : "v"(voff), "s"(ws), "s"(tag) : "vcc", "scc", "memory");
}
DI void flag_set(srd_t ws, u32 off, u64 tag) {
  asm volatile("s_mov_b64 exec, 1\n\tbuffer_store_dwordx2 %0, off, %1, %2 sc1\n\ts_mov_b64 exec, -1" ::"v"(tag), "s"(ws), "s"(off) : "memory");
}
#define SB() __builtin_amdgcn_sched_barrier(0)
#define OPAQUE(x) asm volatile("" : "+v"(x))
DI void interleave4() {
#pragma unroll
  for (int i = 0; i < 4; ++i) { __builtin_amdgcn_sched_group_barrier(0x8, 1, 0); __builtin_amdgcn_sched_group_barrier(0x100, 2, 0); }
}
DI float swap_hi(float lo_wants, int h) {
  auto r = __builtin_amdgcn_permlane32_swap(__float_as_uint(lo_wants), __float_as_uint(lo_wants), false, false);
  (void)h;
  return __uint_as_float(r[1]);
}
DI void both_halves(float v, float& lo, float& hi) {
  auto r = __builtin_amdgcn_permlane32_swap(__float_as_uint(v), __float_as_uint(v), false, false);
  lo = __uint_as_float(r[0]); hi = __uint_as_float(r[1]);
}
#define STAMP(i) do {} while (0)
#define STAMP_FLUSH() do {} while (0)
#define STAMP_ARG
#define STAMP_PASS

constexpr float LOG2E = 1.4426950408889634f;

template <int I> struct IC { static constexpr int v = I; };
template <int N, class F, int I = 0> DI void sfor(F f) { if constexpr (I < N) { f(IC<I>{}); sfor<N, F, I + 1>(f); } }

template <bool FIRST, bool POST, class Hook>
DI void unit_gemm(u32x4 (&xf)[28], f32x16 (&acc)[4], int lane, int w, Hook hook STAMP_ARG) {
  const u32 h = lane >> 5, mat = (lane >> 4) & 1, q = (lane & 15) >> 2, pp = lane & 3;
  const u32 vb0 = (8 * h + q) * 256 + mat * 128 + q * 32 + 8 * pp;
  const u32 r = lane & 31;
  const u32 xb = w * 4096 + r * 128 + ((h ^ ((r >> 1) & 7)) * 16);
  const u32 fa = L_FULL + (lane & 3) * 4, pa = L_PROG + w * 4;
  u32 vb[4];
  u32x4 fr[2][4];
  auto stage_open = [&](auto sc) {
    constexpr int s = decltype(sc)::v, item = FIRST ? 2 * s + 1 : 18 + s;
    STAMP((FIRST ? 16 : 32) + 2 * s);
    lds_wait<item + 1>(fa);
    STAMP((FIRST ? 16 : 32) + 2 * s + 1);
#pragma unroll
    for (int c = 0; c < 4; ++c) { vb[c] = (vb0 ^ (c * 32)) + (item % 7) * 16384; OPAQUE(vb[c]); }
    if (FIRST) {
#pragma unroll
      for (int kk = 0; kk < 4; ++kk) xf[4 * s + kk] = lds128(((2 * s) % 7) * 16384 + (xb ^ (kk * 32)));
    }
    SB();
  };
  auto reads = [&](int buf, int kk) {
#pragma unroll
    for (int c = 0; c < 4; ++c) fr[buf][c] = trfrag(vb[c] + kk * 4096, vb[c] + kk * 4096 + 1024);
  };
#pragma unroll
  for (int c = 0; c < 4; ++c)
#pragma unroll
    for (int e = 0; e < 16; ++e) acc[c][e] = 0.f;
  stage_open(IC<0>{});
  reads(0, 0);
  SB();
  sfor<7>([&](auto sc) {
    constexpr int s = decltype(sc)::v;
#pragma unroll
    for (int kk = 0; kk < 4; ++kk) {
      if (kk == 3) { if constexpr (s < 6) stage_open(IC<s + 1>{}); }
      if (kk < 3) reads((kk + 1) & 1, kk + 1); else if (s < 6) reads(0, 0);
#pragma unroll
      for (int c = 0; c < 4; ++c) acc[c] = mfma(fr[kk & 1][c], xf[4 * s + kk], acc[c]);
      interleave4();
      SB();
      asm volatile("" : "+v"(acc[0]), "+v"(acc[1]), "+v"(acc[2]), "+v"(acc[3]));
      if (kk == 2) { if (POST) lds_post(pa, s + 1); hook(s); SB(); }
    }
  });
}

DI void load_wphi(u32x4 (&wp)[2], u32 base, int lane) {
  const u32 h = lane >> 5, fb = (lane >> 4) & 1, q = (lane & 15) >> 2, pp = lane & 3;
  u32 col = 16 * fb + 4 * pp; if (col == 28) col = 24;
  const u32 a = base + (4 * h + q) * 56 + col * 2;
  wp[0] = trfrag(a, a + 8 * 56);
  u32x2 lo = tr64(a + 16 * 56), hi = tr64(base + (24 + q) * 56 + col * 2);
  wp[1] = u32x4{lo.x, lo.y, h ? 0u : hi.x, h ? 0u : hi.y};
}
DI u32x4 pack8(const f32x16& v, int s) {
  return u32x4{cvtpk(v[8 * s], v[8 * s + 1]), cvtpk(v[8 * s + 2], v[8 * s + 3]), cvtpk(v[8 * s + 4], v[8 * s + 5]), cvtpk(v[8 * s + 6], v[8 * s + 7])};
}
DI void feature_exp(const f32x16& t, const u32x4 (&wp)[2], f32x16& ep, f32x16& en) {
  f32x16 pz;
#pragma unroll
  for (int e = 0; e < 16; ++e) pz[e] = 0.f;
  pz = mfma(wp[0], pack8(t, 0), pz);
  pz = mfma(wp[1], pack8(t, 1), pz);
  float mx = pz[0], mn = pz[0];
#pragma unroll
  for (int e = 1; e < 16; ++e) { mx = fmaxf(mx, pz[e]); mn = fminf(mn, pz[e]); }
  float a, b;
  both_halves(mx, a, b); mx = fmaxf(a, b) * LOG2E;
  both_halves(mn, a, b); mn = fminf(a, b) * LOG2E;
#pragma unroll
  for (int e = 0; e < 16; ++e) {
    ep[e] = __builtin_amdgcn_exp2f(__builtin_fmaf(pz[e], LOG2E, -mx));
    en[e] = __builtin_amdgcn_exp2f(__builtin_fmaf(pz[e], -LOG2E, mn));
  }
}
DI void normalize(f32x16& v, int h) {
#pragma unroll
  for (int e = 12; e < 16; ++e) v[e] = h ? 0.f : v[e];
  float s = 0.f;
#pragma unroll
  for (int e = 0; e < 16; ++e) s += v[e];
  float a, b; both_halves(s, a, b);
  const float r = __builtin_amdgcn_rcpf(a + b);
#pragma unroll
  for (int e = 0; e < 16; ++e) v[e] *= r;
}

DI void epi_q(const f32x16 (&acc)[4], u32x4 (&ef)[4][2][2], u32 wphi_base, int lane) {
#pragma unroll
  for (int c = 0; c < 4; ++c) {
    u32x4 wp[2]; load_wphi(wp, wphi_base + c * 1568, lane);
    f32x16 ep, en; feature_exp(acc[c], wp, ep, en);
    ef[c][0][0] = pack8(ep, 0); ef[c][0][1] = pack8(ep, 1);
    ef[c][1][0] = pack8(en, 0); ef[c][1][1] = pack8(en, 1);
  }
}
DI void stage_o(const u32x4 (&ef)[4][2][2], int head0, int rt32, float g, srd_t ws, u64 tag, int lane) {
  const int h = lane >> 5;
  flag_wait(ws, WS_FF + head0 * 64 + (lane & 31) * 8, tag);
  u32x4 kf[4][2][2];
#pragma unroll
  for (int c = 0; c < 4; ++c)
#pragma unroll
    for (int t = 0; t < 2; ++t)
#pragma unroll
      for (int s = 0; s < 2; ++s) ldg_sc1(kf[c][t][s], lane * 16, ws, WS_KVF + ((head0 + c) * 4 + t * 2 + s) * 1024);
  asm volatile("s_waitcnt vmcnt(0)"
               : "+v"(kf[0][0][0]), "+v"(kf[0][0][1]), "+v"(kf[0][1][0]), "+v"(kf[0][1][1]), "+v"(kf[1][0][0]), "+v"(kf[1][0][1]), "+v"(kf[1][1][0]), "+v"(kf[1][1][1]),
                 "+v"(kf[2][0][0]), "+v"(kf[2][0][1]), "+v"(kf[2][1][0]), "+v"(kf[2][1][1]), "+v"(kf[3][0][0]), "+v"(kf[3][0][1]), "+v"(kf[3][1][0]), "+v"(kf[3][1][1])
               :: "memory");
#pragma unroll
  for (int c = 0; c < 4; ++c) {
    f32x16 np, nn;
#pragma unroll
    for (int e = 0; e < 16; ++e) { np[e] = 0.f; nn[e] = 0.f; }
    np = mfma(kf[c][0][0], ef[c][0][0], np); nn = mfma(kf[c][1][0], ef[c][1][0], nn);
    np = mfma(kf[c][0][1], ef[c][0][1], np); nn = mfma(kf[c][1][1], ef[c][1][1], nn);
    const float rp = __builtin_amdgcn_rcpf(np[13]), rn = __builtin_amdgcn_rcpf(nn[13]);
    const float inv = __builtin_amdgcn_rcpf(np[12] * rp + nn[12] * rn + 1e-6f);
    const float cp = swap_hi(rp * inv, h) * g, cn = swap_hi(rn * inv, h) * g;
    f32x16 o;
#pragma unroll
    for (int e = 0; e < 16; ++e) o[e] = np[e] * cp + nn[e] * cn;
    u32x4 o0 = pack8(o, 0), o1 = pack8(o, 1);
    o1.z = h ? 0u : o1.z; o1.w = h ? 0u : o1.w;
    stg_sc1(o0, lane * 16, ws, WS_O + (rt32 * 32 + (head0 + c) * 2) * 1024);
    stg_sc1(o1, lane * 16, ws, WS_O + (rt32 * 32 + (head0 + c) * 2 + 1) * 1024);
  }
  stg32_sc1(__float_as_uint(1.f - g), (lane & 31) * 4, ws, WS_OMG + ((head0 >> 2) * 2048 + rt32 * 32) * 4);
  VMWAIT(0);
  flag_set(ws, WS_FO + rt32 * 32 + (head0 >> 2) * 8, tag);
}

DI void epi_h(const f32x16 (&acc)[4], srd_t ws, u32 soff, int lane) {
#pragma unroll
  for (int c = 0; c < 4; ++c) {
    float hv[8];
#pragma unroll
    for (int e = 0; e < 8; ++e) {
      const float x = acc[c][e];
      const float t = __builtin_amdgcn_exp2f(x * __builtin_fmaf(x * x, -0.10294323970f, -2.30220819814f));
      hv[e] = x * __builtin_amdgcn_rcpf(1.f + t) * acc[c][e + 8];
    }
    stg_sc1(u32x4{cvtpk(hv[0], hv[1]), cvtpk(hv[2], hv[3]), cvtpk(hv[4], hv[5]), cvtpk(hv[6], hv[7])}, lane * 16, ws, soff + c * 1024);
  }
}

template <bool P1, bool isH> DI void body(const Params& p, const int wave) {
  const int tid = threadIdx.x, lane = tid & 63;
  const int w = wave & 3;
  const int b = blockIdx.x, xcd = b & 7, bi = b >> 3;
  const int m = 4 * (xcd >> 1) + (bi & 3), sl = bi >> 2, half = xcd & 1;
  const int j = 7 * half + sl;
  const int kvhead = isH ? 8 * half + sl + (sl >= 4 - half ? 1 : 0) : 4 + 7 * half;
  const int h = lane >> 5;
  const srd_t ws = mksrd(p.ws, WS_END);
  STAMP(0);

  if (P1) *(LAS u32*)(size_t)(L_FULL + (lane & 15) * 4) = 0u;
  u32x2 ev;
  asm volatile("buffer_load_dwordx2 %0, off, %1, 0 sc1" : "=v"(ev) : "s"(ws) : "memory");

  if (P1) {
    {
      const char* pp = w == 0 ? p.x : w == 1 ? p.wg : w == 2 ? p.wu : p.wd;
      const srd_t sa = mksrd(pp, w == 0 ? S * D * 2 : D * DF * 2);
#pragma unroll
      for (int i = 0; i < 7; ++i) dma_buf(L_SCR + w * 8192 + i * 1024, lane * 16, sa, (i * 256 + b) * 1024);
      const srd_t sb = mksrd(w == 3 ? p.wo : p.wqkv, w == 3 ? D * D * 2 : D * 3 * D * 2);
#pragma unroll
      for (int i = 0; i < 2; ++i) dma_buf(L_SCR + w * 8192 + 7 * 1024, lane * 16, sb, ((w == 3 ? i : w * 2 + i) * 256 + b) * 1024);
    }
    asm volatile("s_waitcnt lgkmcnt(0)\n\ts_barrier" ::: "memory");
    asm volatile("s_waitcnt vmcnt(0)" : "+v"(ev)::"memory");
    const u64 tag = (((u64)rfl(ev.y) << 32) | rfl(ev.x)) + 1;
    STAMP(1);
    u32x4 xf[28];
    f32x16 acc[4];
    unit_gemm<true, true>(xf, acc, lane, w, [](int) {} STAMP_PASS);
    STAMP(2);
    const int rt32 = m * 4 + w;
    if (isH) {
      epi_h(acc, ws, WS_H + (rt32 * 112 + j * 4) * 1024, lane);
      STAMP(3);
      unit_gemm<false, false>(xf, acc, lane, w, [&](int s) {
        if (s == 1) { VMWAIT(0); flag_set(ws, WS_FH + rt32 * 128 + j * 8, tag); }
      } STAMP_PASS);
      STAMP(4);
      epi_h(acc, ws, WS_H + (rt32 * 112 + (14 + j) * 4) * 1024, lane);
      VMWAIT(0);
      flag_set(ws, WS_FH + (64 + rt32) * 128 + j * 8, tag);
      STAMP(5);
    } else {
      u32x4 ef[4][2][2];
      epi_q(acc, ef, L_WPHI, lane);
      STAMP(3);
      {
        const int qw = (m * 2 + half) * 4 + w, hd = qw >> 3, pc = qw & 7;
        flag_wait(ws, WS_FP + (hd * 4 + (pc >> 1)) * 128 + (lane & 15) * 8, tag);
        STAMP(4);
        u32x4 pv[16];
#pragma unroll
        for (int i = 0; i < 16; ++i) ldg_sc1(pv[i], lane * 16, ws, WS_KVP + ((hd * 16 + i) * 8 + pc) * 1024);
        asm volatile("s_waitcnt vmcnt(0)"
                     : "+v"(pv[0]), "+v"(pv[1]), "+v"(pv[2]), "+v"(pv[3]), "+v"(pv[4]), "+v"(pv[5]), "+v"(pv[6]), "+v"(pv[7]), "+v"(pv[8]), "+v"(pv[9]),
                       "+v"(pv[10]), "+v"(pv[11]), "+v"(pv[12]), "+v"(pv[13]), "+v"(pv[14]), "+v"(pv[15])::"memory");
        f32x4 sm = __builtin_bit_cast(f32x4, pv[0]);
#pragma unroll
        for (int i = 1; i < 16; ++i) sm += __builtin_bit_cast(f32x4, pv[i]);
        u32x2 o = {cvtpk(sm.x, sm.y), cvtpk(sm.z, sm.w)};
        if ((lane & 31) == 29) { const u32 one2 = ((pc & 3) == 3 && h) ? 0u : 0x3f803f80u; o = u32x2{one2, one2}; }
        stg64_sc1(o, lane * 16 + (pc & 1) * 8, ws, WS_KVF + (hd * 4 + (pc >> 1)) * 1024);
        VMWAIT(0);
        flag_set(ws, WS_FF + hd * 64 + pc * 8, tag);
      }
      STAMP(5);
      lds_wait<1>(L_SYNC + (lane & 3) * 4);
      const float g = *(LAS float*)(size_t)(L_G + (w * 32 + (lane & 31)) * 4);
      stage_o(ef, 8 * half, rt32, g, ws, tag, lane);
      STAMP(6);
      STAMP_FLUSH();
    }
  } else {
    const srd_t sx = mksrd(p.x, S * D * 2), sqkv = mksrd(p.wqkv, D * 3 * D * 2);
    {
      const srd_t sp = mksrd(p.wphi, 16 * 1568);
      if (isH) { if (w < 2) dma_buf(L_WPHI + w * 1024, lane * 16, sp, kvhead * 1568 + w * 1024); }
      else {
#pragma unroll
        for (int i = 0; i < 4; ++i) if (i * 4 + w < 13) dma_buf(L_WPHI + (i * 4 + w) * 1024, lane * 16, sp, half * 8 * 1568 + (i * 4 + w) * 1024);
        if (w == 3) dma_buf(L_GW, lane * 16, mksrd(p.gw, D * 2), 0);
      }
    }
    u32 xvo[2];
#pragma unroll
    for (int i = 0; i < 2; ++i) xvo[i] = (lane >> 3) * 896 + (((lane & 7) ^ ((4 * i + (lane >> 4)) & 7)) * 16);
    const u32 xso = (m * 128 + w * 32) * 896;
    const char* wptr; u32 wrs, wu2;
    {
      const u32 row = lane >> 4, mat = (lane >> 3) & 1, c = ((lane >> 1) & 3) ^ row, pc = lane & 1;
      if (isH) { wrs = 3584; wptr = (mat ? p.wu : p.wg) + (w * 16 + row) * 3584 + j * 128 + c * 32 + pc * 16; wu2 = 14 * 128; }
      else     { wrs = 2688; wptr = p.wqkv + (w * 16 + row) * 2688 + (8 * half + c) * 56 + mat * 32 + pc * 16; wu2 = 4 * 56; }
    }
    const u32 kvo = (lane >> 2) * 2688 + (lane & 3) * 16;
    const u32 kso = (1 + (w >> 1)) * 896 + kvhead * 56;
    auto issue = [&](int item) {
      const u32 sb = (item % 7) * 16384;
      if (item < 14 && !(item & 1)) {
#pragma unroll
        for (int i = 0; i < 4; ++i) dma_buf(sb + w * 4096 + i * 1024, xvo[i & 1], sx, xso + i * 8 * 896 + (item >> 1) * 128);
      } else if (item < 14 || item >= 18) {
        const int s = item < 14 ? item >> 1 : item - 18;
#pragma unroll
        for (int i = 0; i < 4; ++i) dma_glb(sb + w * 4096 + i * 1024, wptr + ((s * 64 + i * 4) * wrs + (item < 14 ? 0u : wu2)));
      } else if (item < 17) {
#pragma unroll
        for (int i = 0; i < 4; ++i) dma_buf(sb + w * 4096 + i * 1024, kvo, sqkv, kso + ((item - 14) * 128 + (w & 1) * 64 + i * 16) * 2688);
      } else {
#pragma unroll
        for (int i = 0; i < 2; ++i) dma_buf(sb + (w >> 1) * 8192 + (w & 1) * 2048 + i * 1024, kvo, sqkv, kso + (384 + (w & 1) * 32 + i * 16) * 2688);
      }
    };
#pragma unroll
    for (int it = 0; it < 7; ++it) issue(it);
    asm volatile("s_waitcnt lgkmcnt(0)\n\ts_barrier" ::: "memory");
    STAMP(1);
    u32x4 xf[28];
    const u32 r = lane & 31;
    const u32 xb = w * 4096 + r * 128 + ((h ^ ((r >> 1) & 7)) * 16);
    sfor<7>([&](auto sc) {
      constexpr int s = decltype(sc)::v;
      if (s == 0) asm volatile("s_waitcnt vmcnt(20)" : "+v"(ev)::"memory"); else VMWAIT(12);
      STAMP(16 + 3 * s);
      const u32 xs = ((2 * s) % 7) * 16384;
#pragma unroll
      for (int kk = 0; kk < 4; ++kk) xf[4 * s + kk] = lds128(xs + (xb ^ (kk * 32)));
      lds_post(L_FULL + w * 4, 2 * s + 2);
      if (s >= 1) { lds_wait<s>(L_PROG + (lane & 3) * 4); STAMP(16 + 3 * s + 1); issue(2 * s + 5); issue(2 * s + 6); }
      STAMP(16 + 3 * s + 2);
      SB();
    });
    const u64 tag = (((u64)rfl(ev.y) << 32) | rfl(ev.x)) + 1;
    STAMP(2);
    lds_wait<7>(L_PROG + (lane & 3) * 4); issue(19); issue(20);
    f32x16 ka, va;
#pragma unroll
    for (int e = 0; e < 16; ++e) { ka[e] = 0.f; va[e] = 0.f; }
    {
      const u32 cb = (lane >> 4) & 1, q = (lane & 15) >> 2, pp = lane & 3;
      const u32 vb = (8 * h + q) * 64 + cb * 32 + 8 * pp;
      sfor<4>([&](auto tc) {
        constexpr int t = decltype(tc)::v;
        if (t == 0) VMWAIT(22);
        else if (t == 1) VMWAIT(18);
        else if (t == 2) VMWAIT(18);
        else VMWAIT(20);
        STAMP(40 + 3 * t);
        lds_post(L_FULL + w * 4, 15 + t);
        lds_wait<15 + t>(L_FULL + (lane & 3) * 4);
        STAMP(40 + 3 * t + 1);
        if (t >= 1) issue(20 + t);
        STAMP(40 + 3 * t + 2);
        u32 a = ((14 + t) % 7) * 16384 + vb; OPAQUE(a);
        constexpr int NG = t < 3 ? 4 : 2;
        u32x4 fr[2][4];
        auto reads = [&](int buf, int g2) {
#pragma unroll
          for (int u = 0; u < 2; ++u) {
            fr[buf][2 * u] = trfrag(a + (2 * g2 + u) * 1024, a + (2 * g2 + u) * 1024 + 256);
            fr[buf][2 * u + 1] = trfrag(a + (2 * g2 + u) * 1024 + 8192, a + (2 * g2 + u) * 1024 + 8192 + 256);
          }
        };
        reads(0, 0);
        SB();
#pragma unroll
        for (int g2 = 0; g2 < NG; ++g2) {
          if (g2 + 1 < NG) reads((g2 + 1) & 1, g2 + 1);
#pragma unroll
          for (int u = 0; u < 2; ++u) {
            ka = mfma(fr[g2 & 1][2 * u], xf[8 * t + 2 * g2 + u], ka);
            va = mfma(xf[8 * t + 2 * g2 + u], fr[g2 & 1][2 * u + 1], va);
          }
          interleave4();
          SB();
          asm volatile("" : "+v"(ka), "+v"(va));
        }
      });
    }
    VMWAIT(20);
    lds_post(L_FULL + w * 4, 19);
    lds_wait<19>(L_FULL + (lane & 3) * 4);
    issue(24);
    STAMP(3);
    float g = 0.f;
    if (!isH) {
      f32x16 ga;
#pragma unroll
      for (int e = 0; e < 16; ++e) ga[e] = 0.f;
#pragma unroll
      for (int s = 0; s < 28; ++s) ga = mfma(lds128(L_GW + s * 32 + h * 16), xf[s], ga);
      g = __builtin_amdgcn_rcpf(1.f + __builtin_amdgcn_exp2f(-LOG2E * ga[0]));
      *(LAS float*)(size_t)(L_G + (w * 32 + r) * 4) = g;
    }
    {
      const u32 scr = L_SCR + w * 8192;
      u32x4 wp[2]; load_wphi(wp, L_WPHI + (isH ? 0 : (kvhead - 8 * half) * 1568), lane);
      f32x16 ep, en; feature_exp(ka, wp, ep, en);
      STAMP(52); VMWAIT(16); lds_post(L_FULL + w * 4, 21); STAMP(53);
      normalize(ep, h); normalize(en, h);
      const u32 sw = (r >> 1) & 7;
#pragma unroll
      for (int g4 = 0; g4 < 4; ++g4) {
        const u32 a = scr + r * 64 + (((2 * g4 + h) ^ sw) * 8);
        sts64(a, u32x2{cvtpk(ep[4 * g4], ep[4 * g4 + 1]), cvtpk(ep[4 * g4 + 2], ep[4 * g4 + 3])});
        sts64(a + 2048, u32x2{cvtpk(en[4 * g4], en[4 * g4 + 1]), cvtpk(en[4 * g4 + 2], en[4 * g4 + 3])});
      }
      u32x4 vf[2];
#pragma unroll
      for (int s = 0; s < 2; ++s) {
        vf[s] = pack8(va, s);
        const u32 ov = r == 28 ? 0x3f803f80u : 0u;
        if (r >= 28) vf[s] = u32x4{ov, ov, ov, ov};
      }
      f32x16 kp, kn;
#pragma unroll
      for (int e = 0; e < 16; ++e) { kp[e] = 0.f; kn[e] = 0.f; }
      const u32 fb = (lane >> 4) & 1, q = (lane & 15) >> 2, pp = lane & 3;
#pragma unroll
      for (int s = 0; s < 2; ++s) {
        const u32 t0 = 16 * s + 4 * h + q, t1 = t0 + 8;
        const u32 a0 = scr + t0 * 64 + (((4 * fb + pp) ^ ((t0 >> 1) & 7)) * 8), a1 = scr + t1 * 64 + (((4 * fb + pp) ^ ((t1 >> 1) & 7)) * 8);
        kp = mfma(trfrag(a0, a1), vf[s], kp);
        kn = mfma(trfrag(a0 + 2048, a1 + 2048), vf[s], kn);
      }
#pragma unroll
      for (int g4 = 0; g4 < 4; ++g4) {
        sts128(scr + g4 * 1024 + lane * 16, __builtin_bit_cast(u32x4, f32x4{kp[4 * g4], kp[4 * g4 + 1], kp[4 * g4 + 2], kp[4 * g4 + 3]}));
        sts128(scr + 4096 + g4 * 1024 + lane * 16, __builtin_bit_cast(u32x4, f32x4{kn[4 * g4], kn[4 * g4 + 1], kn[4 * g4 + 2], kn[4 * g4 + 3]}));
      }
      lds_post(L_SYNC + w * 4, 1);
      STAMP(54); VMWAIT(8); lds_post(L_FULL + w * 4, 23); STAMP(55);
      lds_wait<1>(L_SYNC + (lane & 3) * 4);
#pragma unroll
      for (int i = 0; i < 2; ++i) {
        f32x4 sm = __builtin_bit_cast(f32x4, lds128(L_SCR + (w * 2 + i) * 1024 + lane * 16));
#pragma unroll
        for (int u = 1; u < 4; ++u) sm += __builtin_bit_cast(f32x4, lds128(L_SCR + u * 8192 + (w * 2 + i) * 1024 + lane * 16));
        stg_sc1(__builtin_bit_cast(u32x4, sm), lane * 16, ws, WS_KVP + ((kvhead * 16 + m) * 8 + w * 2 + i) * 1024);
      }
      lds_post(L_SYNC + w * 4, 2);
      STAMP(56);
      VMWAIT(0);
      lds_post(L_FULL + w * 4, 25);
      flag_set(ws, WS_FP + (kvhead * 4 + w) * 128 + m * 8, tag);
    }
    STAMP(4);
    if (!isH) {
      f32x16 acc[4];
      unit_gemm<false, false>(xf, acc, lane, w, [](int) {} STAMP_PASS);
      STAMP(5);
      u32x4 ef[4][2][2];
      epi_q(acc, ef, L_WPHI + 4 * 1568, lane);
      STAMP(6);
      stage_o(ef, 8 * half + 4, m * 4 + w, g, ws, tag, lane);
      STAMP(7);
      STAMP_FLUSH();
    } else {
      const int rt64 = 4 * xcd + (bi & 3), ct = sl;
      const srd_t swd = mksrd(p.wd, D * DF * 2), swo = mksrd(p.wo, D * D * 2);
      const u32 stg = L_SCR + w * 8192;
      f32x16 acc[2][2];
#pragma unroll
      for (int i = 0; i < 4; ++i)
#pragma unroll
        for (int e = 0; e < 16; ++e) acc[i >> 1][i & 1][e] = 0.f;
      const u32 brow = lane >> 3;
      const u32 bpc = ((lane & 7) ^ (((brow >> 1) & 1) * 4)) * 16;
      const u32 bvo = brow * 896 + bpc;
      const u32 bvo_pad = (brow < 4 ? brow : 3u) * 896 + bpc;
      const u32 cb = (lane >> 4) & 1, q = (lane & 15) >> 2, pp = lane & 3;
      u32 rb[2];
#pragma unroll
      for (int c = 0; c < 2; ++c) rb[c] = stg + (4 * h + q) * 128 + ((c ^ ((q >> 1) & 1)) * 64) + cb * 32 + pp * 8;
      u32x4 af[4][2];
      lds_wait<2>(L_SYNC + (lane & 3) * 4);
      auto job_issue = [&](int seg, int n) {
        const int sl4 = n & 3;
        if (seg < 2) {
          const int ks = (seg * 14 + n) * 4 + w;
          dma_buf(stg + sl4 * 2048, bvo, swd, ks * 16 * 896 + ct * 128);
          dma_buf(stg + sl4 * 2048 + 1024, bvo, swd, (ks * 16 + 8) * 896 + ct * 128);
          ldg_sc1(af[sl4][0], lane * 16, ws, WS_H + ((rt64 * 2) * 112 + ks) * 1024);
          ldg_sc1(af[sl4][1], lane * 16, ws, WS_H + ((rt64 * 2 + 1) * 112 + ks) * 1024);
        } else {
          const int ks = w * 8 + n, row0 = (ks >> 1) * 28 + (n & 1) * 16;
          dma_buf(stg + sl4 * 2048, bvo, swo, row0 * 896 + ct * 128);
          dma_buf(stg + sl4 * 2048 + 1024, (n & 1) ? bvo_pad : bvo, swo, (row0 + 8) * 896 + ct * 128);
          ldg_sc1(af[sl4][0], lane * 16, ws, WS_O + ((rt64 * 2) * 32 + ks) * 1024);
          ldg_sc1(af[sl4][1], lane * 16, ws, WS_O + ((rt64 * 2 + 1) * 32 + ks) * 1024);
        }
      };
#pragma unroll
      for (int seg = 0; seg < 3; ++seg) {
        const int NJ = seg < 2 ? 14 : 8;
        if (seg < 2) {
          const u32 fl = lane & 31;
          flag_wait(ws, WS_FH + (seg * 64 + rt64 * 2 + (fl >> 4)) * 128 + ((fl & 15) < 14 ? (fl & 15) : 0) * 8, tag);
        } else {
          flag_wait(ws, WS_FO + (rt64 * 2 + (lane & 1)) * 32 + w * 8, tag);
          u32 om[2];
          asm volatile("buffer_load_dword %0, %2, %3, %4 offen sc1\n\tbuffer_load_dword %1, %2, %3, %4 offen offset:128 sc1"
                       : "=&v"(om[0]), "=&v"(om[1]) : "v"(r * 4), "s"(ws), "s"(WS_OMG + (w * 2048 + rt64 * 64) * 4) : "memory");
#pragma unroll
          for (int n = 0; n < 3; ++n) job_issue(seg, n);
          asm volatile("s_waitcnt vmcnt(12)" : "+v"(om[0]), "+v"(om[1])::"memory");
#pragma unroll
          for (int i = 0; i < 4; ++i)
#pragma unroll
            for (int e = 0; e < 16; ++e) acc[i >> 1][i & 1][e] *= __uint_as_float(om[i & 1]);
        }
        STAMP(5 + 2 * seg);
        if (seg < 2) {
#pragma unroll
          for (int n = 0; n < 3; ++n) job_issue(seg, n);
        }
#pragma unroll
        for (int n = 0; n < NJ; ++n) {
          const int sl4 = n & 3;
          if (n + 3 < NJ) job_issue(seg, n + 3);
          const int left = (NJ - 1 - n < 3 ? NJ - 1 - n : 3) * 4;
          asm volatile("s_waitcnt vmcnt(%2)" : "+v"(af[sl4][0]), "+v"(af[sl4][1]) : "n"(left) : "memory");
          u32x4 bf[2];
#pragma unroll
          for (int c = 0; c < 2; ++c) bf[c] = trfrag(rb[c] + sl4 * 2048, rb[c] + sl4 * 2048 + 1024);
#pragma unroll
          for (int c = 0; c < 2; ++c)
#pragma unroll
            for (int t = 0; t < 2; ++t) acc[c][t] = mfma(bf[c], af[sl4][t], acc[c][t]);
          SB();
          asm volatile("" : "+v"(acc[0][0]), "+v"(acc[0][1]), "+v"(acc[1][0]), "+v"(acc[1][1]));
        }
        STAMP(6 + 2 * seg);
      }
      if (b == 0 && w == 0) flag_set(ws, WS_E, tag);
      asm volatile("s_barrier" ::: "memory");
#pragma unroll
      for (int c = 0; c < 2; ++c)
#pragma unroll
        for (int t = 0; t < 2; ++t)
#pragma unroll
          for (int g4 = 0; g4 < 4; ++g4)
            sts128(w * 17408 + (t * 32 + r) * 272 + (c * 32 + g4 * 8 + h * 4) * 4,
                   __builtin_bit_cast(u32x4, f32x4{acc[c][t][4 * g4], acc[c][t][4 * g4 + 1], acc[c][t][4 * g4 + 2], acc[c][t][4 * g4 + 3]}));
    }
  }
  if (isH) {
    if (P1) asm volatile("s_barrier" ::: "memory");
    asm volatile("s_waitcnt lgkmcnt(0)\n\ts_barrier" ::: "memory");
    const int tok = tid >> 3, pc = tid & 7;
    f32x4 s0 = {0.f, 0.f, 0.f, 0.f}, s1 = s0;
#pragma unroll
    for (int u = 0; u < 4; ++u) {
      s0 += __builtin_bit_cast(f32x4, lds128(u * 17408 + tok * 272 + pc * 32));
      s1 += __builtin_bit_cast(f32x4, lds128(u * 17408 + tok * 272 + pc * 32 + 16));
    }
    const int rt64 = 4 * xcd + (bi & 3);
    *(u32x4*)(p.out + (rt64 * 64 + tok) * 896 + sl * 128 + pc * 16) = u32x4{cvtpk(s0.x, s0.y), cvtpk(s0.z, s0.w), cvtpk(s1.x, s1.y), cvtpk(s1.z, s1.w)};
    STAMP(12);
    STAMP_FLUSH();
  }
}
__global__ __launch_bounds__(512, 2) void fused(Params p) {
  const int wave = rfl(threadIdx.x >> 6);
  if ((blockIdx.x >> 5) < 7) {
    if (wave < 4) body<true, true>(p, wave); else body<false, true>(p, wave);
  } else {
    if (wave < 4) body<true, false>(p, wave); else body<false, false>(p, wave);
  }
}
}

extern "C" void kernel_launch(void* const* d_in, const int* in_sizes, int n_in, void* d_out, int out_size, void* d_ws, size_t ws_size,
                              hipStream_t stream) {
  hk::Params p;
  p.x = (const char*)d_in[0]; p.wqkv = (const char*)d_in[1]; p.wphi = (const char*)d_in[2]; p.wo = (const char*)d_in[3];
  p.wg = (const char*)d_in[5]; p.wu = (const char*)d_in[6]; p.wd = (const char*)d_in[7]; p.gw = (const char*)d_in[8];
  p.out = (char*)d_out; p.ws = (char*)d_ws;
  (void)hipFuncSetAttribute((const void*)hk::fused, hipFuncAttributeMaxDynamicSharedMemorySize, hk::L_TOTAL);
  hk::fused<<<256, 512, hk::L_TOTAL, stream>>>(p);
}
```

```cpp
#include <hip/hip_runtime.h>
#include <stdint.h>

#define NOWAIT 0
#define PRIO2 3
#define STAMPS 0

namespace hk {
typedef unsigned u32;
typedef unsigned long long u64;
typedef __bf16 bf16x8 __attribute__((ext_vector_type(8)));
typedef __bf16 bf16x2 __attribute__((ext_vector_type(2)));
typedef short s16x4 __attribute__((ext_vector_type(4)));
typedef short s16x8 __attribute__((ext_vector_type(8)));
typedef u32 u32x4 __attribute__((ext_vector_type(4)));
typedef u32 u32x2 __attribute__((ext_vector_type(2)));
typedef float f32x16 __attribute__((ext_vector_type(16)));
typedef float f32x4 __attribute__((ext_vector_type(4)));
typedef float f32x2 __attribute__((ext_vector_type(2)));
typedef __amdgpu_buffer_rsrc_t srd_t;
#define LAS __attribute__((address_space(3)))
#define DI __device__ __forceinline__

constexpr int S = 2048, D = 448, DF = 1792;
constexpr u32 WS_E = 0;
constexpr u32 WS_FH = 4096;
constexpr u32 WS_FP = WS_FH + 2 * 64 * 128;
constexpr u32 WS_FF = WS_FP + 16 * 4 * 128;
constexpr u32 WS_FO = WS_FF + 16 * 64 + 1024;
constexpr u32 WS_OMG = 65536;
constexpr u32 WS_KVF = 131072;
constexpr u32 WS_KVP = 262144;
constexpr u32 WS_O = WS_KVP + 16 * 16 * 8192;
constexpr u32 WS_H = WS_O + 64 * 32 * 1024;
constexpr u32 WS_END = WS_H + 64 * 112 * 1024;
constexpr u32 WS_STAMP = 16u << 20;
constexpr u32 L_SCR = 7 * 16384;
constexpr u32 L_WPHI = L_SCR + 32768;
constexpr u32 L_GW = L_WPHI + 13312;
constexpr u32 L_G = L_GW + 1024;
constexpr u32 L_FULL = L_G + 512;
constexpr u32 L_PROG = L_FULL + 16;
constexpr u32 L_SYNC = L_PROG + 16;
constexpr u32 L_FULK = L_SYNC + 16;
constexpr u32 L_TOTAL = 163840;

struct Params {
  const char *x, *wqkv, *wphi, *wo, *wg, *wu, *wd, *gw;
  char *out, *ws;
};

DI u32 rfl(u32 v) { return __builtin_amdgcn_readfirstlane(v); }
DI srd_t mksrd(const void* p, u32 bytes) { return __builtin_amdgcn_make_buffer_rsrc((void*)p, 0, bytes, 0x00020000); }
DI u32 cvtpk(float lo, float hi) { f32x2 v = {lo, hi}; return __builtin_bit_cast(u32, __builtin_convertvector(v, bf16x2)); }
DI f32x16 mfma(u32x4 a, u32x4 b, f32x16 c) {
  return __builtin_amdgcn_mfma_f32_32x32x16_bf16(__builtin_bit_cast(bf16x8, a), __builtin_bit_cast(bf16x8, b), c, 0, 0, 0);
}
DI u32x2 tr64(u32 addr) { return __builtin_bit_cast(u32x2, __builtin_amdgcn_ds_read_tr16_b64_v4i16((LAS s16x4*)(size_t)addr)); }
DI u32x4 trfrag(u32 a0, u32 a1) { u32x2 lo = tr64(a0), hi = tr64(a1); return u32x4{lo.x, lo.y, hi.x, hi.y}; }
DI u32x4 lds128(u32 addr) { return *(LAS u32x4*)(size_t)addr; }
DI void sts128(u32 addr, u32x4 v) { *(LAS u32x4*)(size_t)addr = v; }
DI void sts64(u32 addr, u32x2 v) { *(LAS u32x2*)(size_t)addr = v; }
#define MEMFENCE() asm volatile("" ::: "memory")
#define VMWAIT(n) asm volatile("s_waitcnt vmcnt(%0)" ::"n"(n) : "memory")
DI void dma_buf(u32 ldsaddr, u32 voff, srd_t srd, u32 soff) {
  asm volatile("s_mov_b32 m0, %0\n\ts_nop 0\n\tbuffer_load_dwordx4 %1, %2, %3 offen lds" ::"s"(ldsaddr), "v"(voff), "s"(srd), "s"(soff) : "memory");
}
DI void dma_glb(u32 ldsaddr, const char* p) {
  asm volatile("s_mov_b32 m0, %0\n\ts_nop 0\n\tglobal_load_lds_dwordx4 %1, off" ::"s"(ldsaddr), "v"(p) : "memory");
}
DI void ldg_sc1(u32x4& dst, u32 voff, srd_t srd, u32 soff) {
  asm volatile("buffer_load_dwordx4 %0, %1, %2, %3 offen sc1" : "=v"(dst) : "v"(voff), "s"(srd), "s"(soff) : "memory");
}
DI void stg_sc1(u32x4 v, u32 voff, srd_t srd, u32 soff) {
  asm volatile("buffer_store_dwordx4 %0, %1, %2, %3 offen sc1\n\ts_nop 1" ::"v"(v), "v"(voff), "s"(srd), "s"(soff) : "memory");
}
DI void stg64_sc1(u32x2 v, u32 voff, srd_t srd, u32 soff) {
  asm volatile("buffer_store_dwordx2 %0, %1, %2, %3 offen sc1" ::"v"(v), "v"(voff), "s"(srd), "s"(soff) : "memory");
}
DI void stg32_sc1(u32 v, u32 voff, srd_t srd, u32 soff) {
  asm volatile("buffer_store_dword %0, %1, %2, %3 offen sc1" ::"v"(v), "v"(voff), "s"(srd), "s"(soff) : "memory");
}
DI void lds_post(u32 addr, u32 val) {
  asm volatile("s_mov_b64 exec, 1\n\tds_write_b32 %0, %1\n\ts_mov_b64 exec, -1" ::"v"(addr), "v"(val) : "memory");
}
template <int T> DI void lds_wait(u32 a) {
  u32 v, cnt;
  asm volatile("s_mov_b32 %1, 0x400000\n"
               ".Lsp%=:\n\tds_read_b32 %0, %2\n\ts_waitcnt lgkmcnt(0)\n\tv_cmp_gt_u32 vcc, %3, %0\n\ts_cbranch_vccz .Lse%=\n\t"
               "s_sub_u32 %1, %1, 1\n\ts_cbranch_scc0 .Lsp%=\n"
               ".Lse%=:"
               : "=&v"(v), "=&s"(cnt) : "v"(a), "n"(T) : "vcc", "scc", "memory");
}
template <int T> DI void lds_wait_pre(u32 a, u32 v) {
  u32 cnt;
  asm volatile("v_cmp_gt_u32 vcc, %3, %0\n\ts_cbranch_vccz .Lse%=\n\ts_mov_b32 %1, 0x400000\n"
               ".Lsp%=:\n\tds_read_b32 %0, %2\n\ts_waitcnt lgkmcnt(0)\n\tv_cmp_gt_u32 vcc, %3, %0\n\ts_cbranch_vccz .Lse%=\n\t"
               "s_sub_u32 %1, %1, 1\n\ts_cbranch_scc0 .Lsp%=\n"
               ".Lse%=:"
               : "+v"(v), "=&s"(cnt) : "v"(a), "n"(T) : "vcc", "scc", "memory");
}
DI u32 lds_peek(u32 a) { return *(volatile LAS u32*)(size_t)a; }
DI void flag_peek(u64& v, srd_t ws, u32 voff) {
  asm volatile("buffer_load_dwordx2 %0, %1, %2, 0 offen sc1" : "=v"(v) : "v"(voff), "s"(ws) : "memory");
}
template <int N> DI void flag_check(u64 v, srd_t ws, u32 voff, u64 tag) {
  u32 cnt;
  asm volatile("s_waitcnt vmcnt(%5)\n\t"
               "v_cmp_ne_u64 vcc, %4, %0\n\ts_cbranch_vccz .Lfe%=\n\ts_mov_b32 %1, 0x8000\n"
               ".Lfp%=:\n\tbuffer_load_dwordx2 %0, %2, %3, 0 offen sc1\n\ts_waitcnt vmcnt(0)\n\tv_cmp_ne_u64 vcc, %4, %0\n\ts_cbranch_vccz .Lfe%=\n\t"
               "s_sleep 2\n\ts_sub_u32 %1, %1, 1\n\ts_cbranch_scc0 .Lfp%=\n"
               ".Lfe%=:"
               : "+v"(v), "=&s"(cnt) : "v"(voff), "s"(ws), "s"(tag), "n"(N) : "vcc", "scc", "memory");
}
DI void flag_wait(srd_t ws, u32 voff, u64 tag) {
  u64 v; u32 cnt;
  asm volatile("s_mov_b32 %1, 0x8000\n"
               ".Lfp%=:\n\tbuffer_load_dwordx2 %0, %2, %3, 0 offen sc1\n\ts_waitcnt vmcnt(0)\n\tv_cmp_ne_u64 vcc, %4, %0\n\ts_cbranch_vccz .Lfe%=\n\t"
               "s_sleep 2\n\ts_sub_u32 %1, %1, 1\n\ts_cbranch_scc0 .Lfp%=\n"
               ".Lfe%=:"
               : "=&v"(v), "=&s"(cnt) : "v"(voff), "s"(ws), "s"(tag) : "vcc", "scc", "memory");
}
DI void flag_set(srd_t ws, u32 off, u64 tag) {
  asm volatile("s_mov_b64 exec, 1\n\tbuffer_store_dwordx2 %0, off, %1, %2 sc1\n\ts_mov_b64 exec, -1" ::"v"(tag), "s"(ws), "s"(off) : "memory");
}
#define SB() __builtin_amdgcn_sched_barrier(0)
#define OPAQUE(x) asm volatile("" : "+v"(x))
DI void interleave4() {
#pragma unroll
  for (int i = 0; i < 4; ++i) { __builtin_amdgcn_sched_group_barrier(0x8, 1, 0); __builtin_amdgcn_sched_group_barrier(0x100, 2, 0); }
}
DI float swap_hi(float lo_wants, int h) {
  auto r = __builtin_amdgcn_permlane32_swap(__float_as_uint(lo_wants), __float_as_uint(lo_wants), false, false);
  (void)h;
  return __uint_as_float(r[1]);
}
DI void both_halves(float v, float& lo, float& hi) {
  auto r = __builtin_amdgcn_permlane32_swap(__float_as_uint(v), __float_as_uint(v), false, false);
  lo = __uint_as_float(r[0]); hi = __uint_as_float(r[1]);
}
#define STAMP(i) do {} while (0)
#define STAMP_FLUSH() do {} while (0)
#define STAMP_ARG
#define STAMP_PASS

#define FSTAMP(i) do {} while (0)
constexpr float LOG2E = 1.4426950408889634f;

template <int I> struct IC { static constexpr int v = I; };
template <int N, class F, int I = 0> DI void sfor(F f) { if constexpr (I < N) { f(IC<I>{}); sfor<N, F, I + 1>(f); } }

template <bool FIRST, bool POST, class Hook>
DI void unit_gemm(u32x4 (&xf)[28], f32x16 (&acc)[4], int lane, int w, Hook hook STAMP_ARG) {
  const u32 h = lane >> 5, mat = (lane >> 4) & 1, q = (lane & 15) >> 2, pp = lane & 3;
  const u32 vb0 = (8 * h + q) * 256 + mat * 128 + q * 32 + 8 * pp;
  const u32 r = lane & 31;
  const u32 xb = w * 4096 + r * 128 + ((h ^ ((r >> 1) & 7)) * 16);
  const u32 fa = L_FULL + (lane & 3) * 4, pa = L_PROG + w * 4;
  u32 vb[4], pre = 0;
  u32x4 fr[2][4];
  auto stage_open = [&](auto sc) {
    constexpr int s = decltype(sc)::v, item = FIRST ? 2 * s + 1 : 14 + s;
    FSTAMP((FIRST ? 16 : 32) + 2 * s);
    lds_wait_pre<item + 1>(fa, pre);
    FSTAMP((FIRST ? 16 : 32) + 2 * s + 1);
#pragma unroll
    for (int c = 0; c < 4; ++c) { vb[c] = (vb0 ^ (c * 32)) + (item % 7) * 16384; OPAQUE(vb[c]); }
    if (FIRST) {
#pragma unroll
      for (int kk = 0; kk < 4; ++kk) xf[4 * s + kk] = lds128(((2 * s) % 7) * 16384 + (xb ^ (kk * 32)));
      lds_post(pa, 2 * s + 1);
    }
    SB();
  };
  auto reads = [&](int buf, int kk) {
#pragma unroll
    for (int c = 0; c < 4; ++c) fr[buf][c] = trfrag(vb[c] + kk * 4096, vb[c] + kk * 4096 + 1024);
  };
#pragma unroll
  for (int c = 0; c < 4; ++c)
#pragma unroll
    for (int e = 0; e < 16; ++e) acc[c][e] = 0.f;
  stage_open(IC<0>{});
  reads(0, 0);
  SB();
  sfor<7>([&](auto sc) {
    constexpr int s = decltype(sc)::v;
#pragma unroll
    for (int kk = 0; kk < 4; ++kk) {
      if (kk == 2 && s < 6) { pre = lds_peek(fa); SB(); }
      if (kk == 3) { if constexpr (s < 6) stage_open(IC<s + 1>{}); }
      if (kk < 3) reads((kk + 1) & 1, kk + 1); else if (s < 6) reads(0, 0);
#pragma unroll
      for (int c = 0; c < 4; ++c) acc[c] = mfma(fr[kk & 1][c], xf[4 * s + kk], acc[c]);
      interleave4();
      SB();
      asm volatile("" : "+v"(acc[0]), "+v"(acc[1]), "+v"(acc[2]), "+v"(acc[3]));
      if (kk == 2) { if (POST) lds_post(pa, FIRST ? 2 * s + 2 : 15 + s); hook(s); SB(); }
    }
  });
}

struct Item { int kind, idx; };
constexpr int NITEM = 26;
constexpr Item ITEMS[NITEM] = {
  {0,0},{1,0},{4,0},{0,1},{1,1},{0,2},{1,2},{0,3},{2,0},{1,3},{0,4},{2,1},{1,4},{0,5},{2,2},{1,5},{0,6},{2,3},{1,6},
  {3,0},{3,1},{3,2},{3,3},{3,4},{3,5},{3,6}};
constexpr int item_n(int it) { return ITEMS[it].kind == 4 ? 9 : ITEMS[it].kind == 2 && ITEMS[it].idx == 3 ? 2 : 4; }
constexpr int item_first(int it) { int n = 0; for (int i = 0; i < it; ++i) n += item_n(i); return n; }
constexpr int item_of(int I) { int it = 0; while (it < NITEM && item_first(it + 1) <= I) ++it; return it; }
constexpr int ring_idx(int it) { return ITEMS[it].kind == 0 ? 2 * ITEMS[it].idx : ITEMS[it].kind == 1 ? 2 * ITEMS[it].idx + 1 : 14 + ITEMS[it].idx; }
constexpr bool needs_prog(int it) { return it < NITEM && ITEMS[it].kind != 2 && ITEMS[it].kind != 4 && ring_idx(it) >= 7; }
constexpr int NLD1 = item_first(NITEM), LAG = 12;
#define F(it) item_first(it)

struct Op2 { int kind, job, i, pre; };
constexpr bool b_in_ring(int job) { return (job >= 14 && job < 28) || job >= 33; }
constexpr int b_off(int job) { return (job < 14 ? job % 5 : job < 28 ? job - 14 : job < 33 ? (job - 28 + 4) % 5 : job - 33) * 2048; }
struct Tab2 { Op2 v[160]; int pos[40]; int n; };
constexpr Tab2 make_ops2() {
  Tab2 t{}; int n = 0;
  auto B = [&](int job, int pre = 0) { t.v[n++] = Op2{0, job, 0, pre}; t.v[n++] = Op2{0, job, 1, 0}; };
  auto A = [&](int job, int pre = 0) { t.v[n++] = Op2{1, job, 0, pre}; t.v[n++] = Op2{1, job, 1, 0}; };
  t.v[n++] = Op2{5, 0, 0, 0};
  for (int j = 0; j < 5; ++j) B(j);
  t.v[n++] = Op2{6, 0, 0, 5};
  for (int j = 0; j < 4; ++j) A(j, j == 0 ? 1 : 0);
  for (int j = 0; j < 36; ++j) {
    t.pos[j] = n;
    if (j < 10) { A(j + 4); if (j < 9) B(j + 5); else B(14, 4); }
    else if (j < 13) { B(15 + 2 * (j - 10)); B(16 + 2 * (j - 10)); }
    else if (j == 13) { for (int k = 21; k < 28; ++k) B(k); t.v[n++] = Op2{5, 1, 0, 0}; for (int k = 28; k < 33; ++k) B(k); for (int k = 14; k < 20; ++k) A(k, k == 14 ? 2 : 0); }
    else if (j < 22) A(j + 6);
    else if (j < 25) { if (j == 22) t.v[n++] = Op2{5, 2, 0, 0}; B(j + 11); }
    else if (j == 25) { t.v[n++] = Op2{7, 0, 0, 3}; t.v[n++] = Op2{7, 0, 1, 0}; A(28); A(29); }
    else if (j < 28) { A(30 + 3 * (j - 26)); A(31 + 3 * (j - 26)); A(32 + 3 * (j - 26)); }
  }
  t.pos[36] = n; t.n = n;
  return t;
}
constexpr Tab2 OPS2 = make_ops2();
constexpr int NOP2 = OPS2.n;
constexpr int job_pos(int j) { return OPS2.pos[j]; }
constexpr int find_op(int kind, int job, int i) { for (int k = 0; k < NOP2; ++k) if (OPS2.v[k].kind == kind && OPS2.v[k].job == job && OPS2.v[k].i == i) return k; return 1000; }
constexpr int cmax(int a, int b) { return a > b ? a : b; }

DI void load_wphi(u32x4 (&wp)[2], u32 base, int lane) {
  const u32 h = lane >> 5, fb = (lane >> 4) & 1, q = (lane & 15) >> 2, pp = lane & 3;
  u32 col = 16 * fb + 4 * pp; if (col == 28) col = 24;
  const u32 a = base + (4 * h + q) * 56 + col * 2;
  wp[0] = trfrag(a, a + 8 * 56);
  u32x2 lo = tr64(a + 16 * 56), hi = tr64(base + (24 + q) * 56 + col * 2);
  wp[1] = u32x4{lo.x, lo.y, h ? 0u : hi.x, h ? 0u : hi.y};
}
DI u32x4 pack8(const f32x16& v, int s) {
  return u32x4{cvtpk(v[8 * s], v[8 * s + 1]), cvtpk(v[8 * s + 2], v[8 * s + 3]), cvtpk(v[8 * s + 4], v[8 * s + 5]), cvtpk(v[8 * s + 6], v[8 * s + 7])};
}
DI float max3(float a, float b, float c) { return fmaxf(fmaxf(a, b), c); }
DI float min3(float a, float b, float c) { return fminf(fminf(a, b), c); }
DI void feature_exp(const f32x16& t, const u32x4 (&wp)[2], f32x16& ep, f32x16& en) {
  f32x16 pz;
#pragma unroll
  for (int e = 0; e < 16; ++e) pz[e] = 0.f;
  pz = mfma(wp[0], pack8(t, 0), pz);
  pz = mfma(wp[1], pack8(t, 1), pz);
  float mx = max3(max3(max3(pz[0], pz[1], pz[2]), max3(pz[3], pz[4], pz[5]), max3(pz[6], pz[7], pz[8])),
                  max3(max3(pz[9], pz[10], pz[11]), max3(pz[12], pz[13], pz[14]), pz[15]), pz[15]);
  float mn = min3(min3(min3(pz[0], pz[1], pz[2]), min3(pz[3], pz[4], pz[5]), min3(pz[6], pz[7], pz[8])),
                  min3(min3(pz[9], pz[10], pz[11]), min3(pz[12], pz[13], pz[14]), pz[15]), pz[15]);
  float a, b;
  both_halves(mx, a, b); mx = fmaxf(a, b) * LOG2E;
  both_halves(mn, a, b); mn = fminf(a, b) * LOG2E;
#pragma unroll
  for (int e = 0; e < 16; ++e) {
    ep[e] = __builtin_amdgcn_exp2f(__builtin_fmaf(pz[e], LOG2E, -mx));
    en[e] = __builtin_amdgcn_exp2f(__builtin_fmaf(pz[e], -LOG2E, mn));
  }
}
DI void normalize(f32x16& v, int h) {
#pragma unroll
  for (int e = 12; e < 16; ++e) v[e] = h ? 0.f : v[e];
  const float s = (((v[0] + v[1]) + (v[2] + v[3])) + ((v[4] + v[5]) + (v[6] + v[7]))) + (((v[8] + v[9]) + (v[10] + v[11])) + ((v[12] + v[13]) + (v[14] + v[15])));
  float a, b; both_halves(s, a, b);
  const float r = __builtin_amdgcn_rcpf(a + b);
#pragma unroll
  for (int e = 0; e < 16; ++e) v[e] *= r;
}

DI void epi_q(const f32x16 (&acc)[4], u32x4 (&ef)[4][2][2], u32 wphi_base, int lane) {
#pragma unroll
  for (int c = 0; c < 4; ++c) {
    u32x4 wp[2]; load_wphi(wp, wphi_base + c * 1568, lane);
    f32x16 ep, en; feature_exp(acc[c], wp, ep, en);
    ef[c][0][0] = pack8(ep, 0); ef[c][0][1] = pack8(ep, 1);
    ef[c][1][0] = pack8(en, 0); ef[c][1][1] = pack8(en, 1);
    asm volatile("" : "+v"(ef[c][0][0]), "+v"(ef[c][0][1]), "+v"(ef[c][1][0]), "+v"(ef[c][1][1]));
  }
}
DI void stage_o(const u32x4 (&ef)[4][2][2], int head0, int rt32, float g, srd_t ws, u64 tag, int lane) {
  const int h = lane >> 5;
  flag_wait(ws, WS_FF + head0 * 64 + (lane & 31) * 8, tag);
  u32x4 kf[4][2][2];
#pragma unroll
  for (int c = 0; c < 4; ++c)
#pragma unroll
    for (int t = 0; t < 2; ++t)
#pragma unroll
      for (int s = 0; s < 2; ++s) ldg_sc1(kf[c][t][s], lane * 16, ws, WS_KVF + ((head0 + c) * 4 + t * 2 + s) * 1024);
  asm volatile("s_waitcnt vmcnt(0)"
               : "+v"(kf[0][0][0]), "+v"(kf[0][0][1]), "+v"(kf[0][1][0]), "+v"(kf[0][1][1]), "+v"(kf[1][0][0]), "+v"(kf[1][0][1]), "+v"(kf[1][1][0]), "+v"(kf[1][1][1]),
                 "+v"(kf[2][0][0]), "+v"(kf[2][0][1]), "+v"(kf[2][1][0]), "+v"(kf[2][1][1]), "+v"(kf[3][0][0]), "+v"(kf[3][0][1]), "+v"(kf[3][1][0]), "+v"(kf[3][1][1])
               :: "memory");
#pragma unroll
  for (int c = 0; c < 4; ++c) {
    f32x16 np, nn;
#pragma unroll
    for (int e = 0; e < 16; ++e) { np[e] = 0.f; nn[e] = 0.f; }
    np = mfma(kf[c][0][0], ef[c][0][0], np); nn = mfma(kf[c][1][0], ef[c][1][0], nn);
    np = mfma(kf[c][0][1], ef[c][0][1], np); nn = mfma(kf[c][1][1], ef[c][1][1], nn);
    const float rp = __builtin_amdgcn_rcpf(np[13]), rn = __builtin_amdgcn_rcpf(nn[13]);
    const float inv = __builtin_amdgcn_rcpf(np[12] * rp + nn[12] * rn + 1e-6f);
    const float cp = swap_hi(rp * inv, h) * g, cn = swap_hi(rn * inv, h) * g;
    f32x16 o;
#pragma unroll
    for (int e = 0; e < 16; ++e) o[e] = np[e] * cp + nn[e] * cn;
    u32x4 o0 = pack8(o, 0), o1 = pack8(o, 1);
    o1.z = h ? 0u : o1.z; o1.w = h ? 0u : o1.w;
    stg_sc1(o0, lane * 16, ws, WS_O + (rt32 * 32 + (head0 + c) * 2) * 1024);
    stg_sc1(o1, lane * 16, ws, WS_O + (rt32 * 32 + (head0 + c) * 2 + 1) * 1024);
  }
  stg32_sc1(__float_as_uint(1.f - g), (lane & 31) * 4, ws, WS_OMG + ((head0 >> 2) * 2048 + rt32 * 32) * 4);
  VMWAIT(0);
  flag_set(ws, WS_FO + rt32 * 32 + (head0 >> 2) * 8, tag);
}

DI void epi_h(const f32x16 (&acc)[4], srd_t ws, u32 soff, int lane) {
#pragma unroll
  for (int c = 0; c < 4; ++c) {
    float hv[8];
#pragma unroll
    for (int e = 0; e < 8; ++e) {
      const float x = acc[c][e];
      const float t = __builtin_amdgcn_exp2f(x * __builtin_fmaf(x * x, -0.10294323970f, -2.30220819814f));
      hv[e] = x * __builtin_amdgcn_rcpf(1.f + t) * acc[c][e + 8];
    }
    stg_sc1(u32x4{cvtpk(hv[0], hv[1]), cvtpk(hv[2], hv[3]), cvtpk(hv[4], hv[5]), cvtpk(hv[6], hv[7])}, lane * 16, ws, soff + c * 1024);
  }
}

template <bool P1, bool isH> DI void body(const Params& p, const int wave) {
  const int tid = threadIdx.x, lane = tid & 63;
  const int w = wave & 3;
  const int b = blockIdx.x, xcd = b & 7, bi = b >> 3;
  const int m = 4 * (xcd >> 1) + (bi & 3), sl = bi >> 2, half = xcd & 1;
  const int j = 7 * half + sl;
  const int kvhead = isH ? 8 * half + sl + (sl >= 4 - half ? 1 : 0) : 4 + 7 * half;
  const int h = lane >> 5;
  const srd_t ws = mksrd(p.ws, WS_END);
  STAMP(0);

  if (P1) *(LAS u32*)(size_t)(L_FULL + (lane & 15) * 4) = 0u;
  u32x2 ev;
  asm volatile("buffer_load_dwordx2 %0, off, %1, 0 sc1" : "=v"(ev) : "s"(ws) : "memory");

  asm volatile("s_waitcnt lgkmcnt(0)\n\ts_barrier" ::: "memory");
  if (P1) {
    asm volatile("s_waitcnt vmcnt(0)" : "+v"(ev)::"memory");
    const u64 tag = (((u64)rfl(ev.y) << 32) | rfl(ev.x)) + 1;
    STAMP(1);
    u32x4 xf[28];
    f32x16 acc[4];
    unit_gemm<true, true>(xf, acc, lane, w, [](int) {} STAMP_PASS);
    STAMP(2);
    const int rt32 = m * 4 + w;
    if (isH) {
      epi_h(acc, ws, WS_H + (rt32 * 112 + j * 4) * 1024, lane);
      STAMP(3);
      unit_gemm<false, true>(xf, acc, lane, w, [&](int s) {
        if (s == 1) { VMWAIT(0); flag_set(ws, WS_FH + rt32 * 128 + j * 8, tag); }
      } STAMP_PASS);
      STAMP(4);
      epi_h(acc, ws, WS_H + (rt32 * 112 + (14 + j) * 4) * 1024, lane);
      VMWAIT(0);
      flag_set(ws, WS_FH + (64 + rt32) * 128 + j * 8, tag);
      STAMP(5);
    } else {
      u32x4 ef[4][2][2];
      epi_q(acc, ef, L_WPHI, lane);
      STAMP(3);
      {
        const int qw = (m * 2 + half) * 4 + w, hd = qw >> 3, pc = qw & 7;
        flag_wait(ws, WS_FP + (hd * 4 + (pc >> 1)) * 128 + (lane & 15) * 8, tag);
        STAMP(4);
        u32x4 pv[16];
#pragma unroll
        for (int i = 0; i < 16; ++i) ldg_sc1(pv[i], lane * 16, ws, WS_KVP + ((hd * 16 + i) * 8 + pc) * 1024);
        asm volatile("s_waitcnt vmcnt(0)"
                     : "+v"(pv[0]), "+v"(pv[1]), "+v"(pv[2]), "+v"(pv[3]), "+v"(pv[4]), "+v"(pv[5]), "+v"(pv[6]), "+v"(pv[7]), "+v"(pv[8]), "+v"(pv[9]),
                       "+v"(pv[10]), "+v"(pv[11]), "+v"(pv[12]), "+v"(pv[13]), "+v"(pv[14]), "+v"(pv[15])::"memory");
        f32x4 sm = __builtin_bit_cast(f32x4, pv[0]);
#pragma unroll
        for (int i = 1; i < 16; ++i) sm += __builtin_bit_cast(f32x4, pv[i]);
        u32x2 o = {cvtpk(sm.x, sm.y), cvtpk(sm.z, sm.w)};
        if ((lane & 31) == 29) { const u32 one2 = ((pc & 3) == 3 && h) ? 0u : 0x3f803f80u; o = u32x2{one2, one2}; }
        stg64_sc1(o, lane * 16 + (pc & 1) * 8, ws, WS_KVF + (hd * 4 + (pc >> 1)) * 1024);
        VMWAIT(0);
        flag_set(ws, WS_FF + hd * 64 + pc * 8, tag);
      }
      STAMP(5);
      lds_wait<5>(L_SYNC + (lane & 3) * 4);
      const float g = *(LAS float*)(size_t)(L_G + (w * 32 + (lane & 31)) * 4);
      stage_o(ef, 8 * half, rt32, g, ws, tag, lane);
      STAMP(6);
      STAMP_FLUSH();
    }
  } else {
    __builtin_amdgcn_s_setprio(PRIO2);
    const srd_t sx = mksrd(p.x, S * D * 2), sqkv = mksrd(p.wqkv, D * 3 * D * 2);
    {
      const srd_t sp = mksrd(p.wphi, 16 * 1568);
      if (isH) { if (w < 2) dma_buf(L_WPHI + w * 1024, lane * 16, sp, kvhead * 1568 + w * 1024); }
      else {
#pragma unroll
        for (int i = 0; i < 4; ++i) if (i * 4 + w < 13) dma_buf(L_WPHI + (i * 4 + w) * 1024, lane * 16, sp, half * 8 * 1568 + (i * 4 + w) * 1024);
        dma_buf(L_GW, lane * 16, mksrd(p.gw, D * 2), 0);
      }
    }
    u32 xvo[2];
#pragma unroll
    for (int i = 0; i < 2; ++i) xvo[i] = (lane >> 3) * 896 + (((lane & 7) ^ ((4 * i + (lane >> 4)) & 7)) * 16);
    const u32 xso = (m * 128 + w * 32) * 896;
    const char* wptr; u32 wrs, wu2;
    {
      const u32 row = lane >> 4, mat = (lane >> 3) & 1, c = ((lane >> 1) & 3) ^ row, pc = lane & 1;
      if (isH) { wrs = 3584; wptr = (mat ? p.wu : p.wg) + (w * 16 + row) * 3584 + j * 128 + c * 32 + pc * 16; wu2 = 14 * 128; }
      else     { wrs = 2688; wptr = p.wqkv + (w * 16 + row) * 2688 + (8 * half + c) * 56 + mat * 32 + pc * 16; wu2 = 4 * 56; }
    }
    const u32 kvo = (lane >> 2) * 2688 + (lane & 3) * 16;
    const u32 kso = (1 + (w >> 1)) * 896 + kvhead * 56;
    u32x4 xf[28];
    const u32 r = lane & 31;
    const u32 xb = w * 4096 + r * 128 + ((h ^ ((r >> 1) & 7)) * 16);
    const u32 l4 = (lane & 3) * 4, l16 = lane * 16;
    const srd_t spa = mksrd(w == 0 ? p.x : w == 1 ? p.wg : w == 2 ? p.wu : p.wd, w == 0 ? S * D * 2 : D * DF * 2);
    const srd_t spb = mksrd(w == 3 ? p.wo : p.wqkv, w == 3 ? D * D * 2 : D * 3 * D * 2);
    f32x16 ga;
#pragma unroll
    for (int e = 0; e < 16; ++e) ga[e] = 0.f;
    u32 pre = 0;
    auto landed = [&](auto ic) {
      constexpr int it = decltype(ic)::v, kind = ITEMS[it].kind, idx = ITEMS[it].idx;
      if constexpr (kind == 0) {
#pragma unroll
        for (int kk = 0; kk < 4; ++kk) xf[4 * idx + kk] = lds128(((2 * idx) % 7) * 16384 + (xb ^ (kk * 32)));
      } else if constexpr (kind == 2) lds_post(L_FULK + w * 4, idx + 1);
      else if constexpr (kind != 4) {
        lds_post(L_FULL + w * 4, ring_idx(it) + 1);
        if constexpr (!isH && kind == 1) {
#pragma unroll
          for (int kk = 0; kk < 4; ++kk) ga = mfma(lds128(L_GW + (4 * idx + kk) * 32 + h * 16), xf[4 * idx + kk], ga);
          asm volatile("" : "+v"(ga));
        }
      }
    };
    auto step = [&](auto ic) {
      constexpr int I = decltype(ic)::v;
      if constexpr (I < NLD1) {
        constexpr int it = item_of(I), i = I - item_first(it), kind = ITEMS[it].kind, idx = ITEMS[it].idx;
        if constexpr (i == 0) {
          if constexpr (kind == 2) { if constexpr (idx >= 2) lds_wait<idx - 1>(L_SYNC + l4); }
          else if constexpr (needs_prog(it)) lds_wait_pre<ring_idx(it) - 6>(L_PROG + l4, pre);
        }
        constexpr u32 sb = kind == 2 ? L_SCR + (idx & 1) * 16384 : (ring_idx(it) % 7) * 16384;
        if constexpr (kind == 0) dma_buf(sb + w * 4096 + i * 1024, xvo[i & 1], sx, xso + i * 8 * 896 + idx * 128);
        else if constexpr (kind == 1 || kind == 3) dma_glb(sb + w * 4096 + i * 1024, wptr + ((idx * 64 + i * 4) * wrs + (kind == 1 ? 0u : wu2)));
        else if constexpr (kind == 4) {
          if constexpr (i < 7) dma_buf(L_SCR + w * 4096, l16, spa, (i * 256 + b) * 1024);
          else dma_buf(L_SCR + w * 4096, l16, spb, ((w == 3 ? i - 7 : w * 2 + i - 7) * 256 + b) * 1024);
        }
        else if constexpr (idx < 3) dma_buf(sb + w * 4096 + i * 1024, kvo, sqkv, kso + (idx * 128 + (w & 1) * 64 + i * 16) * 2688);
        else dma_buf(sb + (w >> 1) * 8192 + (w & 1) * 2048 + i * 1024, kvo, sqkv, kso + (384 + (w & 1) * 32 + i * 16) * 2688);
        if constexpr (i == item_n(it) - 2 && needs_prog(it + 1)) pre = lds_peek(L_PROG + l4);
        constexpr int J = I - LAG;
        if constexpr (J >= 0) {
          if constexpr (item_first(item_of(J) + 1) == J + 1 && ITEMS[item_of(J)].kind != 4) {
            if constexpr (J == 3) asm volatile("s_waitcnt vmcnt(%1)" : "+v"(ev) : "n"(LAG) : "memory"); else VMWAIT(LAG);
            landed(IC<item_of(J)>{});
            FSTAMP(16 + item_of(J));
          }
        }
        SB();
      }
    };
    auto steps = [&](auto ac, auto bc) { sfor<decltype(bc)::v - decltype(ac)::v>([&](auto kc) { step(IC<decltype(ac)::v + decltype(kc)::v>{}); }); };
#define STEPS(a, b) steps(IC<(a)>{}, IC<(b)>{})
#define TICK(a) step(IC<(a)>{})
    f32x16 ka, va;
#pragma unroll
    for (int e = 0; e < 16; ++e) { ka[e] = 0.f; va[e] = 0.f; }
    auto kv_item = [&](auto tc, auto fc) {
      constexpr int t = decltype(tc)::v, F0 = decltype(fc)::v, NK = t < 3 ? 8 : 4;
      const u32 cb = (lane >> 4) & 1, q = (lane & 15) >> 2, pp = lane & 3;
      lds_wait<t + 1>(L_FULK + l4);
      u32 a = L_SCR + (t & 1) * 16384 + (8 * h + q) * 64 + cb * 32 + 8 * pp; OPAQUE(a);
      u32x4 fk[3], fv[3];
      auto reads = [&](int kk) { fk[kk % 3] = trfrag(a + kk * 1024, a + kk * 1024 + 256); fv[kk % 3] = trfrag(a + kk * 1024 + 8192, a + kk * 1024 + 8192 + 256); };
      reads(0); reads(1);
      SB();
      sfor<NK>([&](auto kc) {
        constexpr int kk = decltype(kc)::v;
        TICK(F0 + kk);
        if (kk + 2 < NK) reads(kk + 2);
        if (kk + 3 == NK) lds_post(L_SYNC + w * 4, t + 1);
        SB();
        ka = mfma(fk[kk % 3], xf[8 * t + kk], ka);
        va = mfma(xf[8 * t + kk], fv[kk % 3], va);
        SB();
        asm volatile("" : "+v"(ka), "+v"(va));
      });
    };
    STEPS(0, F(12));
    STAMP(1);
    kv_item(IC<0>{}, IC<F(12)>{});
    STEPS(F(14), F(15));
    kv_item(IC<1>{}, IC<F(15)>{});
    STEPS(F(17), F(18) + 2);
    kv_item(IC<2>{}, IC<F(18) + 2>{});
    STEPS(F(20) + 2, F(21));
    u32x4 wp[2]; load_wphi(wp, L_WPHI + (isH ? 0 : (kvhead - 8 * half) * 1568), lane);
    kv_item(IC<3>{}, IC<F(21)>{});
    STAMP(3);
    const u64 tag = (((u64)rfl(ev.y) << 32) | rfl(ev.x)) + 1;
    float g = 0.f;
    if (!isH) {
      g = __builtin_amdgcn_rcpf(1.f + __builtin_amdgcn_exp2f(-LOG2E * ga[0]));
      *(LAS float*)(size_t)(L_G + (w * 32 + r) * 4) = g;
    }
    {
      constexpr int E = F(22);
      const u32 scr = L_SCR + w * 8192;
      TICK(E);
      f32x16 pz;
#pragma unroll
      for (int e = 0; e < 16; ++e) pz[e] = 0.f;
      pz = mfma(wp[0], pack8(ka, 0), pz);
      pz = mfma(wp[1], pack8(ka, 1), pz);
      u32x4 vf[2];
#pragma unroll
      for (int s = 0; s < 2; ++s) {
        vf[s] = pack8(va, s);
        const u32 ov = r == 28 ? 0x3f803f80u : 0u;
        if (r >= 28) vf[s] = u32x4{ov, ov, ov, ov};
      }
      asm volatile("" : "+v"(pz), "+v"(vf[0]), "+v"(vf[1]));
      TICK(E + 1);
      float mx = max3(max3(max3(pz[0], pz[1], pz[2]), max3(pz[3], pz[4], pz[5]), max3(pz[6], pz[7], pz[8])),
                      max3(max3(pz[9], pz[10], pz[11]), max3(pz[12], pz[13], pz[14]), pz[15]), pz[15]);
      float mn = min3(min3(min3(pz[0], pz[1], pz[2]), min3(pz[3], pz[4], pz[5]), min3(pz[6], pz[7], pz[8])),
                      min3(min3(pz[9], pz[10], pz[11]), min3(pz[12], pz[13], pz[14]), pz[15]), pz[15]);
      asm volatile("" : "+v"(mx), "+v"(mn));
      TICK(E + 2);
      { float a, c; both_halves(mx, a, c); mx = fmaxf(a, c) * LOG2E; both_halves(mn, a, c); mn = fminf(a, c) * LOG2E; }
      asm volatile("" : "+v"(mx), "+v"(mn));
      TICK(E + 3);
      float ev2[2][16];
      sfor<6>([&](auto gc) {
        constexpr int g6 = decltype(gc)::v, lo = g6 * 32 / 6, hi = (g6 + 1) * 32 / 6;
#pragma unroll
        for (int e = lo; e < hi; ++e) {
          ev2[e >> 4][e & 15] = __builtin_amdgcn_exp2f(e < 16 ? __builtin_fmaf(pz[e & 15], LOG2E, -mx) : __builtin_fmaf(pz[e & 15], -LOG2E, mn));
          asm volatile("" : "+v"(ev2[e >> 4][e & 15]));
        }
        TICK(E + 4 + g6);
      });
      f32x16 ep, en;
#pragma unroll
      for (int e = 0; e < 16; ++e) { ep[e] = ev2[0][e]; en[e] = ev2[1][e]; }
      normalize(ep, h);
      asm volatile("" : "+v"(ep));
      TICK(E + 10);
      pre = lds_peek(L_SYNC + l4);
      normalize(en, h);
      asm volatile("" : "+v"(en));
      lds_wait_pre<4>(L_SYNC + l4, pre);
      pre = 0;
      TICK(E + 11);
      const u32 sw = (r >> 1) & 7;
#pragma unroll
      for (int g4 = 0; g4 < 4; ++g4) {
        const u32 a = scr + r * 64 + (((2 * g4 + h) ^ sw) * 8);
        sts64(a, u32x2{cvtpk(ep[4 * g4], ep[4 * g4 + 1]), cvtpk(ep[4 * g4 + 2], ep[4 * g4 + 3])});
        sts64(a + 2048, u32x2{cvtpk(en[4 * g4], en[4 * g4 + 1]), cvtpk(en[4 * g4 + 2], en[4 * g4 + 3])});
      }
      const u32 fb = (lane >> 4) & 1, q = (lane & 15) >> 2, pp = lane & 3;
      u32x4 pf[2][2];
#pragma unroll
      for (int s = 0; s < 2; ++s) {
        const u32 t0 = 16 * s + 4 * h + q, t1 = t0 + 8;
        const u32 a0 = scr + t0 * 64 + (((4 * fb + pp) ^ ((t0 >> 1) & 7)) * 8), a1 = scr + t1 * 64 + (((4 * fb + pp) ^ ((t1 >> 1) & 7)) * 8);
        pf[s][0] = trfrag(a0, a1); pf[s][1] = trfrag(a0 + 2048, a1 + 2048);
      }
      TICK(E + 12);
      TICK(E + 13);
      f32x16 kp, kn;
#pragma unroll
      for (int e = 0; e < 16; ++e) { kp[e] = 0.f; kn[e] = 0.f; }
#pragma unroll
      for (int s = 0; s < 2; ++s) { kp = mfma(pf[s][0], vf[s], kp); kn = mfma(pf[s][1], vf[s], kn); }
      asm volatile("" : "+v"(kp), "+v"(kn));
      TICK(E + 14);
      TICK(E + 15);
#pragma unroll
      for (int g4 = 0; g4 < 4; ++g4) {
        sts128(scr + g4 * 1024 + l16, __builtin_bit_cast(u32x4, f32x4{kp[4 * g4], kp[4 * g4 + 1], kp[4 * g4 + 2], kp[4 * g4 + 3]}));
        sts128(scr + 4096 + g4 * 1024 + l16, __builtin_bit_cast(u32x4, f32x4{kn[4 * g4], kn[4 * g4 + 1], kn[4 * g4 + 2], kn[4 * g4 + 3]}));
      }
      lds_post(L_SYNC + w * 4, 5);
      VMWAIT(8); landed(IC<23>{});
      lds_wait<5>(L_SYNC + l4);
#pragma unroll
      for (int i = 0; i < 2; ++i) {
        f32x4 sm = __builtin_bit_cast(f32x4, lds128(L_SCR + (w * 2 + i) * 1024 + l16));
#pragma unroll
        for (int u = 1; u < 4; ++u) sm += __builtin_bit_cast(f32x4, lds128(L_SCR + u * 8192 + (w * 2 + i) * 1024 + l16));
        stg_sc1(__builtin_bit_cast(u32x4, sm), l16, ws, WS_KVP + ((kvhead * 16 + m) * 8 + w * 2 + i) * 1024);
      }
      lds_post(L_SYNC + w * 4, 6);
      VMWAIT(2); landed(IC<25>{});
    }
    STAMP(4);
    if (!isH) {
      VMWAIT(0);
      flag_set(ws, WS_FP + (kvhead * 4 + w) * 128 + m * 8, tag);
      f32x16 acc[4];
      unit_gemm<false, false>(xf, acc, lane, w, [](int) {} STAMP_PASS);
      STAMP(5);
      u32x4 ef[4][2][2];
      epi_q(acc, ef, L_WPHI + 4 * 1568, lane);
      STAMP(6);
      stage_o(ef, 8 * half + 4, m * 4 + w, g, ws, tag, lane);
      STAMP(7);
      STAMP_FLUSH();
    } else {
      const int rt64 = 4 * xcd + (bi & 3), ct = sl;
      const srd_t swd = mksrd(p.wd, D * DF * 2), swo = mksrd(p.wo, D * D * 2);
      const u32 stg = L_SCR + w * 10240, rng = w * 28672;
      f32x16 acc[2][2];
#pragma unroll
      for (int i = 0; i < 4; ++i)
#pragma unroll
        for (int e = 0; e < 16; ++e) acc[i >> 1][i & 1][e] = 0.f;
      const u32 brow = lane >> 3;
      const u32 bpc = ((lane & 7) ^ (((brow >> 1) & 1) * 4)) * 16;
      const u32 bvo = brow * 896 + bpc;
      const u32 bvo_pad = (brow < 4 ? brow : 3u) * 896 + bpc;
      const u32 cb = (lane >> 4) & 1, q = (lane & 15) >> 2, pp = lane & 3;
      u32 rbs[2], rbr[2];
#pragma unroll
      for (int c = 0; c < 2; ++c) {
        const u32 o = (4 * h + q) * 128 + ((c ^ ((q >> 1) & 1)) * 64) + cb * 32 + pp * 8;
        rbs[c] = stg + o; rbr[c] = rng + o; OPAQUE(rbs[c]); OPAQUE(rbr[c]);
      }
      u32x4 af[14][2], bf[2][2];
      u32 om[2];
      u64 fv;
      const u32 hso = WS_H + (rt64 * 2 * 112 + w) * 1024, oso = WS_O + (rt64 * 2 * 32 + w * 8) * 1024;
      const u32 dso = w * 16 * 896 + ct * 128, wso = w * 112 * 896 + ct * 128;
      const u32 fl = lane & 31;
      const u32 fho = WS_FH + (rt64 * 2 + (fl >> 4)) * 128 + ((fl & 15) < 14 ? (fl & 15) : 0) * 8;
      const u32 foo = WS_FO + (rt64 * 2 + (lane & 1)) * 32 + w * 8;
      auto op = [&](auto kc) {
        constexpr int K = decltype(kc)::v;
        if constexpr (K < NOP2) {
          constexpr Op2 o = OPS2.v[K];
          if constexpr (o.pre == 1) { flag_check<K - 1 - find_op(5, 0, 0)>(fv, ws, fho, tag); STAMP(5); }
          if constexpr (o.pre == 2) { flag_check<K - 1 - find_op(5, 1, 0)>(fv, ws, fho + 64 * 128, tag); STAMP(7); }
          if constexpr (o.pre == 3) { flag_check<K - 1 - find_op(5, 2, 0)>(fv, ws, foo, tag); STAMP(9); }
          if constexpr (o.pre == 4) lds_wait<21>(L_PROG + l4);
          if constexpr (o.pre == 5) VMWAIT(K);
          if constexpr (o.kind == 0) {
            const u32 dst = (b_in_ring(o.job) ? rng : stg) + b_off(o.job) + o.i * 1024;
            if constexpr (o.job < 28) dma_buf(dst, bvo, swd, dso + (o.job * 64 + o.i * 8) * 896);
            else dma_buf(dst, ((o.job & 1) && o.i) ? bvo_pad : bvo, swo, wso + (((o.job - 28) >> 1) * 28 + (o.job & 1) * 16 + o.i * 8) * 896);
          } else if constexpr (o.kind == 1) {
            if constexpr (o.job < 28) ldg_sc1(af[o.job % 14][o.i], l16, ws, hso + o.job * 4096 + o.i * 112 * 1024);
            else ldg_sc1(af[o.job % 14][o.i], l16, ws, oso + (o.job - 28) * 1024 + o.i * 32 * 1024);
          } else if constexpr (o.kind == 5) flag_peek(fv, ws, o.job == 0 ? fho : o.job == 1 ? fho + 64 * 128 : foo);
          else if constexpr (o.kind == 6) flag_set(ws, WS_FP + (kvhead * 4 + w) * 128 + m * 8, tag);
          else asm volatile("buffer_load_dword %0, %1, %2, %3 offen sc1" : "=v"(om[o.i]) : "v"(r * 4), "s"(ws), "s"(WS_OMG + (w * 2048 + rt64 * 64 + o.i * 32) * 4) : "memory");
          SB();
        }
      };
      auto ops = [&](auto ac, auto bc) { sfor<decltype(bc)::v - decltype(ac)::v>([&](auto kc) { op(IC<decltype(ac)::v + decltype(kc)::v>{}); }); };
      auto bread = [&](int job, int c) {
        const u32 a = (b_in_ring(job) ? rbr[c] : rbs[c]) + b_off(job);
        bf[job & 1][c] = trfrag(a, a + 1024);
      };
      lds_wait<6>(L_SYNC + l4);
      ops(IC<0>{}, IC<job_pos(0)>{});
      VMWAIT(job_pos(0) - 1 - find_op(0, 0, 1));
      bread(0, 0); bread(0, 1);
      sfor<36>([&](auto nc) {
        constexpr int n = decltype(nc)::v, P = job_pos(n), Q = job_pos(n + 1), NL = Q - P;
        constexpr int need = n < 35 ? cmax(find_op(1, n, 1), find_op(0, n + 1, 1)) : find_op(1, n, 1);
        static_assert(need < P, "stream order");
        if constexpr (n == 28) {
          asm volatile("s_waitcnt vmcnt(%2)" : "+v"(om[0]), "+v"(om[1]) : "n"(P - 1 - find_op(7, 0, 1)) : "memory");
#pragma unroll
          for (int i = 0; i < 4; ++i)
#pragma unroll
            for (int e = 0; e < 16; ++e) acc[i >> 1][i & 1][e] *= __uint_as_float(om[i & 1]);
        }
        asm volatile("s_waitcnt vmcnt(%2)" : "+v"(af[n % 14][0]), "+v"(af[n % 14][1]) : "n"(P - 1 - need) : "memory");
        sfor<4>([&](auto uc) {
          constexpr int u = decltype(uc)::v, c = u >> 1, t = u & 1;
          acc[c][t] = mfma(bf[n & 1][c], af[n % 14][t], acc[c][t]);
          if constexpr (n < 35 && u < 2) bread(n + 1, u);
          SB();
          asm volatile("" : "+v"(acc[c][t]));
          ops(IC<P + NL * u / 4>{}, IC<P + NL * (u + 1) / 4>{});
        });
        if constexpr (n == 13) STAMP(6);
        if constexpr (n == 27) STAMP(8);
      });
      STAMP(10);
      if (b == 0 && w == 0) flag_set(ws, WS_E, tag);
      asm volatile("s_barrier" ::: "memory");
#pragma unroll
      for (int c = 0; c < 2; ++c)
#pragma unroll
        for (int t = 0; t < 2; ++t)
#pragma unroll
          for (int g4 = 0; g4 < 4; ++g4)
            sts128(w * 17408 + (t * 32 + r) * 272 + (c * 32 + g4 * 8 + h * 4) * 4,
                   __builtin_bit_cast(u32x4, f32x4{acc[c][t][4 * g4], acc[c][t][4 * g4 + 1], acc[c][t][4 * g4 + 2], acc[c][t][4 * g4 + 3]}));
    }
  }
  if (isH) {
    if (P1) asm volatile("s_barrier" ::: "memory");
    asm volatile("s_waitcnt lgkmcnt(0)\n\ts_barrier" ::: "memory");
    const int tok = tid >> 3, pc = tid & 7;
    f32x4 s0 = {0.f, 0.f, 0.f, 0.f}, s1 = s0;
#pragma unroll
    for (int u = 0; u < 4; ++u) {
      s0 += __builtin_bit_cast(f32x4, lds128(u * 17408 + tok * 272 + pc * 32));
      s1 += __builtin_bit_cast(f32x4, lds128(u * 17408 + tok * 272 + pc * 32 + 16));
    }
    const int rt64 = 4 * xcd + (bi & 3);
    *(u32x4*)(p.out + (rt64 * 64 + tok) * 896 + sl * 128 + pc * 16) = u32x4{cvtpk(s0.x, s0.y), cvtpk(s0.z, s0.w), cvtpk(s1.x, s1.y), cvtpk(s1.z, s1.w)};
    STAMP(12);
    STAMP_FLUSH();
  }
}
__global__ __launch_bounds__(512, 2) void fused(Params p) {
  const u64 t0_ = __builtin_amdgcn_s_memrealtime();
  const int wave = rfl(threadIdx.x >> 6);
  if ((blockIdx.x >> 5) < 7) {
    if (wave < 4) body<true, true>(p, wave); else body<false, true>(p, wave);
  } else {
    if (wave < 4) body<true, false>(p, wave); else body<false, false>(p, wave);
  }
  while (__builtin_amdgcn_s_memrealtime() - t0_ < 4500) __builtin_amdgcn_s_sleep(1);
}
}

extern "C" void kernel_launch(void* const* d_in, const int* in_sizes, int n_in, void* d_out, int out_size, void* d_ws, size_t ws_size,
                              hipStream_t stream) {
  hk::Params p;
  p.x = (const char*)d_in[0]; p.wqkv = (const char*)d_in[1]; p.wphi = (const char*)d_in[2]; p.wo = (const char*)d_in[3];
  p.wg = (const char*)d_in[5]; p.wu = (const char*)d_in[6]; p.wd = (const char*)d_in[7]; p.gw = (const char*)d_in[8];
  p.out = (char*)d_out; p.ws = (char*)d_ws;
  (void)hipFuncSetAttribute((const void*)hk::fused, hipFuncAttributeMaxDynamicSharedMemorySize, hk::L_TOTAL);
  hk::fused<<<256, 512, hk::L_TOTAL, stream>>>(p);
}
```

```cpp
#include <hip/hip_runtime.h>
#include <stdint.h>

#define NOWAIT 0
#define SLEEPN "1"
#define PRIO0 2
#define PRIOE 3
#define NOBEPI 7
#define PFM 15
#define KVT 16
#define T2S 3
#define QAS 3
#define PRIOQ 1
#define PAUSE 0
#define PRIO2 2
#define PRIO1 1
#define PRIO3 0
#define REDAT 1
#define STAMPS 0

#if defined(__HIP_DEVICE_COMPILE__)
#pragma clang attribute push(__attribute__((target("no-packed-fp32-ops"))), apply_to = function)
#endif
namespace hk {
typedef unsigned u32;
typedef unsigned long long u64;
typedef __bf16 bf16x8 __attribute__((ext_vector_type(8)));
typedef __bf16 bf16x2 __attribute__((ext_vector_type(2)));
typedef short s16x4 __attribute__((ext_vector_type(4)));
typedef short s16x8 __attribute__((ext_vector_type(8)));
typedef u32 u32x4 __attribute__((ext_vector_type(4)));
typedef u32 u32x2 __attribute__((ext_vector_type(2)));
typedef float f32x16 __attribute__((ext_vector_type(16)));
typedef float f32x4 __attribute__((ext_vector_type(4)));
typedef float f32x2 __attribute__((ext_vector_type(2)));
typedef __amdgpu_buffer_rsrc_t srd_t;
#define LAS __attribute__((address_space(3)))
#define DI __device__ __forceinline__

constexpr int S = 2048, D = 448, DF = 1792;
constexpr u32 WS_E = 0;
constexpr u32 WS_FK = 4096;
constexpr u32 WS_G = 32768;
constexpr u32 WS_FQ = 131072;
constexpr u32 WS_FO = 262144;
constexpr u32 WS_FH = 393216;
constexpr u32 WS_KVF = 655360;
constexpr u32 WS_KVP = 720896;
constexpr u32 WS_O = WS_KVP + 16 * 16 * 4096;
constexpr u32 WS_Q = WS_O + 64 * 32 * 1024;
constexpr u32 WS_H = WS_Q + 64 * 32 * 1024;
constexpr u32 WS_KT = WS_H + 64 * 112 * 1024;
constexpr u32 WS_END = WS_KT + 16 * 56 * 1024;
constexpr u32 WS_STAMP = 24u << 20;
constexpr u32 OOB = 0x7ff00000u;
constexpr u32 L_K3 = 9 * 16384;
constexpr u32 L_WPHI = L_K3 + 8192;
constexpr u32 L_GW = L_WPHI + 2048;
constexpr u32 L_Z = L_GW + 1024;
constexpr u32 L_CNT = L_Z + 4096;
constexpr u32 L_DUM = L_CNT + 64;
constexpr u32 L_TOTAL = 163840;
enum { C_PROG = 0, C_SYNC = 16 };

struct Params {
  const char *x, *wqkv, *wphi, *wo, *wg, *wu, *wd, *gw;
  char *out, *ws;
};

DI u32 f2u(float f) { return __builtin_bit_cast(u32, f); }
DI float u2f(u32 u) { return __builtin_bit_cast(float, u); }
DI u32 min(u32 a, u32 b) { return a < b ? a : b; }
DI u32 max(u32 a, u32 b) { return a > b ? a : b; }
DI u32 rfl(u32 v) { return __builtin_amdgcn_readfirstlane(v); }
DI srd_t mksrd(const void* p, u32 bytes) { return __builtin_amdgcn_make_buffer_rsrc((void*)p, 0, bytes, 0x00020000); }
DI u32 cvtpk(float lo, float hi) { f32x2 v = {lo, hi}; return __builtin_bit_cast(u32, __builtin_convertvector(v, bf16x2)); }
DI f32x16 mfma(u32x4 a, u32x4 b, f32x16 c) {
  return __builtin_amdgcn_mfma_f32_32x32x16_bf16(__builtin_bit_cast(bf16x8, a), __builtin_bit_cast(bf16x8, b), c, 0, 0, 0);
}
DI u32x2 tr64(u32 addr) { return __builtin_bit_cast(u32x2, __builtin_amdgcn_ds_read_tr16_b64_v4i16((LAS s16x4*)(size_t)addr)); }
DI u32x4 trfrag(u32 a0, u32 a1) { u32x2 lo = tr64(a0), hi = tr64(a1); return u32x4{lo.x, lo.y, hi.x, hi.y}; }
DI u32x4 lds128(u32 addr) { return *(LAS u32x4*)(size_t)addr; }
DI void sts128(u32 addr, u32x4 v) { *(LAS u32x4*)(size_t)addr = v; }
DI void sts64(u32 addr, u32x2 v) { *(LAS u32x2*)(size_t)addr = v; }
#define MEMFENCE() asm volatile("" ::: "memory")
#define VMWAIT(n) asm volatile("s_waitcnt vmcnt(%0)" ::"n"(n) : "memory")
DI void dma_buf(u32 ldsaddr, u32 voff, srd_t srd, u32 soff) {
  asm volatile("s_mov_b32 m0, %0\n\ts_nop 0\n\tbuffer_load_dwordx4 %1, %2, %3 offen lds" ::"s"(ldsaddr), "v"(voff), "s"(srd), "s"(soff) : "memory");
}
template <u32 IMM, u32 SIMM> DI u32 dma_open(u32 base, u32 sbase) {
  u32 so;
  asm volatile("s_add_u32 m0, %1, %2\n\ts_add_u32 %0, %3, %4" : "=s"(so) : "s"(base), "n"(IMM), "s"(sbase), "n"(SIMM) : "scc", "memory");
  return so;
}
template <int OFF> DI void dma_off(u32 voff, srd_t srd, u32 soff) {
  asm volatile("buffer_load_dwordx4 %0, %1, %2 offen offset:%3 lds" ::"v"(voff), "s"(srd), "s"(soff), "n"(OFF) : "memory");
}
template <int OFF> DI void dma_off_sc1(u32 voff, srd_t srd, u32 soff) {
  asm volatile("buffer_load_dwordx4 %0, %1, %2 offen offset:%3 sc1 lds" ::"v"(voff), "s"(srd), "s"(soff), "n"(OFF) : "memory");
}
template <int OFF> DI void ldg_sc1o(u32x4& dst, u32 voff, srd_t srd, u32 soff) {
  asm volatile("buffer_load_dwordx4 %0, %1, %2, %3 offen offset:%4 sc1" : "=v"(dst) : "v"(voff), "s"(srd), "s"(soff), "n"(OFF) : "memory");
}
DI void ldg(u32x4& dst, u32 voff, srd_t srd, u32 soff) {
  asm volatile("buffer_load_dwordx4 %0, %1, %2, %3 offen" : "=v"(dst) : "v"(voff), "s"(srd), "s"(soff) : "memory");
}
DI void touch(u32& sink, u32 voff, srd_t srd) {
  asm volatile("buffer_load_dword %0, %1, %2, 0 offen" : "+v"(sink) : "v"(voff), "s"(srd) : "memory");
}
DI void ldg_sc1(u32x4& dst, u32 voff, srd_t srd, u32 soff) {
  asm volatile("buffer_load_dwordx4 %0, %1, %2, %3 offen sc1" : "=v"(dst) : "v"(voff), "s"(srd), "s"(soff) : "memory");
}
DI void stg_sc1(u32x4 v, u32 voff, srd_t srd, u32 soff) {
  asm volatile("buffer_store_dwordx4 %0, %1, %2, %3 offen sc1\n\ts_nop 1" ::"v"(v), "v"(voff), "s"(srd), "s"(soff) : "memory");
}
DI void stg64_sc1(u32x2 v, u32 voff, srd_t srd, u32 soff) {
  asm volatile("buffer_store_dwordx2 %0, %1, %2, %3 offen sc1" ::"v"(v), "v"(voff), "s"(srd), "s"(soff) : "memory");
}
DI void stg32_sc1(u32 v, u32 voff, srd_t srd, u32 soff) {
  asm volatile("buffer_store_dword %0, %1, %2, %3 offen sc1" ::"v"(v), "v"(voff), "s"(srd), "s"(soff) : "memory");
}
struct Sync { u32 pa, ra, one; };
template <int C> DI void lds_post(const Sync& y) { asm volatile("ds_add_u32 %0, %1 offset:%2" ::"v"(y.pa), "v"(y.one), "n"(C) : "memory"); }
template <int C> DI u32 lds_peek(const Sync& y) { return *(volatile LAS u32*)(size_t)(y.ra + C); }
template <int C, int T> DI void lds_wait_pre(u32 a, u32 v) {
  u32 cnt;
  asm volatile("v_cmp_gt_u32 vcc, %3, %0\n\ts_cbranch_vccnz .Lsl%=\n.Lse%=:\n\t.subsection 1\n"
               ".Lsl%=:\n\ts_mov_b32 %1, 0x40000\n.Lsp%=:\n\tds_read_b32 %0, %2 offset:%4\n\ts_waitcnt lgkmcnt(0)\n\tv_cmp_gt_u32 vcc, %3, %0\n\ts_cbranch_vccz .Lse%=\n\t"
               "s_sub_u32 %1, %1, 1\n\ts_cbranch_scc0 .Lsp%=\n\ts_branch .Lse%=\n\t.subsection 0"
               : "+v"(v), "=&s"(cnt) : "v"(a), "n"(T), "n"(C) : "vcc", "scc", "memory");
}
template <int C, int T> DI void lds_wait(const Sync& y) { lds_wait_pre<C, T>(y.ra, lds_peek<C>(y)); }
template <int K> DI void z_post(u32 oob, srd_t srd) {
  asm volatile("buffer_load_dword %0, %1, 0 offen offset:%2 lds" ::"v"(oob), "s"(srd), "n"(4 * K + 4) : "memory");
}
DI void set_m0(u32 v) { asm volatile("s_mov_b32 m0, %0\n\ts_nop 0" ::"s"(v) : "memory"); }
template <int K> DI u32 z_peek(u32 za) { return *(volatile LAS u32*)(size_t)(za + 4 * K); }
template <int K> DI void z_wait_pre(u32 za, u32 v) {
  u32 cnt;
  asm volatile("v_cmp_ne_u32 vcc, 0, %0\n\ts_cbranch_vccnz .Lsl%=\n.Lse%=:\n\t.subsection 1\n"
               ".Lsl%=:\n\ts_mov_b32 %1, 0x40000\n.Lsp%=:\n\tds_read_b32 %0, %2 offset:%3\n\ts_waitcnt lgkmcnt(0)\n\tv_cmp_ne_u32 vcc, 0, %0\n\ts_cbranch_vccz .Lse%=\n\t"
               "s_sub_u32 %1, %1, 1\n\ts_cbranch_scc0 .Lsp%=\n\ts_branch .Lse%=\n\t.subsection 0"
               : "+v"(v), "=&s"(cnt) : "v"(za), "n"(4 * K) : "vcc", "scc", "memory");
}
template <int K> DI void z_wait(u32 za) { z_wait_pre<K>(za, z_peek<K>(za)); }
DI void flag_peek(u64& v, srd_t ws, u32 voff) {
  asm volatile("buffer_load_dwordx2 %0, %1, %2, 0 offen sc1" : "=v"(v) : "v"(voff), "s"(ws) : "memory");
}
template <int N> DI void flag_check(u64 v, srd_t ws, u32 voff, u64 tag) {
  u32 cnt;
  asm volatile("s_waitcnt vmcnt(%5)\n\t"
               "v_cmp_ne_u64 vcc, %4, %0\n\ts_cbranch_vccnz .Lfl%=\n.Lfe%=:\n\t.subsection 1\n"
               ".Lfl%=:\n\ts_mov_b32 %1, 0x8000\n"
               ".Lfp%=:\n\tbuffer_load_dwordx2 %0, %2, %3, 0 offen sc1\n\ts_waitcnt vmcnt(0)\n\tv_cmp_ne_u64 vcc, %4, %0\n\ts_cbranch_vccz .Lfe%=\n\t"
               "s_sleep " SLEEPN "\n\ts_sub_u32 %1, %1, 1\n\ts_cbranch_scc0 .Lfp%=\n\ts_branch .Lfe%=\n.Lfz%=:\n\t.subsection 0"
               : "+v"(v), "=&s"(cnt) : "v"(voff), "s"(ws), "s"(tag), "n"(N) : "vcc", "scc", "memory");
}
DI void flag_wait(srd_t ws, u32 voff, u64 tag) { u64 v; flag_peek(v, ws, voff); flag_check<0>(v, ws, voff, tag); }
DI void flag_set(srd_t ws, u32 voff, u64 tag) {
  asm volatile("buffer_store_dwordx2 %0, %1, %2, 0 offen sc1" ::"v"(tag), "v"(voff), "s"(ws) : "memory");
}
DI u32x4 not_ones(u32x4 v) { return u32x4{min(v.x, 0xfffffffeu), min(v.y, 0xfffffffeu), min(v.z, 0xfffffffeu), min(v.w, 0xfffffffeu)}; }
template <int N> DI bool arrived(const u32x4 (&v)[N]) {
  u32 m = 0;
#pragma unroll
  for (int i = 0; i < N; ++i) m = max(max(max(m, v[i].x), v[i].y), max(v[i].z, v[i].w));
  return NOWAIT || __builtin_amdgcn_ballot_w64(m == 0xffffffffu) == 0;
}
template <bool FORCE, int NZ> DI void flag_try(u32& done, srd_t ws, u32 voff, u64 tag) {
  u32 t;
#define FT_TAIL "s_cbranch_scc0 .Lfp%=\n.Lfe%=:\n\t.subsection 1\n.Lfp%=:\n\ts_waitcnt vmcnt(%5)\n\tbuffer_store_dwordx2 %2, %3, %4, 0 offen sc1\n\t" \
                "s_mov_b32 %0, 64\n\ts_branch .Lfe%=\n.Lfz%=:\n\t.subsection 0"
  if (FORCE) asm volatile("s_cmp_lg_u32 %0, 0\n\t" FT_TAIL : "+s"(done), "=&s"(t) : "v"(tag), "v"(voff), "s"(ws), "n"(NZ) : "scc", "memory");
  else asm volatile("s_getreg_b32 %1, hwreg(HW_REG_IB_STS, 0, 4)\n\ts_or_b32 %1, %1, %0\n\ts_cmp_gt_u32 %1, %5\n\t" FT_TAIL
                    : "+s"(done), "=&s"(t) : "v"(tag), "v"(voff), "s"(ws), "n"(NZ) : "scc", "memory");
}
#define SB() __builtin_amdgcn_sched_barrier(0)
#define OPAQUE(x) asm volatile("" : "+v"(x))
DI void interleave4() {
#pragma unroll
  for (int i = 0; i < 4; ++i) { __builtin_amdgcn_sched_group_barrier(0x8, 1, 0); __builtin_amdgcn_sched_group_barrier(0x100, 2, 0); }
}
DI float swap_hi(float lo_wants, int h) {
  auto r = __builtin_amdgcn_permlane32_swap(f2u(lo_wants), f2u(lo_wants), false, false);
  (void)h;
  return u2f(r[1]);
}
DI void both_halves(float v, float& lo, float& hi) {
  auto r = __builtin_amdgcn_permlane32_swap(f2u(v), f2u(v), false, false);
  lo = u2f(r[0]); hi = u2f(r[1]);
}
#define STAMP(i) do {} while (0)
#define STAMP_FLUSH() do {} while (0)
#define STAMP_ARG
#define STAMP_PASS

#define FSTAMP(i) do {} while (0)
constexpr float LOG2E = 1.4426950408889634f;

template <int I> struct IC { static constexpr int v = I; };
template <int N, class F, int I = 0> DI void sfor(F f) { if constexpr (I < N) { f(IC<I>{}); sfor<N, F, I + 1>(f); } }

enum { OX, OW, OU, OK, OZ, OPEEK, OB, OA, OST, OFLAG, OOM };
struct Op { int kind, idx, i; };
enum { M_KV0, M_EPI, M_ST, M_JOB };
struct Tab { Op v[300]; int mark[48]; int n; };
#define AWV 4
constexpr int AW = AWV;
constexpr int zidx(bool H, int kind, int idx) { return kind == OW ? idx : kind == OK ? (H ? 7 : 8) + idx : (!H && idx == 0) ? 7 : 11 + idx; }
constexpr int rpos(int kind, int idx) { return kind == OX ? 2 * idx : kind == OW ? 2 * idx + 1 : kind == OK ? 14 + idx : 17 + idx; }
constexpr u32 slot_of(int kind, int idx) { return kind == OK && idx == 3 ? 9 : rpos(kind, idx) % 9; }
constexpr Tab make_tab(bool H) {
  Tab t{}; int n = 0;
  auto I4 = [&](int kind, int idx, int lo = 0, int hi = 4) {
    for (int i = lo; i < hi; ++i) t.v[n++] = Op{kind, idx, i};
    if (kind != OX && kind <= OK && hi == (kind == OK && idx == 3 ? 2 : 4)) t.v[n++] = Op{OZ, zidx(H, kind, idx), 0};
  };
  for (int s = 0; s < 7; ++s) { if (s == 6) t.v[n++] = Op{OPEEK, 0, 0}; I4(OX, s); I4(OW, s); }
  if (!H) I4(OU, 0);
  I4(OK, 0); if (!H) t.v[n++] = Op{OPEEK, 4, 0}; I4(OK, 1); I4(OK, 2); I4(OK, 3, 0, 2);
  t.mark[M_KV0] = n;
  for (int u = H ? 0 : 4; u < 28; ++u) { if (u == (H ? KVT : 24)) t.mark[M_EPI] = n; I4(OU, u >> 2, u & 3, (u & 3) + 1); }
  if (H) for (int j = 0; j < 14 - NOBEPI; ++j) { if (j == 11) t.v[n++] = Op{OPEEK, 2, 0}; I4(OB, j, 0, 2); }
  t.mark[M_ST] = n; t.v[n++] = Op{OST, 0, 0};
  if (H) {
    for (int j = 14 - NOBEPI; j < 14; ++j) { if (j == 11) t.v[n++] = Op{OPEEK, 2, 0}; I4(OB, j, 0, 2); }
    for (int j = 0; j < AW; ++j) I4(OA, j, 0, 2);
    for (int j = 0; j < 28; ++j) {
      t.mark[M_JOB + j] = n;
      if (j == 12) t.v[n++] = Op{OPEEK, 3, 0};
      if (j < 14 - AW) I4(OA, j + AW, 0, 2);
      if (j < 14) I4(OB, j + 14, 0, 2);
      if (j >= 10 && j < 14) { I4(OB, 28 + 2 * (j - 10), 0, 2); I4(OB, 29 + 2 * (j - 10), 0, 2); }
      if (j == 13) { t.v[n++] = Op{OZ, 18, 0}; for (int k = 14; k < 26; ++k) I4(OA, k, 0, 2); }
      if (j == 14 || j == 15) I4(OA, j + 12, 0, 2);
    }
    t.mark[M_JOB + 28] = n;
  }
  t.n = n;
  return t;
}
template <bool H> struct TB {
  static constexpr Tab t = make_tab(H);
  static constexpr int find(int kind, int idx, int i) { for (int k = 0; k < t.n; ++k) if (t.v[k].kind == kind && t.v[k].idx == idx && t.v[k].i == i) return k; return 1000; }
  static constexpr int etick(int n) {
    int k = t.mark[M_EPI];
    for (int i = 0; i < n && k < t.mark[M_ST]; ++i) { ++k; while (k < t.mark[M_ST] && (t.v[k].kind == OZ || t.v[k - 1].kind == OPEEK)) ++k; }
    return n >= 29 ? t.mark[M_ST] : k;
  }
  static constexpr int need0(int k) {
    if (k >= t.n || t.v[k].kind > OK || t.v[k].i != 0 || slot_of(t.v[k].kind, t.v[k].idx) == 9) return -1;
    const int p = rpos(t.v[k].kind, t.v[k].idx);
    return p < 9 || p > 22 ? -1 : (p - 9) >> 1;
  }
  static constexpr int need(int k) { for (int i = 0; i < k && i < t.n; ++i) if (need0(i) >= need0(k)) return -1; return need0(k); }
};
static_assert(KVT >= 12 && KVT <= 24, "signals of U2 / U6 double as progress marks of the k/v GEMM");
constexpr u32 L_R2 = 0, L_R1 = 4 * 18432;
constexpr int LAG = 12;
constexpr int cmax(int a, int b) { return a > b ? a : b; }

template <bool HG, bool FIRST, class Hook>
DI void unit_gemm(u32x4 (&xf)[28], f32x16 (&acc)[4], int lane, int w, u32 za, Hook hook STAMP_ARG) {
  const u32 h = lane >> 5, mat = (lane >> 4) & 1, q = (lane & 15) >> 2, pp = lane & 3;
  const u32 vb0 = HG ? mat * 8192 + (8 * h + q) * 128 + ((q & 2) | mat) * 32 + 8 * pp : (8 * h + q) * 256 + mat * 128 + q * 32 + 8 * pp;
  constexpr u32 KS = HG ? 2048 : 4096;
  constexpr int KIND = FIRST ? OW : OU;
  const u32 r = lane & 31;
  const u32 xb = w * 4096 + r * 128 + ((h ^ ((r >> 1) & 7)) * 16);
  u32 vb[2][4], xa[2][4], pre = 0;
#pragma unroll
  for (int c = 0; c < 8; ++c) {
    vb[c >> 2][c & 3] = (vb0 ^ ((c & 3) * 32)) + (c < 4 && !FIRST ? 131072 : (c >> 2) * 65536); OPAQUE(vb[c >> 2][c & 3]);
    if (FIRST) { xa[c >> 2][c & 3] = (xb ^ ((c & 3) * 32)) + (c >> 2) * 81920; OPAQUE(xa[c >> 2][c & 3]); }
  }
  u32x4 fr[4];
  auto rdw = [&](int s, int n) {
    const int slot = slot_of(KIND, s);
    const u32 a = vb[slot >> 2 & 1][n & 3] + (slot & 3) * 16384 + (n >> 2) * KS;
    fr[n & 3] = trfrag(a, a + KS / 4);
  };
  auto rdx = [&](int s, int kk) { const int slot = slot_of(OX, s); xf[4 * s + kk] = lds128(slot < 5 ? xa[0][kk] + slot * 16384 : xa[1][kk] + (slot - 5) * 16384); };
#pragma unroll
  for (int c = 0; c < 4; ++c)
#pragma unroll
    for (int e = 0; e < 16; ++e) acc[c][e] = 0.f;
  z_wait<zidx(HG, KIND, 0)>(za);
  STAMP(FIRST ? 16 : 32);
  if (FIRST) { rdx(0, 0); rdx(0, 1); rdx(0, 2); }
#pragma unroll
  for (int n = 0; n < 4; ++n) rdw(0, n);
  SB();
  sfor<112>([&](auto gc) {
    constexpr int G = decltype(gc)::v, s = G >> 4, n = G & 15;
    acc[n & 3] = mfma(fr[n & 3], xf[4 * s + (n >> 2)], acc[n & 3]);
    SB();
    asm volatile("" : "+v"(acc[n & 3]));
    if constexpr (n == 12 && s < 6) z_wait_pre<zidx(HG, KIND, s + 1)>(za, pre);
    if constexpr (n == 12 && s < 6 && FIRST) rdx(s + 1, 0);
    if constexpr (!FIRST && s == 0 && n >= 12) { vb[0][n & 3] -= 131072; OPAQUE(vb[0][n & 3]); }
    if constexpr (n < 12) rdw(s, n + 4); else if constexpr (s < 6) rdw(s + 1, n - 12);
    if constexpr (FIRST) {
      if constexpr (n == 0) rdx(s, 3);
      if constexpr (n >= 13 && n < 15 && s < 6) rdx(s + 1, n - 12);
    }
    if constexpr (n == 8 && s < 6) pre = z_peek<zidx(HG, KIND, s + 1)>(za);
    hook(gc);
    SB();
  });
}

DI void load_wphi(u32x4 (&wp)[2], u32 base, int lane) {
  const u32 h = lane >> 5, fb = (lane >> 4) & 1, q = (lane & 15) >> 2, pp = lane & 3;
  u32 col = 16 * fb + 4 * pp; if (col == 28) col = 24;
  const u32 a = base + (4 * h + q) * 56 + col * 2;
  wp[0] = trfrag(a, a + 8 * 56);
  u32x2 lo = tr64(a + 16 * 56), hi = tr64(base + (24 + q) * 56 + col * 2);
  wp[1] = u32x4{lo.x, lo.y, h ? 0u : hi.x, h ? 0u : hi.y};
}
DI u32x4 pack8(const f32x16& v, int s) {
  return u32x4{cvtpk(v[8 * s], v[8 * s + 1]), cvtpk(v[8 * s + 2], v[8 * s + 3]), cvtpk(v[8 * s + 4], v[8 * s + 5]), cvtpk(v[8 * s + 6], v[8 * s + 7])};
}
DI float sigmoid(float t) { return __builtin_amdgcn_rcpf(1.f + __builtin_amdgcn_exp2f(-LOG2E * t)); }
DI float max3(float a, float b, float c) { return __builtin_fmaxf(__builtin_fmaxf(a, b), c); }
DI float min3(float a, float b, float c) { return __builtin_fminf(__builtin_fminf(a, b), c); }
DI void feature_exp(const u32x4 (&t)[2], const u32x4 (&wp)[2], f32x16& ep, f32x16& en) {
  f32x16 pz;
#pragma unroll
  for (int e = 0; e < 16; ++e) pz[e] = 0.f;
  pz = mfma(wp[0], t[0], pz);
  pz = mfma(wp[1], t[1], pz);
  float mx = max3(max3(max3(pz[0], pz[1], pz[2]), max3(pz[3], pz[4], pz[5]), max3(pz[6], pz[7], pz[8])),
                  max3(max3(pz[9], pz[10], pz[11]), max3(pz[12], pz[13], pz[14]), pz[15]), pz[15]);
  float mn = min3(min3(min3(pz[0], pz[1], pz[2]), min3(pz[3], pz[4], pz[5]), min3(pz[6], pz[7], pz[8])),
                  min3(min3(pz[9], pz[10], pz[11]), min3(pz[12], pz[13], pz[14]), pz[15]), pz[15]);
  float a, b;
  both_halves(mx, a, b); mx = __builtin_fmaxf(a, b) * LOG2E;
  both_halves(mn, a, b); mn = __builtin_fminf(a, b) * LOG2E;
#pragma unroll
  for (int e = 0; e < 16; ++e) {
    ep[e] = __builtin_amdgcn_exp2f(__builtin_fmaf(pz[e], LOG2E, -mx));
    en[e] = __builtin_amdgcn_exp2f(__builtin_fmaf(pz[e], -LOG2E, mn));
  }
}
template <int NS, class Epi>
DI void task_o(int head, int rt32, int w, srd_t ws, u64 tag, int lane, const Sync& y, u32 fdone, Epi epi STAMP_ARG) {
  const int h = lane >> 5;
  const u32 l16 = lane * 16, fqa = WS_FQ + (rt32 * 4 + (head >> 2)) * 128;
  u32x4 qf[2], wp[2], kf[4];
  auto ldq = [&](u32 vo) {
    ldg_sc1o<0>(qf[0], vo, ws, WS_Q + (rt32 * 32 + head * 2) * 1024);
    ldg_sc1o<1024>(qf[1], vo, ws, WS_Q + (rt32 * 32 + head * 2) * 1024);
  };
  auto ldk = [&]() { sfor<4>([&](auto ic) { constexpr int i = decltype(ic)::v; ldg_sc1o<i * 1024>(kf[i], l16, ws, WS_KVF + head * 4096); }); };
  u64 fq = 0;
  bool early = false;
  epi([&](int c) {
    if (c == 0) flag_peek(fq, ws, fqa);
    if (c == 2) {
      asm volatile("s_waitcnt vmcnt(%1)" : "+v"(fq) : "n"(2 * NS) : "memory");
      early = __builtin_amdgcn_ballot_w64(fq != tag) == 0;
      ldq(early ? l16 : OOB);
    }
  });
  STAMP(5);
  u32 done = 0;
  asm volatile("s_waitcnt vmcnt(%2)" : "+v"(qf[0]), "+v"(qf[1]) : "n"(NS) : "memory");
  if (!early) {
    flag_try<true, 0>(done, ws, fdone, tag);
    flag_wait(ws, fqa, tag);
    ldq(l16);
    asm volatile("s_waitcnt vmcnt(0)" : "+v"(qf[0]), "+v"(qf[1])::"memory");
  }
  ldk();
  load_wphi(wp, L_WPHI, lane);
  lds_wait<C_SYNC, 1>(y);
  const float logit = *(LAS float*)(size_t)(L_GW + 256 + (w * 32 + (lane & 31)) * 4);
  STAMP(6);
  float gate = sigmoid(logit);
  f32x16 ep, en; feature_exp(qf, wp, ep, en);
  u32x4 ef[2][2] = {{pack8(ep, 0), pack8(ep, 1)}, {pack8(en, 0), pack8(en, 1)}};
  asm volatile("" : "+v"(ef[0][0]), "+v"(ef[0][1]), "+v"(ef[1][0]), "+v"(ef[1][1]), "+v"(gate));
  asm volatile("s_waitcnt vmcnt(0)" : "+v"(kf[0]), "+v"(kf[1]), "+v"(kf[2]), "+v"(kf[3])::"memory");
  flag_try<true, 0>(done, ws, fdone, tag);
  STAMP(7);
  for (int it = 0; it < 20000 && !arrived(kf); ++it) {
    for (; it < 20000; ++it) {
      u32 v;
      asm volatile("buffer_load_dword %0, %1, %2, %3 offen sc1\n\ts_waitcnt vmcnt(0)" : "=v"(v) : "v"((lane >> 3) * 512), "s"(ws), "s"(WS_KVF + head * 4096) : "memory");
      if (__builtin_amdgcn_ballot_w64(v == 0xffffffffu) == 0) break;
    }
    ldk();
    asm volatile("s_waitcnt vmcnt(0)" : "+v"(kf[0]), "+v"(kf[1]), "+v"(kf[2]), "+v"(kf[3])::"memory");
  }
  STAMP(8);
  f32x16 np, nn;
#pragma unroll
  for (int e = 0; e < 16; ++e) { np[e] = 0.f; nn[e] = 0.f; }
  np = mfma(kf[0], ef[0][0], np); nn = mfma(kf[2], ef[1][0], nn);
  np = mfma(kf[1], ef[0][1], np); nn = mfma(kf[3], ef[1][1], nn);
  const float rp = __builtin_amdgcn_rcpf(np[13]), rn = __builtin_amdgcn_rcpf(nn[13]);
  const float inv = __builtin_amdgcn_rcpf(np[12] * rp + nn[12] * rn + 1e-6f) * gate;
  const float cp = swap_hi(rp * inv, h), cn = swap_hi(rn * inv, h);
  f32x16 o;
#pragma unroll
  for (int e = 0; e < 16; ++e) o[e] = np[e] * cp + nn[e] * cn;
  u32x4 o0 = pack8(o, 0), o1 = pack8(o, 1);
  o1.z = h ? 0u : o1.z; o1.w = h ? 0u : o1.w;
  stg_sc1(o0, l16, ws, WS_O + (rt32 * 32 + head * 2) * 1024);
  stg_sc1(o1, l16, ws, WS_O + (rt32 * 32 + head * 2 + 1) * 1024);
  stg32_sc1(f2u(1.f - gate), ((head & 3) | h) ? OOB : lane * 4, ws, WS_G + ((head >> 2) * 2048 + rt32 * 32) * 4);
  VMWAIT(0);
  flag_set(ws, lane == 0 ? WS_FO + (rt32 * 16 + head) * 128 : OOB, tag);
}
DI void dump_tile(const f32x16 (&acc)[2][2], u32 base, int lane) {
  const u32 a = base + (lane & 31) * 272 + (lane >> 5) * 16;
#pragma unroll
  for (int c = 0; c < 2; ++c)
#pragma unroll
    for (int t = 0; t < 2; ++t)
#pragma unroll
      for (int g4 = 0; g4 < 4; ++g4)
        sts128(a + t * 32 * 272 + (c * 32 + g4 * 8) * 4,
               __builtin_bit_cast(u32x4, f32x4{acc[c][t][4 * g4], acc[c][t][4 * g4 + 1], acc[c][t][4 * g4 + 2], acc[c][t][4 * g4 + 3]}));
  asm volatile("s_waitcnt lgkmcnt(0)\n\ts_barrier" ::: "memory");
}
template <class Hook> DI void store_q(const f32x16 (&acc)[4], srd_t ws, u32 soff, int lane, Hook hook) {
#pragma unroll
  for (int c = 0; c < 8; ++c) { stg_sc1(pack8(acc[c >> 1], c & 1), lane * 16, ws, soff + c * 1024); if (c & 1) hook(c >> 1); }
}
DI u32 brow_off(int lane) { const u32 row = lane >> 3; return row * 896 + ((lane & 7) ^ (((row >> 1) & 1) * 4)) * 16; }
struct BPath { u32 wa, ra[2]; };
DI BPath make_bpath(u32 base, int lane) {
  const u32 h = lane >> 5, cb = (lane >> 4) & 1, q = (lane & 15) >> 2, pp = lane & 3;
  BPath t;
  t.wa = base + lane * 16; OPAQUE(t.wa);
#pragma unroll
  for (int c = 0; c < 2; ++c) { t.ra[c] = base + (4 * h + q) * 128 + ((c ^ ((q >> 1) & 1)) * 64) + cb * 32 + pp * 8; OPAQUE(t.ra[c]); }
  return t;
}

template <int PE, class Hook> DI void epi_h(const f32x16 (&acc)[4], srd_t ws, u32 soff, int lane, Hook hook) {
#pragma unroll
  for (int c = 0; c < 4; ++c) {
    float hv[8];
#pragma unroll
    for (int e = 0; e < 8; ++e) {
      const float x = acc[c][e];
      const float t = __builtin_amdgcn_exp2f(x * __builtin_fmaf(x * x, -0.10294323970f, -2.30220819814f));
      hv[e] = x * __builtin_amdgcn_rcpf(1.f + t) * acc[c][e + 8];
    }
    u32x4 hp = u32x4{cvtpk(hv[0], hv[1]), cvtpk(hv[2], hv[3]), cvtpk(hv[4], hv[5]), cvtpk(hv[6], hv[7])};
    if (PE != 3) { asm volatile("s_setprio 3" : "+v"(hp)); }
    stg_sc1(hp, lane * 16, ws, soff + c * 1024);
    if (PE != 3) asm volatile("s_setprio %0" ::"n"(PE));
    hook(c);
  }
}

template <bool P1, bool isH> DI void body(const Params& p, const int wave) {
  const int tid = threadIdx.x, lane = tid & 63;
  const int w = wave & 3;
  const int b = blockIdx.x, xcd = b & 7, bi = b >> 3;
  const int m = 8 * (xcd >> 2) + (bi & 7), sl = (xcd & 1) * 4 + (bi >> 3), half = (xcd >> 1) & 1;
  const int tl = half * 56 + (xcd & 1) * 32 + bi, rt64 = 16 * (xcd >> 2) + tl / 7, ct2 = tl % 7;
  const int j = 7 * half + sl;
  const int kvhead = 8 * half + sl;
  const int h = lane >> 5;
  const u32 r = lane & 31, l4 = (lane & 3) * 4, l16 = lane * 16;
  const u32 cb = (lane >> 4) & 1, q = (lane & 15) >> 2, pp = lane & 3;
  const srd_t ws = mksrd(p.ws, WS_END), sqkv = mksrd(p.wqkv, D * 3 * D * 2);
  const int rt32 = m * 4 + w;
  STAMP(0);

  if (P1) __builtin_amdgcn_s_setprio(PRIO0);
  const u32 zm = L_Z + wave * 512;
  u32 z1 = L_Z + (lane & 3) * 512 + 256, z2 = z1 + 2048, oob = OOB;
  OPAQUE(z1); OPAQUE(z2); OPAQUE(oob);
  Sync y{lane == 0 ? L_CNT + w * 4 : L_DUM + lane * 4, L_CNT + l4, 1u};
  OPAQUE(y.pa); OPAQUE(y.ra); OPAQUE(y.one);
  u32x2 ev;
  asm volatile("buffer_load_dwordx2 %0, off, %1, 0 sc1" : "=v"(ev) : "s"(ws) : "memory");

  if (P1) {
    u32 fvo, sink = 0, tvo2;
    const srd_t st2 = mksrd(w == 0 ? p.wg : w == 1 ? p.wu : w == 2 ? p.wd : p.wo, w < 3 ? D * DF * 2 : D * D * 2);
    {
      const u32 scr = L_K3 + w * 1024;
      const int km = rt32;
      dma_buf(scr, km < 56 ? (lane >> 2) * 2688 + (lane & 3) * 16 : OOB, sqkv, (km >> 1) * 16 * 2688 + (1 + (km & 1)) * 896 + kvhead * 56);
      u32x4 gwf;
      ldg(gwf, r == 30 ? h * 16 : OOB, mksrd(p.gw, D * 2), (km >> 1) * 32);
      {
        const u32 row = (xcd >> 2) * 224 + bi * 7 + (lane >> 2), c4 = lane & 3;
        const u32 wv = (lane < 28 && !((xcd & 1) && c4 == 3)) ? row * 3584 + (half * 7 + (xcd & 1) * 4 + c4) * 128 : OOB;
        const u32 tvo1 = w < 2 ? ((PFM & 1) ? wv : OOB) : w == 2 ? ((PFM & 4) && lane < 56 ? ((xcd * 32 + bi) * 56 + lane) * 128 : OOB)
                               : ((PFM & 8) && (xcd & 1) && lane < 28 ? row * 2688 + (half * 3 + c4) * 128 : OOB);
        touch(sink, tvo1, w < 2 ? st2 : w == 2 ? mksrd(p.x, S * D * 2) : sqkv);
        tvo2 = w < 2 ? wv + 14 * 128 : w == 2 ? (lane < 49 ? (b * 49 + lane) * 128 : OOB) : (lane < 13 ? (b * 13 + lane) * 128 : OOB);
        if (T2S < 0) touch(sink, tvo2, st2);
      }
      const u32x4 ones = {~0u, ~0u, ~0u, ~0u};
      stg_sc1(ones, l16, ws, WS_KVP + ((kvhead * 16 + m) * 4 + w) * 1024);
      __builtin_amdgcn_s_setprio(3);
      asm volatile("s_waitcnt vmcnt(%2)" : "+v"(ev), "+v"(gwf) : "n"(T2S < 0 ? 3 : 2) : "memory");
      u32x4 kt;
      if (w & 1) {
        const u32 a = scr + (8 * h + q) * 64 + cb * 32 + 8 * pp;
        kt = trfrag(a, a + 256);
        if (r >= 28) kt = gwf;
      } else {
        z_wait<0>(z2);
        u32x4 wp[2]; load_wphi(wp, L_WPHI, lane);
        const u32 a = scr + ((lane & 3) + ((lane >> 1) & 4) + ((lane << 1) & 8)) * 64 + h * 8;
        f32x16 t;
#pragma unroll
        for (int e = 0; e < 16; ++e) t[e] = 0.f;
#pragma unroll
        for (int s = 0; s < 2; ++s) {
          const u32x2 lo = *(LAS u32x2*)(size_t)(a + s * 32), hi = *(LAS u32x2*)(size_t)(a + s * 32 + 16);
          t = mfma(u32x4{lo.x, lo.y, hi.x, hi.y}, wp[s], t);
        }
#pragma unroll
        for (int e = 0; e < 8; ++e) t[e] *= LOG2E;
        kt = pack8(t, 0);
      }
      if (m == 15) kt = ones;
      stg_sc1(kt, m != 14 ? l16 : OOB, ws, m == 15 ? WS_KVF + (kvhead * 4 + w) * 1024 : WS_KT + (kvhead * 56 + km) * 1024);
      fvo = lane == 0 ? WS_FK + (kvhead * 64 + km) * 8 : OOB;
    }
    const u64 tag = (((u64)rfl(ev.y) << 32) | rfl(ev.x)) + 1;
    STAMP(1);
    u32x4 xf[28];
    f32x16 acc[4];
    u32 done = 0;
    set_m0(zm);
    unit_gemm<isH, true>(xf, acc, lane, w, z2, [&](auto gc) {
      constexpr int s = decltype(gc)::v >> 4, n = decltype(gc)::v & 15;
      if constexpr (n == 15) z_post<s>(oob, ws);
      if constexpr (s < 3 && (n == 3 || n == 9)) flag_try<s == 2 && n == 9, s>(done, ws, fvo, tag);
      if constexpr (s == T2S && n == 11) touch(sink, tvo2, st2);
    } STAMP_PASS);
    STAMP(2);
    __builtin_amdgcn_s_setprio(isH ? PRIOE : 3);
    if (isH) epi_h<PRIOE>(acc, ws, WS_H + (rt32 * 112 + j * 4) * 1024, lane, [](int) {});
    u32x4 qa[8];
    if (!isH) {
#pragma unroll
      for (int c = 0; c < 8; ++c) { qa[c] = pack8(acc[c >> 1], c & 1); OPAQUE(qa[c]); }
    }
    __builtin_amdgcn_s_setprio(isH ? PRIO1 : PRIOQ);
    STAMP(3);
    done = 0;
    fvo = lane != 0 ? OOB : isH ? WS_FH + (rt32 * 16 + j) * 128 : WS_FQ + (rt32 * 4 + half * 2) * 128;
    if (isH && PAUSE) {
      flag_try<true, 0>(done, ws, fvo, tag);
      for (int it = 0; it < 4000; ++it) {
        if (rfl(*(volatile LAS u32*)(size_t)(L_CNT + C_SYNC + w * 4)) >= PAUSE) break;
        __builtin_amdgcn_s_sleep(1);
      }
    }
    unit_gemm<isH, false>(xf, acc, lane, w, z2, [&](auto gc) {
      constexpr int s = decltype(gc)::v >> 4, n = decltype(gc)::v & 15;
      if constexpr (isH && s < 4 && (n == 3 || n == 9)) flag_try<s == 3 && n == 9, 0>(done, ws, fvo, tag);
      if constexpr (!isH && (s == QAS || s == QAS + 1) && (n & 3) == 1) stg_sc1(qa[(s - QAS) * 4 + (n >> 2)], l16, ws, WS_Q + (rt32 * 32 + half * 16 + (s - QAS) * 4 + (n >> 2)) * 1024);
      if constexpr (!isH && s > QAS + 1 && (n == 3 || n == 9)) flag_try<s == 6 && n == 9, 0>(done, ws, fvo, tag);
    } STAMP_PASS);
    OPAQUE(sink);
    lds_post<C_PROG>(y);
    STAMP(4);
    __builtin_amdgcn_s_setprio(3);
    u32x4 bo[8][2];
    task_o<isH ? 1 : 2>(kvhead, rt32, w, ws, tag, lane, y, fvo + (isH ? 64 * 16 * 128 : 128), [&](auto hook) {
      if (isH) epi_h<3>(acc, ws, WS_H + (rt32 * 112 + (14 + j) * 4) * 1024, lane, hook);
      else store_q(acc, ws, WS_Q + (rt32 * 32 + half * 16 + 8) * 1024, lane, hook);
    } STAMP_PASS);
    STAMP(10);
    if (!isH) STAMP_FLUSH();
    if (isH) {
      u64 fo;
      const u32 foa = WS_FO + ((rt64 * 2 + h) * 16 + w * 4 + ((lane >> 3) & 3)) * 128;
      flag_peek(fo, ws, foa);
      z_wait<18>(L_Z + 2048 + w * 512 + 256);
      {
        const BPath bp = make_bpath(L_R1 + w * 17408, lane);
#pragma unroll
        for (int i = 0; i < 16; ++i) bo[i >> 1][i & 1] = trfrag(bp.ra[i & 1] + (i >> 1) * 2048, bp.ra[i & 1] + (i >> 1) * 2048 + 1024);
      }
      flag_check<0>(fo, ws, foa, tag);
      STAMP(14);
      u32x4 ao[8][2];
      u32 gu;
      asm volatile("buffer_load_dword %0, %1, %2, %3 offen sc1" : "=v"(gu) : "v"(lane * 4), "s"(ws), "s"(WS_G + w * 8192 + rt64 * 256) : "memory");
      const u32 l16o = l16 + 32 * 1024;
      sfor<16>([&](auto ic) {
        constexpr int i = decltype(ic)::v;
        ldg_sc1o<((i >> 1) & 3) * 1024>(ao[i >> 1][i & 1], (i & 1) ? l16o : l16, ws, WS_O + (rt64 * 2 * 32 + w * 8) * 1024 + (i >> 3) * 4096);
      });
      asm volatile("s_waitcnt vmcnt(16)" : "+v"(gu)::"memory");
      *(LAS u32*)(size_t)(L_GW + lane * 4) = gu;
      f32x16 oc[2][2];
#pragma unroll
      for (int i = 0; i < 4; ++i)
#pragma unroll
        for (int e = 0; e < 16; ++e) oc[i >> 1][i & 1][e] = 0.f;
      sfor<8>([&](auto nc) {
        constexpr int n = decltype(nc)::v;
        asm volatile("s_waitcnt vmcnt(%2)" : "+v"(ao[n][0]), "+v"(ao[n][1]) : "n"(14 - 2 * n) : "memory");
#pragma unroll
        for (int u = 0; u < 4; ++u) oc[u >> 1][u & 1] = mfma(bo[n][u >> 1], ao[n][u & 1], oc[u >> 1][u & 1]);
        SB();
        asm volatile("" : "+v"(oc[0][0]), "+v"(oc[0][1]), "+v"(oc[1][0]), "+v"(oc[1][1]));
      });
      STAMP(15);
      dump_tile(oc, L_R1 + w * 17408, lane);
      flag_set(ws, (b | w | lane) ? OOB : WS_E, tag);
    }
  } else {
    using T = TB<isH>;
    const srd_t sx = mksrd(p.x, S * D * 2);
    {
      const srd_t sp = mksrd(p.wphi, 16 * 1568);
      if (w < 2) dma_buf(L_WPHI + w * 1024, l16, sp, kvhead * 1568 + w * 1024);
    }
    constexpr u32 wrs = isH ? 3584 : 2688, wu2 = isH ? 14 * 128 : 4 * 56;
    u32 xvo[4], wvo[4];
#pragma unroll
    for (int i = 0; i < 4; ++i) {
      xvo[i] = (lane >> 3) * 896 + (((lane & 7) ^ ((4 * i + (lane >> 4)) & 7)) * 16) + i * (8 * 896 - 1024);
      if (isH) wvo[i] = (lane >> 3) * 3584 + ((((lane >> 1) & 3) ^ (((lane >> 3) & 2) | (w >> 1))) * 32) + (lane & 1) * 16 + i * (8 * 3584 - 1024);
      else     wvo[i] = (lane >> 4) * 2688 + ((((lane >> 1) & 3) ^ (lane >> 4)) * 56) + ((lane >> 3) & 1) * 32 + (lane & 1) * 16 + i * (4 * 2688 - 1024);
    }
    const srd_t sw = isH ? mksrd(w < 2 ? p.wg : p.wu, D * DF * 2) : sqkv;
    const u32 xso = (m * 128 + w * 32) * 896;
    const u32 wso1 = isH ? (w & 1) * 32 * 3584 + j * 128 : w * 16 * 2688 + 8 * half * 56;
    const u32 mq = w * 4096, mq2 = w * 2048;
    const u32 kso4 = WS_KT + kvhead * 56 * 1024 + mq, kso2 = WS_KT + kvhead * 56 * 1024 + mq2;
    const u32 l16h = l16 + 112 * 1024;
    const u32 hso = WS_H + (rt64 * 2 * 112 + w * 14) * 1024, bso = w * 14 * 16 * 896 + ct2 * 128;
    const srd_t swd = mksrd(p.wd, D * DF * 2);
    const u32 bvo = brow_off(lane);
    auto fadr = [&](int k) -> u32 {
      return k == 0 ? WS_FK + (kvhead * 64 + lane) * 8 : k == 4 ? WS_FK + (m * 64 + lane) * 8 : WS_FH + (((k - 2) * 64 + rt64 * 2 + h) * 16 + ((w * 14) >> 2) + ((lane >> 3) & 3)) * 128;
    };
    u32 so = 0, pre = 0, fa = 0;
    u64 fv = 0, tag = 0;
    u32x4 xf[28], af[28][2], bf[19][2], kvs;
    const srd_t swo = mksrd(p.wo, D * D * 2);
    const u32 bvo1 = bvo + 8 * 896 - 1024, bvop = lane < 32 ? bvo1 : OOB;
    const u32 xb = w * 4096 + r * 128 + ((h ^ ((r >> 1) & 7)) * 16);
    auto op = [&](auto kc) {
      constexpr int K = decltype(kc)::v;
      constexpr Op o = T::t.v[K];
      if constexpr (T::need(K) >= 0) z_wait_pre<T::need(K)>(z1, pre);
      if constexpr (o.kind == OU && o.idx == 6 && o.i == 0) z_wait<zidx(isH, OU, 2)>(z2);
      constexpr int chk = o.i != 0 ? -1 : (o.kind == OK && o.idx == 0) ? 0 : (o.kind == OA && o.idx == 0) ? 2
                        : (o.kind == OA && o.idx == 14) ? 3 : -1;
      if constexpr (chk >= 0) { flag_check<K - 1 - T::find(OPEEK, chk, 0)>(fv, ws, fa, tag); STAMP(11 + chk); }
      if constexpr (o.kind <= OK) {
        constexpr u32 sb = slot_of(o.kind, o.idx) * 16384;
        if constexpr (o.i == 0) {
          if constexpr (o.kind == OX) so = dma_open<sb, o.idx * 128>(mq, xso);
          else if constexpr (o.kind == OK) so = dma_open<sb, o.idx * 16384>(o.idx < 3 ? mq : mq2, o.idx < 3 ? kso4 : kso2);
          else so = dma_open<sb, o.idx * 64 * wrs + (o.kind == OW ? 0u : wu2)>(mq, wso1);
        }
        if constexpr (o.kind == OK) dma_off_sc1<o.i * 1024>(l16, ws, so);
        else dma_off<o.i * 1024>(o.kind == OX ? xvo[o.i] : wvo[o.i], o.kind == OX ? sx : sw, so);
      } else if constexpr (o.kind == OZ) { set_m0(zm); z_post<o.idx>(oob, ws); }
      else if constexpr (o.kind == OPEEK) { fa = fadr(o.idx); flag_peek(fv, ws, fa); }
      else if constexpr (o.kind == OB) {
        if constexpr (o.idx < 19) ldg(bf[o.idx][o.i], bvo, swd, bso + ((o.idx >= 14 ? o.idx + 42 : o.idx) * 16 + o.i * 8) * 896);
        else {
          if constexpr (o.idx == 19 && o.i == 0) lds_wait<C_PROG, 1>(y);
          if constexpr (o.idx == 28 && o.i == 0) lds_wait<C_SYNC, 2>(y);
          if constexpr (o.i == 0) {
            if constexpr (o.idx < 28) so = dma_open<L_R2 + (o.idx - 19) * 2048, (o.idx + 42) * 16 * 896>(w * 18432, bso);
            else so = dma_open<L_R1 + (o.idx - 28) * 2048, (((o.idx - 28) >> 1) * 28 + (o.idx & 1) * 16) * 896>(w * 17408, w * 112 * 896 + ct2 * 128);
          }
          dma_off<o.i * 1024>(o.i == 0 ? bvo : (o.idx >= 28 && (o.idx & 1)) ? bvop : bvo1, o.idx < 28 ? swd : swo, so);
        }
      }
      else if constexpr (o.kind == OA) {
        constexpr int ks = o.idx >= 14 ? o.idx + 42 : o.idx;
        ldg_sc1o<(ks & 3) * 1024>(af[o.idx][o.i], o.i ? l16h : l16, ws, hso + (ks >> 2) * 4096);
      } else if constexpr (o.kind == OST) stg_sc1(not_ones(kvs), l16, ws, WS_KVP + ((kvhead * 16 + m) * 4 + w) * 1024);
      if constexpr (T::need(K + 3) >= 0) pre = z_peek<T::need(K + 3)>(z1);
      constexpr int J = K - LAG;
      if constexpr (J >= 0) {
        constexpr Op oj = T::t.v[J];
        if constexpr (oj.kind == OX && oj.i == 3) {
          if constexpr (J == 3) {
            asm volatile("s_waitcnt vmcnt(%1)" : "+v"(ev) : "n"(LAG) : "memory");
            tag = (((u64)rfl(ev.y) << 32) | rfl(ev.x)) + 1;
          } else VMWAIT(LAG);
#pragma unroll
          for (int kk = 0; kk < 4; ++kk) xf[4 * oj.idx + kk] = lds128(slot_of(OX, oj.idx) * 16384 + (xb ^ (kk * 32)));
          FSTAMP(16 + oj.idx);
        }
      }
      SB();
    };
    auto ops = [&](auto ac, auto bc) { sfor<decltype(bc)::v - decltype(ac)::v>([&](auto kc) { op(IC<decltype(ac)::v + decltype(kc)::v>{}); }); };
#define OPS(a, b) ops(IC<(a)>{}, IC<(b)>{})
#define MK(m) (T::t.mark[m])
#define TICK(M, n, N) OPS(MK(M) + (MK((M) + 1) - MK(M)) * (n) / (N), MK(M) + (MK((M) + 1) - MK(M)) * ((n) + 1) / (N))
#define ETICK(n) OPS(T::etick(n), T::etick((n) + 1))
    f32x16 ka, va;
#pragma unroll
    for (int e = 0; e < 16; ++e) { ka[e] = 0.f; va[e] = r == 28 ? 1.f : 0.f; }
    __builtin_amdgcn_s_setprio(PRIO2);
    OPS(0, MK(M_KV0));
    STAMP(1);
    {
      u32 ak[4];
#pragma unroll
      for (int t = 0; t < 4; ++t) { ak[t] = slot_of(OK, t) * 16384 + l16; OPAQUE(ak[t]); }
      u32x4 fk[3], fw[3];
      u32 pk = 0;
      auto reads = [&](int ks) { fk[ks % 3] = lds128(ak[ks >> 3] + (ks & 7) * 2048); fw[ks % 3] = lds128(ak[ks >> 3] + (ks & 7) * 2048 + 1024); };
      z_wait<zidx(isH, OK, 0)>(z2);
      reads(0); reads(1); reads(2);
      SB();
      sfor<28>([&](auto kc) {
        constexpr int ks = decltype(kc)::v, t = ks >> 3;
        ka = mfma(fk[ks % 3], xf[ks], ka);
        SB();
        asm volatile("" : "+v"(ka));
        TICK(M_KV0, ks, 28);
        va = mfma(xf[ks], fw[ks % 3], va);
        SB();
        asm volatile("" : "+v"(va));
        if constexpr ((ks & 7) == 3 && t < 3) pk = z_peek<zidx(isH, OK, t + 1)>(z2);
        if constexpr ((ks & 7) == 5) z_wait_pre<zidx(isH, OK, t + 1)>(z2, pk);
        if constexpr (ks + 3 < 28) reads(ks + 3);
        SB();
      });
    }
    STAMP(3);
    {
      ETICK(0);
      u32x4 vf[2] = {pack8(va, 0), pack8(va, 1)};
      asm volatile("" : "+v"(vf[0]), "+v"(vf[1]));
      ETICK(1);
      float mx = max3(max3(max3(ka[0], ka[1], ka[2]), max3(ka[3], ka[4], ka[5]), max3(ka[6], ka[7], ka[8])),
                      max3(max3(ka[9], ka[10], ka[11]), max3(ka[12], ka[13], ka[14]), ka[15]), ka[15]);
      asm volatile("" : "+v"(mx));
      ETICK(2);
      float mn = min3(min3(min3(ka[0], ka[1], ka[2]), min3(ka[3], ka[4], ka[5]), min3(ka[6], ka[7], ka[8])),
                      min3(min3(ka[9], ka[10], ka[11]), min3(ka[12], ka[13], ka[14]), ka[15]), ka[15]);
      asm volatile("" : "+v"(mn));
      ETICK(3);
      { float a, c; both_halves(mx, a, c); mx = max3(a, c, c); both_halves(mn, a, c); mn = min3(a, c, c); }
      asm volatile("" : "+v"(mx), "+v"(mn));
      ETICK(4);
      constexpr u32 L_PHI = slot_of(OK, 1) * 16384, L_VF = slot_of(OK, 2) * 16384;
      const u32 wa = L_PHI + w * 4096 + r * 64, sw8 = (h ^ ((r >> 1) & 7)) * 8;
      const float keep = h ? 0.f : 1.f;
      u32x2 ph[2][4];
      sfor<2>([&](auto sc) {
        constexpr int sg = decltype(sc)::v, E0 = 5 + 9 * sg;
        f32x16 e;
        float ps[4];
        sfor<4>([&](auto gc) {
          constexpr int g4 = decltype(gc)::v;
#pragma unroll
          for (int i = 4 * g4; i < 4 * g4 + 4; ++i) e[i] = __builtin_amdgcn_exp2f(sg == 0 ? ka[i] - mx : mn - ka[i]);
          ps[g4] = (e[4 * g4] + e[4 * g4 + 1]) + (e[4 * g4 + 2] + e[4 * g4 + 3]);
          asm volatile("" : "+v"(ps[g4]));
          ETICK(E0 + g4);
        });
        float a, c; both_halves(__builtin_fmaf(ps[3], keep, ps[2]) + (ps[0] + ps[1]), a, c);
        float rs = __builtin_amdgcn_rcpf(a + c);
        asm volatile("" : "+v"(rs));
        ETICK(E0 + 4);
        if constexpr (sg == 1) pre = z_peek<zidx(isH, OU, 6)>(z2);
        sfor<4>([&](auto gc) {
          constexpr int g4 = decltype(gc)::v;
          ph[sg][g4] = u32x2{cvtpk(e[4 * g4] * rs, e[4 * g4 + 1] * rs), cvtpk(e[4 * g4 + 2] * rs, e[4 * g4 + 3] * rs)};
          asm volatile("" : "+v"(ph[sg][g4]));
          ETICK(E0 + 5 + g4);
        });
      });
      z_wait_pre<zidx(isH, OU, 6)>(z2, pre);
      sts128(L_VF + w * 2048 + l16, vf[0]); sts128(L_VF + w * 2048 + 1024 + l16, vf[1]);
      {
        const u32 ga = r == 30 ? L_GW + 256 + w * 128 + h * 16 : L_VF + 8192 + l16;
#pragma unroll
        for (int g4 = 0; g4 < 4; ++g4) sts128(ga + g4 * 32, __builtin_bit_cast(u32x4, f32x4{va[4 * g4], va[4 * g4 + 1], va[4 * g4 + 2], va[4 * g4 + 3]}));
      }
      ETICK(23);
#pragma unroll
      for (int i = 0; i < 8; ++i) sts64(wa + (i >> 2) * 2048 + (sw8 ^ ((i & 3) * 16)), ph[i >> 2][i & 3]);
      ETICK(24);
      lds_post<C_SYNC>(y);
      ETICK(25); ETICK(26);
      lds_wait<C_SYNC, 1>(y);
      const u32 t0 = 4 * h + q, ra = L_PHI + (w >> 1) * 2048 + t0 * 64;
      u32 a0 = ra + (((4 * cb + pp) ^ (t0 >> 1)) * 8), a1 = ra + 512 + (((4 * cb + pp) ^ ((t0 >> 1) + 4)) * 8), av = L_VF + l16;
      OPAQUE(a0); OPAQUE(a1); OPAQUE(av);
      f32x16 kp;
#pragma unroll
      for (int e = 0; e < 16; ++e) kp[e] = 0.f;
      sfor<2>([&](auto uc) {
        constexpr int u2 = decltype(uc)::v;
        u32x4 pf[4], vv[4];
#pragma unroll
        for (int i = 0; i < 4; ++i) {
          const int k8 = u2 * 4 + i, off = (k8 >> 1) * 4096 + (k8 & 1) * 1024;
          pf[i] = trfrag(a0 + off, a1 + off); vv[i] = lds128(av + k8 * 1024);
        }
        SB();
        ETICK(27 + u2);
#pragma unroll
        for (int i = 0; i < 4; ++i) kp = mfma(pf[i], vv[i], kp);
        asm volatile("" : "+v"(kp));
      });
      lds_post<C_SYNC>(y);
      if (w & 1) kvs = pack8(kp, 1); else kvs = pack8(kp, 0);
      OPS(MK(M_ST), MK(M_ST) + 1);
    }
    STAMP(4);
    if constexpr (!isH) {
      const int hd = m, pc = half * 4 + w;
      flag_check<1>(fv, ws, fa, tag);
      u32x4 pv[8];
      for (int it = 0; it < 20000; ++it) {
#pragma unroll
        for (int i = 0; i < 8; ++i) ldg_sc1(pv[i], h * 4096 + r * 16, ws, WS_KVP + (hd * 16 + 2 * i) * 4096 + pc * 512);
        asm volatile("s_waitcnt vmcnt(0)" : "+v"(pv[0]), "+v"(pv[1]), "+v"(pv[2]), "+v"(pv[3]), "+v"(pv[4]), "+v"(pv[5]), "+v"(pv[6]), "+v"(pv[7])::"memory");
        if (arrived(pv)) break;
      }
      STAMP(8);
      u32x4 o;
#pragma unroll
      for (int e = 0; e < 4; ++e) {
        float lo[8], hi[8];
#pragma unroll
        for (int i = 0; i < 8; ++i) { lo[i] = u2f(pv[i][e] << 16); hi[i] = u2f(pv[i][e] & 0xffff0000u); }
        float a, c, d, f;
        both_halves(((lo[0] + lo[1]) + (lo[2] + lo[3])) + ((lo[4] + lo[5]) + (lo[6] + lo[7])), a, c);
        both_halves(((hi[0] + hi[1]) + (hi[2] + hi[3])) + ((hi[4] + hi[5]) + (hi[6] + hi[7])), d, f);
        o[e] = cvtpk(a + c, d + f);
        if (r == 29) o[e] = 0x3f803f80u;
        if (e >= 2 && (pc & 3) == 3) o[e] = 0u;
      }
      stg_sc1(not_ones(o), h ? OOB : l16, ws, WS_KVF + hd * 4096 + pc * 512);
      STAMP(7);
      STAMP_FLUSH();
    } else {
      f32x16 acc[2][2];
#pragma unroll
      for (int i = 0; i < 4; ++i)
#pragma unroll
        for (int e = 0; e < 16; ++e) acc[i >> 1][i & 1][e] = 0.f;
      OPS(MK(M_ST) + 1, MK(M_JOB));
      __builtin_amdgcn_s_setprio(PRIO3);
      BPath bp = make_bpath(L_K3 + w * 2048, lane);
      u32x4 tf[2][2];
      asm volatile("s_waitcnt vmcnt(%2)" : "+v"(bf[0][0]), "+v"(bf[0][1]) : "n"(MK(M_JOB) - 1 - T::find(OB, 0, 1)) : "memory");
      sts128(bp.wa, bf[0][0]); sts128(bp.wa + 1024, bf[0][1]);
      tf[0][0] = trfrag(bp.ra[0], bp.ra[0] + 1024); tf[0][1] = trfrag(bp.ra[1], bp.ra[1] + 1024);
      sfor<28>([&](auto nc) {
        constexpr int n = decltype(nc)::v, P = MK(M_JOB + n), NL = MK(M_JOB + n + 1) - P, nb = n < 27 ? n + 1 : n;
        constexpr int need = cmax(T::find(OA, n, 1), T::find(OB, nb, 1));
        static_assert(need < P, "stream order");
        if constexpr (nb < 19) asm volatile("s_waitcnt vmcnt(%4)" : "+v"(af[n][0]), "+v"(af[n][1]), "+v"(bf[nb][0]), "+v"(bf[nb][1]) : "n"(P - 1 - need) : "memory");
        else asm volatile("s_waitcnt vmcnt(%2)" : "+v"(af[n][0]), "+v"(af[n][1]) : "n"(P - 1 - need) : "memory");
        sfor<4>([&](auto uc) {
          constexpr int u = decltype(uc)::v, c = u >> 1, t = u & 1;
          acc[c][t] = mfma(tf[n & 1][c], af[n][t], acc[c][t]);
          SB();
          asm volatile("" : "+v"(acc[c][t]));
          if constexpr (nb < 19) {
            if constexpr (u == 0) { sts128(bp.wa, bf[nb][0]); sts128(bp.wa + 1024, bf[nb][1]); }
            if constexpr (u == 1) { tf[nb & 1][0] = trfrag(bp.ra[0], bp.ra[0] + 1024); tf[nb & 1][1] = trfrag(bp.ra[1], bp.ra[1] + 1024); }
            SB();
          } else if constexpr (n < 27 && u < 2) {
            if constexpr (nb == 19) { bp.ra[u] += L_R2 + w * 18432 - (L_K3 + w * 2048); OPAQUE(bp.ra[u]); }
            tf[nb & 1][u] = trfrag(bp.ra[u] + (nb - 19) * 2048, bp.ra[u] + (nb - 19) * 2048 + 1024);
            SB();
          }
          OPS(P + NL * u / 4, P + NL * (u + 1) / 4);
        });
        if constexpr (n == 12) STAMP(6);
      });
      STAMP(10);
      dump_tile(acc, L_R2 + w * 18432, lane);
    }
  }
  if (isH) {
    const int tok = tid >> 3, pc = tid & 7;
    const u32 a = tok * 272 + pc * 32;
    f32x4 v[8][2];
#pragma unroll
    for (int u = 0; u < 8; ++u)
#pragma unroll
      for (int i = 0; i < 2; ++i) v[u][i] = __builtin_bit_cast(f32x4, lds128((u < 4 ? L_R2 + u * 18432 : L_R1 + (u - 4) * 17408) + a + i * 16));
    const float omg = *(LAS float*)(size_t)(L_GW + tok * 4);
    const f32x4 s0 = ((v[0][0] + v[1][0]) + (v[2][0] + v[3][0])) * omg + ((v[4][0] + v[5][0]) + (v[6][0] + v[7][0]));
    const f32x4 s1 = ((v[0][1] + v[1][1]) + (v[2][1] + v[3][1])) * omg + ((v[4][1] + v[5][1]) + (v[6][1] + v[7][1]));
    *(u32x4*)(p.out + (rt64 * 64 + tok) * 896 + ct2 * 128 + pc * 16) = u32x4{cvtpk(s0.x, s0.y), cvtpk(s0.z, s0.w), cvtpk(s1.x, s1.y), cvtpk(s1.z, s1.w)};
    STAMP(9);
    STAMP_FLUSH();
  }
}

__global__ __launch_bounds__(512, 2) void fused(Params p) {
  asm volatile(".subsection 2\n\t.fill 256, 4, 0xBF800000\n\t.subsection 0");
  const int wave = rfl(threadIdx.x >> 6);
  *(LAS u32*)(size_t)(L_Z + (threadIdx.x >> 6) * 512 + 256 + (threadIdx.x & 63) * 4) = 1u;
  *(LAS u32*)(size_t)(L_CNT + (threadIdx.x & 15) * 4) = 0u;
  asm volatile("s_waitcnt lgkmcnt(0)\n\ts_barrier" ::: "memory");
  if (!((blockIdx.x & 1) && blockIdx.x >= 192)) {
    if (wave < 4) body<true, true>(p, wave); else body<false, true>(p, wave);
  } else {
    if (wave < 4) body<true, false>(p, wave); else body<false, false>(p, wave);
  }
}
}
#if defined(__HIP_DEVICE_COMPILE__)
#pragma clang attribute pop
#endif

extern "C" void kernel_launch(void* const* d_in, const int* in_sizes, int n_in, void* d_out, int out_size, void* d_ws, size_t ws_size,
                              hipStream_t stream) {
  hk::Params p;
  p.x = (const char*)d_in[0]; p.wqkv = (const char*)d_in[1]; p.wphi = (const char*)d_in[2]; p.wo = (const char*)d_in[3];
  p.wg = (const char*)d_in[5]; p.wu = (const char*)d_in[6]; p.wd = (const char*)d_in[7]; p.gw = (const char*)d_in[8];
  p.out = (char*)d_out; p.ws = (char*)d_ws;
  (void)hipFuncSetAttribute((const void*)hk::fused, hipFuncAttributeMaxDynamicSharedMemorySize, hk::L_TOTAL);
  hk::fused<<<256, 512, hk::L_TOTAL, stream>>>(p);
}
```

```cpp
#include <hip/hip_runtime.h>
#include <stdint.h>

#define NOWAIT 0
#define SLEEPN "1"
#define PRIO0 2
#define PRIOE 3
#define NOBEPI 7
#define PFM 15
#define KVT 16
#define PRIO2 2
#define PRIO1 1
#define PRIO3 0
#define REDAT 1
#define STAMPS 0

#if defined(__HIP_DEVICE_COMPILE__)
#pragma clang attribute push(__attribute__((target("no-packed-fp32-ops"))), apply_to = function)
#endif
namespace hk {
typedef unsigned u32;
typedef unsigned long long u64;
typedef __bf16 bf16x8 __attribute__((ext_vector_type(8)));
typedef __bf16 bf16x2 __attribute__((ext_vector_type(2)));
typedef short s16x4 __attribute__((ext_vector_type(4)));
typedef short s16x8 __attribute__((ext_vector_type(8)));
typedef u32 u32x4 __attribute__((ext_vector_type(4)));
typedef u32 u32x2 __attribute__((ext_vector_type(2)));
typedef float f32x16 __attribute__((ext_vector_type(16)));
typedef float f32x4 __attribute__((ext_vector_type(4)));
typedef float f32x2 __attribute__((ext_vector_type(2)));
typedef __amdgpu_buffer_rsrc_t srd_t;
#define LAS __attribute__((address_space(3)))
#define DI __device__ __forceinline__

constexpr int S = 2048, D = 448, DF = 1792;
constexpr u32 WS_E = 0;
constexpr u32 WS_FK = 4096;
constexpr u32 WS_FB = WS_FK + 16 * 512;
constexpr u32 WS_G = 32768;
constexpr u32 WS_FF = 49152;
constexpr u32 WS_FQ = 65536;
constexpr u32 WS_FP = 131072;
constexpr u32 WS_FO = 262144;
constexpr u32 WS_FH = 393216;
constexpr u32 WS_KVF = 655360;
constexpr u32 WS_KVP = 720896;
constexpr u32 WS_O = WS_KVP + 16 * 16 * 4096;
constexpr u32 WS_Q = WS_O + 64 * 32 * 1024;
constexpr u32 WS_H = WS_Q + 64 * 32 * 1024;
constexpr u32 WS_KT = WS_H + 64 * 112 * 1024;
constexpr u32 WS_BT = WS_KT + 16 * 56 * 1024;
constexpr u32 WS_END = WS_BT + 7 * 144 * 2048;
constexpr u32 WS_STAMP = 24u << 20;
constexpr u32 OOB = 0x7ff00000u;
constexpr u32 L_K3 = 9 * 16384;
constexpr u32 L_WPHI = L_K3 + 8192;
constexpr u32 L_GW = L_WPHI + 2048;
constexpr u32 L_Z = L_GW + 1024;
constexpr u32 L_CNT = L_Z + 4096;
constexpr u32 L_DUM = L_CNT + 64;
constexpr u32 L_TOTAL = 163840;
enum { C_PROG = 0, C_SYNC = 16 };

struct Params {
  const char *x, *wqkv, *wphi, *wo, *wg, *wu, *wd, *gw;
  char *out, *ws;
};

DI u32 f2u(float f) { return __builtin_bit_cast(u32, f); }
DI float u2f(u32 u) { return __builtin_bit_cast(float, u); }
DI u32 rfl(u32 v) { return __builtin_amdgcn_readfirstlane(v); }
DI srd_t mksrd(const void* p, u32 bytes) { return __builtin_amdgcn_make_buffer_rsrc((void*)p, 0, bytes, 0x00020000); }
DI u32 cvtpk(float lo, float hi) { f32x2 v = {lo, hi}; return __builtin_bit_cast(u32, __builtin_convertvector(v, bf16x2)); }
DI f32x16 mfma(u32x4 a, u32x4 b, f32x16 c) {
  return __builtin_amdgcn_mfma_f32_32x32x16_bf16(__builtin_bit_cast(bf16x8, a), __builtin_bit_cast(bf16x8, b), c, 0, 0, 0);
}
DI u32x2 tr64(u32 addr) { return __builtin_bit_cast(u32x2, __builtin_amdgcn_ds_read_tr16_b64_v4i16((LAS s16x4*)(size_t)addr)); }
DI u32x4 trfrag(u32 a0, u32 a1) { u32x2 lo = tr64(a0), hi = tr64(a1); return u32x4{lo.x, lo.y, hi.x, hi.y}; }
DI u32x4 lds128(u32 addr) { return *(LAS u32x4*)(size_t)addr; }
DI void sts128(u32 addr, u32x4 v) { *(LAS u32x4*)(size_t)addr = v; }
DI void sts64(u32 addr, u32x2 v) { *(LAS u32x2*)(size_t)addr = v; }
#define MEMFENCE() asm volatile("" ::: "memory")
#define VMWAIT(n) asm volatile("s_waitcnt vmcnt(%0)" ::"n"(n) : "memory")
DI void dma_buf(u32 ldsaddr, u32 voff, srd_t srd, u32 soff) {
  asm volatile("s_mov_b32 m0, %0\n\ts_nop 0\n\tbuffer_load_dwordx4 %1, %2, %3 offen lds" ::"s"(ldsaddr), "v"(voff), "s"(srd), "s"(soff) : "memory");
}
template <u32 IMM, u32 SIMM> DI u32 dma_open(u32 base, u32 sbase) {
  u32 so;
  asm volatile("s_add_u32 m0, %1, %2\n\ts_add_u32 %0, %3, %4" : "=s"(so) : "s"(base), "n"(IMM), "s"(sbase), "n"(SIMM) : "scc", "memory");
  return so;
}
template <int OFF> DI void dma_off(u32 voff, srd_t srd, u32 soff) {
  asm volatile("buffer_load_dwordx4 %0, %1, %2 offen offset:%3 lds" ::"v"(voff), "s"(srd), "s"(soff), "n"(OFF) : "memory");
}
template <int OFF> DI void dma_off_sc1(u32 voff, srd_t srd, u32 soff) {
  asm volatile("buffer_load_dwordx4 %0, %1, %2 offen offset:%3 sc1 lds" ::"v"(voff), "s"(srd), "s"(soff), "n"(OFF) : "memory");
}
template <int OFF> DI void ldg_sc1o(u32x4& dst, u32 voff, srd_t srd, u32 soff) {
  asm volatile("buffer_load_dwordx4 %0, %1, %2, %3 offen offset:%4 sc1" : "=v"(dst) : "v"(voff), "s"(srd), "s"(soff), "n"(OFF) : "memory");
}
DI void ldg_sc1(u32x4& dst, u32 voff, srd_t srd, u32 soff) {
  asm volatile("buffer_load_dwordx4 %0, %1, %2, %3 offen sc1" : "=v"(dst) : "v"(voff), "s"(srd), "s"(soff) : "memory");
}
DI void stg_sc1(u32x4 v, u32 voff, srd_t srd, u32 soff) {
  asm volatile("buffer_store_dwordx4 %0, %1, %2, %3 offen sc1\n\ts_nop 1" ::"v"(v), "v"(voff), "s"(srd), "s"(soff) : "memory");
}
DI void stg64_sc1(u32x2 v, u32 voff, srd_t srd, u32 soff) {
  asm volatile("buffer_store_dwordx2 %0, %1, %2, %3 offen sc1" ::"v"(v), "v"(voff), "s"(srd), "s"(soff) : "memory");
}
DI void stg32_sc1(u32 v, u32 voff, srd_t srd, u32 soff) {
  asm volatile("buffer_store_dword %0, %1, %2, %3 offen sc1" ::"v"(v), "v"(voff), "s"(srd), "s"(soff) : "memory");
}
struct Sync { u32 pa, ra, one; };
template <int C> DI void lds_post(const Sync& y) { asm volatile("ds_add_u32 %0, %1 offset:%2" ::"v"(y.pa), "v"(y.one), "n"(C) : "memory"); }
template <int C> DI u32 lds_peek(const Sync& y) { return *(volatile LAS u32*)(size_t)(y.ra + C); }
template <int C, int T> DI void lds_wait_pre(u32 a, u32 v) {
  u32 cnt;
  asm volatile("v_cmp_gt_u32 vcc, %3, %0\n\ts_cbranch_vccnz .Lsl%=\n.Lse%=:\n\t.subsection 1\n"
               ".Lsl%=:\n\ts_mov_b32 %1, 0x40000\n.Lsp%=:\n\tds_read_b32 %0, %2 offset:%4\n\ts_waitcnt lgkmcnt(0)\n\tv_cmp_gt_u32 vcc, %3, %0\n\ts_cbranch_vccz .Lse%=\n\t"
               "s_sub_u32 %1, %1, 1\n\ts_cbranch_scc0 .Lsp%=\n\ts_branch .Lse%=\n\t.subsection 0"
               : "+v"(v), "=&s"(cnt) : "v"(a), "n"(T), "n"(C) : "vcc", "scc", "memory");
}
template <int C, int T> DI void lds_wait(const Sync& y) { lds_wait_pre<C, T>(y.ra, lds_peek<C>(y)); }
template <int K> DI void z_post(u32 oob, srd_t srd) {
  asm volatile("buffer_load_dword %0, %1, 0 offen offset:%2 lds" ::"v"(oob), "s"(srd), "n"(4 * K + 4) : "memory");
}
DI void set_m0(u32 v) { asm volatile("s_mov_b32 m0, %0\n\ts_nop 0" ::"s"(v) : "memory"); }
template <int K> DI u32 z_peek(u32 za) { return *(volatile LAS u32*)(size_t)(za + 4 * K); }
template <int K> DI void z_wait_pre(u32 za, u32 v) {
  u32 cnt;
  asm volatile("v_cmp_ne_u32 vcc, 0, %0\n\ts_cbranch_vccnz .Lsl%=\n.Lse%=:\n\t.subsection 1\n"
               ".Lsl%=:\n\ts_mov_b32 %1, 0x40000\n.Lsp%=:\n\tds_read_b32 %0, %2 offset:%3\n\ts_waitcnt lgkmcnt(0)\n\tv_cmp_ne_u32 vcc, 0, %0\n\ts_cbranch_vccz .Lse%=\n\t"
               "s_sub_u32 %1, %1, 1\n\ts_cbranch_scc0 .Lsp%=\n\ts_branch .Lse%=\n\t.subsection 0"
               : "+v"(v), "=&s"(cnt) : "v"(za), "n"(4 * K) : "vcc", "scc", "memory");
}
template <int K> DI void z_wait(u32 za) { z_wait_pre<K>(za, z_peek<K>(za)); }
DI void flag_peek(u64& v, srd_t ws, u32 voff) {
  asm volatile("buffer_load_dwordx2 %0, %1, %2, 0 offen sc1" : "=v"(v) : "v"(voff), "s"(ws) : "memory");
}
template <int N> DI void flag_check(u64 v, srd_t ws, u32 voff, u64 tag) {
  u32 cnt;
  asm volatile("s_waitcnt vmcnt(%5)\n\t"
               "v_cmp_ne_u64 vcc, %4, %0\n\ts_cbranch_vccnz .Lfl%=\n.Lfe%=:\n\t.subsection 1\n"
               ".Lfl%=:\n\ts_mov_b32 %1, 0x8000\n"
               ".Lfp%=:\n\tbuffer_load_dwordx2 %0, %2, %3, 0 offen sc1\n\ts_waitcnt vmcnt(0)\n\tv_cmp_ne_u64 vcc, %4, %0\n\ts_cbranch_vccz .Lfe%=\n\t"
               "s_sleep " SLEEPN "\n\ts_sub_u32 %1, %1, 1\n\ts_cbranch_scc0 .Lfp%=\n\ts_branch .Lfe%=\n.Lfz%=:\n\t.subsection 0"
               : "+v"(v), "=&s"(cnt) : "v"(voff), "s"(ws), "s"(tag), "n"(N) : "vcc", "scc", "memory");
}
DI void flag_wait(srd_t ws, u32 voff, u64 tag) { u64 v; flag_peek(v, ws, voff); flag_check<0>(v, ws, voff, tag); }
DI void flag_set(srd_t ws, u32 voff, u64 tag) {
  asm volatile("buffer_store_dwordx2 %0, %1, %2, 0 offen sc1" ::"v"(tag), "v"(voff), "s"(ws) : "memory");
}
template <bool FORCE, int NZ> DI void flag_try(u32& done, srd_t ws, u32 voff, u64 tag) {
  u32 t;
#define FT_TAIL "s_cbranch_scc0 .Lfp%=\n.Lfe%=:\n\t.subsection 1\n.Lfp%=:\n\ts_waitcnt vmcnt(%5)\n\tbuffer_store_dwordx2 %2, %3, %4, 0 offen sc1\n\t" \
                "s_mov_b32 %0, 64\n\ts_branch .Lfe%=\n.Lfz%=:\n\t.subsection 0"
  if (FORCE) asm volatile("s_cmp_lg_u32 %0, 0\n\t" FT_TAIL : "+s"(done), "=&s"(t) : "v"(tag), "v"(voff), "s"(ws), "n"(NZ) : "scc", "memory");
  else asm volatile("s_getreg_b32 %1, hwreg(HW_REG_IB_STS, 0, 4)\n\ts_or_b32 %1, %1, %0\n\ts_cmp_gt_u32 %1, %5\n\t" FT_TAIL
                    : "+s"(done), "=&s"(t) : "v"(tag), "v"(voff), "s"(ws), "n"(NZ) : "scc", "memory");
}
#define SB() __builtin_amdgcn_sched_barrier(0)
#define OPAQUE(x) asm volatile("" : "+v"(x))
DI void interleave4() {
#pragma unroll
  for (int i = 0; i < 4; ++i) { __builtin_amdgcn_sched_group_barrier(0x8, 1, 0); __builtin_amdgcn_sched_group_barrier(0x100, 2, 0); }
}
DI float swap_hi(float lo_wants, int h) {
  auto r = __builtin_amdgcn_permlane32_swap(f2u(lo_wants), f2u(lo_wants), false, false);
  (void)h;
  return u2f(r[1]);
}
DI void both_halves(float v, float& lo, float& hi) {
  auto r = __builtin_amdgcn_permlane32_swap(f2u(v), f2u(v), false, false);
  lo = u2f(r[0]); hi = u2f(r[1]);
}
#define STAMP(i) do {} while (0)
#define STAMP_FLUSH() do {} while (0)
#define STAMP_ARG
#define STAMP_PASS

#define FSTAMP(i) do {} while (0)
constexpr float LOG2E = 1.4426950408889634f;

template <int I> struct IC { static constexpr int v = I; };
template <int N, class F, int I = 0> DI void sfor(F f) { if constexpr (I < N) { f(IC<I>{}); sfor<N, F, I + 1>(f); } }

enum { OX, OW, OU, OK, OZ, OPEEK, OB, OA, OST, OFLAG, OOM };
struct Op { int kind, idx, i; };
enum { M_KV0, M_EPI, M_ST, M_JOB };
struct Tab { Op v[300]; int mark[48]; int n; };
#define AWV 5
constexpr int AW = AWV;
constexpr int zidx(int kind, int idx) { return kind == OW ? idx : kind == OK ? 7 + idx : 11 + idx; }
constexpr int rpos(int kind, int idx) { return kind == OX ? 2 * idx : kind == OW ? 2 * idx + 1 : kind == OK ? 14 + idx : 17 + idx; }
constexpr u32 slot_of(int kind, int idx) { return kind == OK && idx == 3 ? 9 : rpos(kind, idx) % 9; }
constexpr Tab make_tab(bool H) {
  Tab t{}; int n = 0;
  auto I4 = [&](int kind, int idx, int lo = 0, int hi = 4) {
    for (int i = lo; i < hi; ++i) t.v[n++] = Op{kind, idx, i};
    if (kind != OX && kind <= OK && hi == (kind == OK && idx == 3 ? 2 : 4)) t.v[n++] = Op{OZ, zidx(kind, idx), 0};
  };
  for (int s = 0; s < 7; ++s) { if (s == 6) t.v[n++] = Op{OPEEK, 0, 0}; I4(OX, s); I4(OW, s); }
  I4(OK, 0); I4(OK, 1); I4(OK, 2); I4(OK, 3, 0, 2);
  t.mark[M_KV0] = n;
  for (int u = 0; u < 28; ++u) { if (u == KVT) t.mark[M_EPI] = n; if (u == 22 && H) t.v[n++] = Op{OPEEK, 1, 0}; I4(OU, u >> 2, u & 3, (u & 3) + 1); }
  if (H) for (int j = 0; j < 14 - NOBEPI; ++j) { if (j == 11) t.v[n++] = Op{OPEEK, 2, 0}; I4(OB, j, 0, 2); }
  t.mark[M_ST] = n; t.v[n++] = Op{OST, 0, 0};
  if (H) {
    for (int j = 14 - NOBEPI; j < 14; ++j) { if (j == 11) t.v[n++] = Op{OPEEK, 2, 0}; if (NOBEPI > 5 && j == 19 - NOBEPI) t.v[n++] = Op{OFLAG, 0, 0}; I4(OB, j, 0, 2); }
    for (int j = 0; j < AW; ++j) I4(OA, j, 0, 2);
    for (int j = 0; j < 28; ++j) {
      t.mark[M_JOB + j] = n;
      if (j == 0 && NOBEPI <= 5) t.v[n++] = Op{OFLAG, 0, 0};
      if (j == 10) t.v[n++] = Op{OPEEK, 3, 0};
      if (j < 14 - AW) I4(OA, j + AW, 0, 2);
      if (j >= 1 && j < 15) I4(OB, j + 13, 0, 2);
      if (j == 13) for (int k = 14; k < 14 + AW; ++k) I4(OA, k, 0, 2);
      if (j >= 14 && j < 28 - AW) I4(OA, j + AW, 0, 2);
    }
    t.mark[M_JOB + 28] = n;
  }
  t.n = n;
  return t;
}
template <bool H> struct TB {
  static constexpr Tab t = make_tab(H);
  static constexpr int find(int kind, int idx, int i) { for (int k = 0; k < t.n; ++k) if (t.v[k].kind == kind && t.v[k].idx == idx && t.v[k].i == i) return k; return 1000; }
  static constexpr int etick(int n) {
    int k = t.mark[M_EPI];
    for (int i = 0; i < n && k < t.mark[M_ST]; ++i) { ++k; while (k < t.mark[M_ST] && (t.v[k].kind == OZ || t.v[k - 1].kind == OPEEK)) ++k; }
    return n >= 29 ? t.mark[M_ST] : k;
  }
  static constexpr int need(int k) {
    if (k >= t.n || t.v[k].kind > OK || t.v[k].i != 0 || slot_of(t.v[k].kind, t.v[k].idx) == 9) return -1;
    const int p = rpos(t.v[k].kind, t.v[k].idx);
    return p < 8 || p > 22 || (p > 9 && (p & 1) == 0) ? -1 : p == 8 ? 0 : 1 + ((p - 8) >> 1) - (p == 9 ? 0 : 0);
  }
};
static_assert(KVT >= 12 && KVT <= 24, "signals of U2 / U6 double as progress marks of the k/v GEMM");
constexpr int LAG = 12;
constexpr int cmax(int a, int b) { return a > b ? a : b; }

template <bool HG, bool FIRST, class Hook>
DI void unit_gemm(u32x4 (&xf)[28], f32x16 (&acc)[4], int lane, int w, u32 za, Hook hook STAMP_ARG) {
  const u32 h = lane >> 5, mat = (lane >> 4) & 1, q = (lane & 15) >> 2, pp = lane & 3;
  const u32 vb0 = HG ? mat * 8192 + (8 * h + q) * 128 + ((q & 2) | mat) * 32 + 8 * pp : (8 * h + q) * 256 + mat * 128 + q * 32 + 8 * pp;
  constexpr u32 KS = HG ? 2048 : 4096;
  constexpr int KIND = FIRST ? OW : OU;
  const u32 r = lane & 31;
  const u32 xb = w * 4096 + r * 128 + ((h ^ ((r >> 1) & 7)) * 16);
  u32 vb[2][4], xa[2][4], pre = 0;
#pragma unroll
  for (int c = 0; c < 8; ++c) {
    vb[c >> 2][c & 3] = (vb0 ^ ((c & 3) * 32)) + (c < 4 && !FIRST ? 131072 : (c >> 2) * 65536); OPAQUE(vb[c >> 2][c & 3]);
    if (FIRST) { xa[c >> 2][c & 3] = (xb ^ ((c & 3) * 32)) + (c >> 2) * 81920; OPAQUE(xa[c >> 2][c & 3]); }
  }
  u32x4 fr[4];
  auto rdw = [&](int s, int n) {
    const int slot = slot_of(KIND, s);
    const u32 a = vb[slot >> 2 & 1][n & 3] + (slot & 3) * 16384 + (n >> 2) * KS;
    fr[n & 3] = trfrag(a, a + KS / 4);
  };
  auto rdx = [&](int s, int kk) { const int slot = slot_of(OX, s); xf[4 * s + kk] = lds128(slot < 5 ? xa[0][kk] + slot * 16384 : xa[1][kk] + (slot - 5) * 16384); };
#pragma unroll
  for (int c = 0; c < 4; ++c)
#pragma unroll
    for (int e = 0; e < 16; ++e) acc[c][e] = 0.f;
  z_wait<zidx(KIND, 0)>(za);
  STAMP(FIRST ? 16 : 32);
  if (FIRST) { rdx(0, 0); rdx(0, 1); rdx(0, 2); }
#pragma unroll
  for (int n = 0; n < 4; ++n) rdw(0, n);
  SB();
  sfor<112>([&](auto gc) {
    constexpr int G = decltype(gc)::v, s = G >> 4, n = G & 15;
    acc[n & 3] = mfma(fr[n & 3], xf[4 * s + (n >> 2)], acc[n & 3]);
    SB();
    asm volatile("" : "+v"(acc[n & 3]));
    if constexpr (n == 12 && s < 6) z_wait_pre<zidx(KIND, s + 1)>(za, pre);
    if constexpr (n == 12 && s < 6 && FIRST) rdx(s + 1, 0);
    if constexpr (!FIRST && s == 0 && n >= 12) { vb[0][n & 3] -= 131072; OPAQUE(vb[0][n & 3]); }
    if constexpr (n < 12) rdw(s, n + 4); else if constexpr (s < 6) rdw(s + 1, n - 12);
    if constexpr (FIRST) {
      if constexpr (n == 0) rdx(s, 3);
      if constexpr (n >= 13 && n < 15 && s < 6) rdx(s + 1, n - 12);
    }
    if constexpr (n == 8 && s < 6) pre = z_peek<zidx(KIND, s + 1)>(za);
    hook(gc);
    SB();
  });
}

DI void load_wphi(u32x4 (&wp)[2], u32 base, int lane) {
  const u32 h = lane >> 5, fb = (lane >> 4) & 1, q = (lane & 15) >> 2, pp = lane & 3;
  u32 col = 16 * fb + 4 * pp; if (col == 28) col = 24;
  const u32 a = base + (4 * h + q) * 56 + col * 2;
  wp[0] = trfrag(a, a + 8 * 56);
  u32x2 lo = tr64(a + 16 * 56), hi = tr64(base + (24 + q) * 56 + col * 2);
  wp[1] = u32x4{lo.x, lo.y, h ? 0u : hi.x, h ? 0u : hi.y};
}
DI u32x4 pack8(const f32x16& v, int s) {
  return u32x4{cvtpk(v[8 * s], v[8 * s + 1]), cvtpk(v[8 * s + 2], v[8 * s + 3]), cvtpk(v[8 * s + 4], v[8 * s + 5]), cvtpk(v[8 * s + 6], v[8 * s + 7])};
}
DI float sigmoid(float t) { return __builtin_amdgcn_rcpf(1.f + __builtin_amdgcn_exp2f(-LOG2E * t)); }
DI float max3(float a, float b, float c) { return __builtin_fmaxf(__builtin_fmaxf(a, b), c); }
DI float min3(float a, float b, float c) { return __builtin_fminf(__builtin_fminf(a, b), c); }
DI void feature_exp(const u32x4 (&t)[2], const u32x4 (&wp)[2], f32x16& ep, f32x16& en) {
  f32x16 pz;
#pragma unroll
  for (int e = 0; e < 16; ++e) pz[e] = 0.f;
  pz = mfma(wp[0], t[0], pz);
  pz = mfma(wp[1], t[1], pz);
  float mx = max3(max3(max3(pz[0], pz[1], pz[2]), max3(pz[3], pz[4], pz[5]), max3(pz[6], pz[7], pz[8])),
                  max3(max3(pz[9], pz[10], pz[11]), max3(pz[12], pz[13], pz[14]), pz[15]), pz[15]);
  float mn = min3(min3(min3(pz[0], pz[1], pz[2]), min3(pz[3], pz[4], pz[5]), min3(pz[6], pz[7], pz[8])),
                  min3(min3(pz[9], pz[10], pz[11]), min3(pz[12], pz[13], pz[14]), pz[15]), pz[15]);
  float a, b;
  both_halves(mx, a, b); mx = __builtin_fmaxf(a, b) * LOG2E;
  both_halves(mn, a, b); mn = __builtin_fminf(a, b) * LOG2E;
#pragma unroll
  for (int e = 0; e < 16; ++e) {
    ep[e] = __builtin_amdgcn_exp2f(__builtin_fmaf(pz[e], LOG2E, -mx));
    en[e] = __builtin_amdgcn_exp2f(__builtin_fmaf(pz[e], -LOG2E, mn));
  }
}
template <int NS, int NB, class Epi, class Drain>
DI void task_o(int head, int rt32, srd_t ws, u64 tag, int lane, u64 fq, u32 fqa, u32 fdone, Epi epi, Drain drain STAMP_ARG) {
  const int h = lane >> 5;
  const u32 l16 = lane * 16;
  u32x4 qf[2], wp[2], kf[2][2];
  u32 gu;
  auto ldq = [&]() {
    ldg_sc1o<0>(qf[0], l16, ws, WS_Q + (rt32 * 32 + head * 2) * 1024);
    ldg_sc1o<1024>(qf[1], l16, ws, WS_Q + (rt32 * 32 + head * 2) * 1024);
    asm volatile("buffer_load_dword %0, %1, %2, %3 offen sc1" : "=v"(gu) : "v"((lane & 31) * 4), "s"(ws), "s"(WS_G + rt32 * 128) : "memory");
  };
  asm volatile("s_waitcnt vmcnt(0)" : "+v"(fq)::"memory");
  const bool early = __builtin_amdgcn_ballot_w64(fq != tag) == 0;
  ldg_sc1o<0>(qf[0], early ? l16 : OOB, ws, WS_Q + (rt32 * 32 + head * 2) * 1024);
  ldg_sc1o<1024>(qf[1], early ? l16 : OOB, ws, WS_Q + (rt32 * 32 + head * 2) * 1024);
  asm volatile("buffer_load_dword %0, %1, %2, %3 offen sc1" : "=v"(gu) : "v"(early ? (lane & 31) * 4 : OOB), "s"(ws), "s"(WS_G + rt32 * 128) : "memory");
  epi();
  STAMP(5);
  u32 done = 0;
  asm volatile("s_waitcnt vmcnt(%3)" : "+v"(qf[0]), "+v"(qf[1]), "+v"(gu) : "n"(NS + NB) : "memory");
  if (!early) {
    flag_try<true, NB>(done, ws, fdone, tag);
    STAMP(11);
    flag_wait(ws, fqa, tag);
    STAMP(12);
    ldq();
    asm volatile("s_waitcnt vmcnt(0)" : "+v"(qf[0]), "+v"(qf[1]), "+v"(gu)::"memory");
  }
  load_wphi(wp, L_WPHI, lane);
  STAMP(6);
  float gate = sigmoid(u2f(gu));
  f32x16 ep, en; feature_exp(qf, wp, ep, en);
  u32x4 ef[2][2] = {{pack8(ep, 0), pack8(ep, 1)}, {pack8(en, 0), pack8(en, 1)}};
  asm volatile("" : "+v"(ef[0][0]), "+v"(ef[0][1]), "+v"(ef[1][0]), "+v"(ef[1][1]), "+v"(gate));
  flag_try<true, NB>(done, ws, fdone, tag);
  STAMP(7);
  flag_wait(ws, WS_FF + (head * 8 + (lane >> 3)) * 128, tag);
  STAMP(13);
  sfor<4>([&](auto ic) { constexpr int i = decltype(ic)::v; ldg_sc1o<i * 1024>(kf[i >> 1][i & 1], l16, ws, WS_KVF + head * 4096); });
  asm volatile("s_waitcnt vmcnt(0)" : "+v"(kf[0][0]), "+v"(kf[0][1]), "+v"(kf[1][0]), "+v"(kf[1][1])::"memory");
  STAMP(8);
  f32x16 np, nn;
#pragma unroll
  for (int e = 0; e < 16; ++e) { np[e] = 0.f; nn[e] = 0.f; }
  np = mfma(kf[0][0], ef[0][0], np); nn = mfma(kf[1][0], ef[1][0], nn);
  np = mfma(kf[0][1], ef[0][1], np); nn = mfma(kf[1][1], ef[1][1], nn);
  const float rp = __builtin_amdgcn_rcpf(np[13]), rn = __builtin_amdgcn_rcpf(nn[13]);
  const float inv = __builtin_amdgcn_rcpf(np[12] * rp + nn[12] * rn + 1e-6f) * gate;
  const float cp = swap_hi(rp * inv, h), cn = swap_hi(rn * inv, h);
  f32x16 o;
#pragma unroll
  for (int e = 0; e < 16; ++e) o[e] = np[e] * cp + nn[e] * cn;
  u32x4 o0 = pack8(o, 0), o1 = pack8(o, 1);
  o1.z = h ? 0u : o1.z; o1.w = h ? 0u : o1.w;
  stg_sc1(o0, l16, ws, WS_O + (rt32 * 32 + head * 2) * 1024);
  stg_sc1(o1, l16, ws, WS_O + (rt32 * 32 + head * 2 + 1) * 1024);
  drain();
  flag_set(ws, lane == 0 ? WS_FO + (rt32 * 16 + head) * 128 : OOB, tag);
}
DI void dump_tile(const f32x16 (&acc)[2][2], int part, int lane) {
  const u32 a = part * 17408 + (lane & 31) * 272 + (lane >> 5) * 16;
#pragma unroll
  for (int c = 0; c < 2; ++c)
#pragma unroll
    for (int t = 0; t < 2; ++t)
#pragma unroll
      for (int g4 = 0; g4 < 4; ++g4)
        sts128(a + t * 32 * 272 + (c * 32 + g4 * 8) * 4,
               __builtin_bit_cast(u32x4, f32x4{acc[c][t][4 * g4], acc[c][t][4 * g4 + 1], acc[c][t][4 * g4 + 2], acc[c][t][4 * g4 + 3]}));
  asm volatile("s_waitcnt lgkmcnt(0)\n\ts_barrier" ::: "memory");
}
DI void store_q(const f32x16 (&acc)[4], srd_t ws, u32 soff, int lane) {
#pragma unroll
  for (int c = 0; c < 8; ++c) stg_sc1(pack8(acc[c >> 1], c & 1), lane * 16, ws, soff + c * 1024);
}

template <int PE> DI void epi_h(const f32x16 (&acc)[4], srd_t ws, u32 soff, int lane) {
#pragma unroll
  for (int c = 0; c < 4; ++c) {
    float hv[8];
#pragma unroll
    for (int e = 0; e < 8; ++e) {
      const float x = acc[c][e];
      const float t = __builtin_amdgcn_exp2f(x * __builtin_fmaf(x * x, -0.10294323970f, -2.30220819814f));
      hv[e] = x * __builtin_amdgcn_rcpf(1.f + t) * acc[c][e + 8];
    }
    u32x4 hp = u32x4{cvtpk(hv[0], hv[1]), cvtpk(hv[2], hv[3]), cvtpk(hv[4], hv[5]), cvtpk(hv[6], hv[7])};
    if (PE != 3) { asm volatile("s_setprio 3" : "+v"(hp)); }
    stg_sc1(hp, lane * 16, ws, soff + c * 1024);
    if (PE != 3) asm volatile("s_setprio %0" ::"n"(PE));
  }
}

template <bool P1, bool isH> DI void body(const Params& p, const int wave) {
  const int tid = threadIdx.x, lane = tid & 63;
  const int w = wave & 3;
  const int b = blockIdx.x, xcd = b & 7, bi = b >> 3;
  const int m = 8 * (xcd >> 2) + (bi & 7), sl = (xcd & 1) * 4 + (bi >> 3), half = (xcd >> 1) & 1;
  const int tl = half * 56 + (xcd & 1) * 32 + bi, rt64 = 16 * (xcd >> 2) + tl / 7, ct2 = tl % 7;
  const int j = 7 * half + sl;
  const int kvhead = 8 * half + sl;
  const int h = lane >> 5;
  const u32 r = lane & 31, l4 = (lane & 3) * 4, l16 = lane * 16;
  const u32 cb = (lane >> 4) & 1, q = (lane & 15) >> 2, pp = lane & 3;
  const srd_t ws = mksrd(p.ws, WS_END), sqkv = mksrd(p.wqkv, D * 3 * D * 2);
  const int rt32 = m * 4 + w;
  STAMP(0);

  if (P1) __builtin_amdgcn_s_setprio(PRIO0);
  const u32 zm = L_Z + wave * 512;
  u32 z1 = L_Z + (lane & 3) * 512 + 256, z2 = z1 + 2048, oob = OOB;
  OPAQUE(z1); OPAQUE(z2); OPAQUE(oob);
  Sync y{lane == 0 ? L_CNT + w * 4 : L_DUM + lane * 4, L_CNT + l4, 1u};
  OPAQUE(y.pa); OPAQUE(y.ra); OPAQUE(y.one);
  u32x2 ev;
  asm volatile("buffer_load_dwordx2 %0, off, %1, 0 sc1" : "=v"(ev) : "s"(ws) : "memory");

  if (P1) {
    u32 fvo;
    {
      const u32 scr = 8 * 16384 + w * 4096;
      const int g = b * 4 + w < 1008 ? b * 4 + w : 1007;
      const int cw = g / 36, job = g - cw * 36, ct = cw >> 2, w2 = cw & 3;
      const bool isd = job < 28;
      const int row0 = isd ? ((job >= 14 ? 42 + job : job) + w2 * 14) * 16 : (w2 * 4 + ((job - 28) >> 1)) * 28 + (job & 1) * 16;
      const srd_t sb = mksrd(isd ? p.wd : p.wo, isd ? D * DF * 2 : D * D * 2);
      const u32 brow = lane >> 3, bvo = brow * 896 + ((lane & 7) ^ (((brow >> 1) & 1) * 4)) * 16;
      dma_buf(scr, bvo, sb, row0 * 896 + ct * 128);
      dma_buf(scr + 1024, bvo, sb, (row0 + 8) * 896 + ct * 128);
      const int km = m * 4 + w;
      dma_buf(scr + 2048, km < 56 ? (lane >> 2) * 2688 + (lane & 3) * 16 : OOB, sqkv, (km >> 1) * 16 * 2688 + (1 + (km & 1)) * 896 + kvhead * 56);
      u32x4 gwf;
      asm volatile("buffer_load_dwordx4 %0, %1, %2, %3 offen" : "=v"(gwf) : "v"(r == 30 ? h * 16 : OOB), "s"(mksrd(p.gw, D * 2)), "s"((km >> 1) * 32) : "memory");
      set_m0(L_K3 + w * 1024);
      {
        const srd_t sa = mksrd(w < 2 ? p.wg : p.wu, D * DF * 2);
        const u32 vo = (((xcd & 1) && r >= 24) || !((PFM >> (w & 1)) & 1)) ? OOB : h * 3584 + r * 16;
        const u32 so = ((xcd >> 2) * 224 + bi * 7) * 3584 + ((w & 1) * 14 + half * 7 + (xcd & 1) * 4) * 128;
#pragma unroll
        for (int i = 0; i < 4; ++i) dma_off<0>((i == 3 && h) ? OOB : vo, sa, so + i * 2 * 3584);
        const srd_t sx = mksrd(p.x, S * D * 2);
        dma_off<0>((PFM & 4) ? l16 : OOB, sx, (xcd * 224 + w * 64 + bi) * 1024);
        dma_off<0>((w == 3 || !(PFM & 4)) ? OOB : l16, sx, (xcd * 224 + w * 64 + 32 + bi) * 1024);
        dma_off<0>((PFM & 8) && (xcd & 1) && !(w == 3 && h) ? h * 2688 + r * 16 : OOB, sqkv, ((xcd >> 2) * 224 + bi * 7 + w * 2) * 2688 + half * 384);
      }
      __builtin_amdgcn_s_setprio(3);
      asm volatile("s_waitcnt vmcnt(7)" : "+v"(ev), "+v"(gwf)::"memory");
#pragma unroll
      for (int c = 0; c < 2; ++c) {
        const u32 a = scr + (4 * h + q) * 128 + ((c ^ ((q >> 1) & 1)) * 64) + cb * 32 + pp * 8;
        stg_sc1(trfrag(a, a + 1024), l16, ws, WS_BT + (g * 2 + c) * 1024);
      }
      u32x4 kt;
      if (w & 1) {
        const u32 a = scr + 2048 + (8 * h + q) * 64 + cb * 32 + 8 * pp;
        kt = trfrag(a, a + 256);
        if (r >= 28) kt = gwf;
      } else {
        z_wait<0>(z2);
        u32x4 wp[2]; load_wphi(wp, L_WPHI, lane);
        const u32 a = scr + 2048 + ((lane & 3) + ((lane >> 1) & 4) + ((lane << 1) & 8)) * 64 + h * 8;
        f32x16 t;
#pragma unroll
        for (int e = 0; e < 16; ++e) t[e] = 0.f;
#pragma unroll
        for (int s = 0; s < 2; ++s) {
          const u32x2 lo = *(LAS u32x2*)(size_t)(a + s * 32), hi = *(LAS u32x2*)(size_t)(a + s * 32 + 16);
          t = mfma(u32x4{lo.x, lo.y, hi.x, hi.y}, wp[s], t);
        }
#pragma unroll
        for (int e = 0; e < 8; ++e) t[e] *= LOG2E;
        kt = pack8(t, 0);
      }
      stg_sc1(kt, km < 56 ? l16 : OOB, ws, WS_KT + (kvhead * 56 + km) * 1024);
      fvo = lane == 0 ? WS_FB + (cw * 64 + job) * 8 : (lane == 1 && km < 56) ? WS_FK + (kvhead * 64 + km) * 8 : OOB;
    }
    const u64 tag = (((u64)rfl(ev.y) << 32) | rfl(ev.x)) + 1;
    STAMP(1);
    u32x4 xf[28];
    f32x16 acc[4];
    u32 done = 0;
    set_m0(zm);
    z_post<0>(oob, ws);
    for (int it = 0; it < 100000 && !done; ++it) {
      if (__builtin_amdgcn_ballot_w64(z_peek<0>(z2) != 0u) == 0) break;
      flag_try<false, 1>(done, ws, fvo, tag);
    }
    unit_gemm<isH, true>(xf, acc, lane, w, z2, [&](auto gc) {
      constexpr int s = decltype(gc)::v >> 4, n = decltype(gc)::v & 15;
      if constexpr (n == 15) z_post<1 + s>(oob, ws);
      if constexpr (s < 3 && (n == 3 || n == 9)) flag_try<s == 2 && n == 9, 1 + s>(done, ws, fvo, tag);
    } STAMP_PASS);
    STAMP(2);
    __builtin_amdgcn_s_setprio(isH ? PRIOE : 3);
    if (isH) epi_h<PRIOE>(acc, ws, WS_H + (rt32 * 112 + j * 4) * 1024, lane); else store_q(acc, ws, WS_Q + (rt32 * 32 + half * 16) * 1024, lane);
    __builtin_amdgcn_s_setprio(PRIO1);
    STAMP(3);
    done = 0;
    fvo = lane != 0 ? OOB : isH ? WS_FH + (rt32 * 16 + j) * 128 : WS_FQ + (rt32 * 8 + half * 2) * 128;
    u64 fq = 0;
    const u32 fqa = WS_FQ + (rt32 * 8 + (h ? 4 : kvhead >> 2)) * 128;
    u64 fb = 0;
    const u32 fba = WS_FB + ((ct2 * 4 + w) * 64 + 28 + (lane & 7)) * 8;
    unit_gemm<isH, false>(xf, acc, lane, w, z2, [&](auto gc) {
      constexpr int s = decltype(gc)::v >> 4, n = decltype(gc)::v & 15;
      if constexpr (s < 4 && (n == 3 || n == 9)) flag_try<s == 3 && n == 9, 0>(done, ws, fvo, tag);
      if constexpr (s == 5 && n == 3 && isH) flag_peek(fb, ws, fba);
      if constexpr (s == 5 && n == 6) flag_peek(fq, ws, fqa);
    } STAMP_PASS);
    lds_post<C_PROG>(y);
    STAMP(4);
    __builtin_amdgcn_s_setprio(3);
    u32x4 bo[8][2];
    task_o<isH ? 4 : 8, 0>(kvhead, rt32, ws, tag, lane, fq, fqa, fvo + (isH ? 64 * 16 * 128 : 128), [&]() {
      if (isH) {
        epi_h<3>(acc, ws, WS_H + (rt32 * 112 + (14 + j) * 4) * 1024, lane);
      } else store_q(acc, ws, WS_Q + (rt32 * 32 + half * 16 + 8) * 1024, lane);
    }, [&]() {
      if (isH) {
        flag_check<2>(fb, ws, fba, tag);
        sfor<16>([&](auto ic) { constexpr int i = decltype(ic)::v; ldg_sc1o<(i & 3) * 1024>(bo[i >> 1][i & 1], l16, ws, WS_BT + ((ct2 * 4 + w) * 36 + 28) * 2048 + (i >> 2) * 4096); });
        VMWAIT(16);
      } else VMWAIT(0);
    } STAMP_PASS);
    STAMP(10);
    if (!isH) STAMP_FLUSH();
    if (isH) {
      flag_wait(ws, WS_FO + ((rt64 * 2 + h) * 16 + w * 4 + ((lane >> 3) & 3)) * 128, tag);
      STAMP(14);
      u32x4 ao[8][2];
      u32 gu;
      asm volatile("buffer_load_dword %0, %1, %2, %3 offen sc1" : "=v"(gu) : "v"(lane * 4), "s"(ws), "s"(WS_G + rt64 * 256) : "memory");
      const u32 l16o = l16 + 32 * 1024;
      sfor<16>([&](auto ic) {
        constexpr int i = decltype(ic)::v;
        ldg_sc1o<((i >> 1) & 3) * 1024>(ao[i >> 1][i & 1], (i & 1) ? l16o : l16, ws, WS_O + (rt64 * 2 * 32 + w * 8) * 1024 + (i >> 3) * 4096);
      });
      lds_wait<C_PROG, 1>(y);
      asm volatile("s_waitcnt vmcnt(16)" : "+v"(gu)::"memory");
      *(LAS float*)(size_t)(L_GW + lane * 4) = 1.f - sigmoid(u2f(gu));
      f32x16 oc[2][2];
#pragma unroll
      for (int i = 0; i < 4; ++i)
#pragma unroll
        for (int e = 0; e < 16; ++e) oc[i >> 1][i & 1][e] = 0.f;
      sfor<8>([&](auto nc) {
        constexpr int n = decltype(nc)::v;
        asm volatile("s_waitcnt vmcnt(%4)" : "+v"(ao[n][0]), "+v"(ao[n][1]), "+v"(bo[n][0]), "+v"(bo[n][1]) : "n"(14 - 2 * n) : "memory");
#pragma unroll
        for (int u = 0; u < 4; ++u) oc[u >> 1][u & 1] = mfma(bo[n][u >> 1], ao[n][u & 1], oc[u >> 1][u & 1]);
        SB();
        asm volatile("" : "+v"(oc[0][0]), "+v"(oc[0][1]), "+v"(oc[1][0]), "+v"(oc[1][1]));
      });
      STAMP(15);
      dump_tile(oc, 4 + w, lane);
      flag_set(ws, (b | w | lane) ? OOB : WS_E, tag);
    }
  } else {
    using T = TB<isH>;
    const srd_t sx = mksrd(p.x, S * D * 2);
    {
      const srd_t sp = mksrd(p.wphi, 16 * 1568);
      if (w < 2) dma_buf(L_WPHI + w * 1024, l16, sp, kvhead * 1568 + w * 1024);
    }
    constexpr u32 wrs = isH ? 3584 : 2688, wu2 = isH ? 14 * 128 : 4 * 56;
    u32 xvo[4], wvo[4];
#pragma unroll
    for (int i = 0; i < 4; ++i) {
      xvo[i] = (lane >> 3) * 896 + (((lane & 7) ^ ((4 * i + (lane >> 4)) & 7)) * 16) + i * (8 * 896 - 1024);
      if (isH) wvo[i] = (lane >> 3) * 3584 + ((((lane >> 1) & 3) ^ (((lane >> 3) & 2) | (w >> 1))) * 32) + (lane & 1) * 16 + i * (8 * 3584 - 1024);
      else     wvo[i] = (lane >> 4) * 2688 + ((((lane >> 1) & 3) ^ (lane >> 4)) * 56) + ((lane >> 3) & 1) * 32 + (lane & 1) * 16 + i * (4 * 2688 - 1024);
    }
    const srd_t sw = isH ? mksrd(w < 2 ? p.wg : p.wu, D * DF * 2) : sqkv;
    const u32 xso = (m * 128 + w * 32) * 896;
    const u32 wso1 = isH ? (w & 1) * 32 * 3584 + j * 128 : w * 16 * 2688 + 8 * half * 56;
    const u32 mq = w * 4096, mq2 = w * 2048;
    const u32 kso4 = WS_KT + kvhead * 56 * 1024 + mq, kso2 = WS_KT + kvhead * 56 * 1024 + mq2;
    const u32 l16h = l16 + 112 * 1024;
    const u32 hso = WS_H + (rt64 * 2 * 112 + w * 14) * 1024, bso = WS_BT + (ct2 * 4 + w) * 36 * 2048;
    auto fadr = [&](int k) -> u32 {
      return k == 0 ? WS_FK + (kvhead * 64 + (lane < 56 ? lane : 0)) * 8 : k == 1 ? WS_FB + ((ct2 * 4 + w) * 64 + (lane < 28 ? lane : 0)) * 8
           : WS_FH + (((k - 2) * 64 + rt64 * 2 + h) * 16 + ((w * 14) >> 2) + ((lane >> 3) & 3)) * 128;
    };
    u32 so = 0, pre = 0, fa = 0;
    u64 fv = 0, tag = 0;
    u32x4 xf[28], af[AW + 1][2], bf[14][2], kvs;
    const u32 xb = w * 4096 + r * 128 + ((h ^ ((r >> 1) & 7)) * 16);
    auto op = [&](auto kc) {
      constexpr int K = decltype(kc)::v;
      constexpr Op o = T::t.v[K];
      if constexpr (T::need(K) >= 0) z_wait_pre<T::need(K)>(z1, pre);
      if constexpr (o.kind == OU && o.idx == 6 && o.i == 0) z_wait<zidx(OU, 2)>(z2);
      constexpr int chk = o.i != 0 ? -1 : (o.kind == OK && o.idx == 0) ? 0 : (o.kind == OB && o.idx == 0) ? 1 : (o.kind == OA && o.idx == 0) ? 2
                        : (o.kind == OA && o.idx == 14) ? 3 : -1;
      if constexpr (chk >= 0) { flag_check<K - 1 - T::find(OPEEK, chk, 0)>(fv, ws, fa, tag); STAMP(11 + chk); }
      if constexpr (o.kind <= OK) {
        constexpr u32 sb = slot_of(o.kind, o.idx) * 16384;
        if constexpr (o.i == 0) {
          if constexpr (o.kind == OX) so = dma_open<sb, o.idx * 128>(mq, xso);
          else if constexpr (o.kind == OK) so = dma_open<sb, o.idx * 16384>(o.idx < 3 ? mq : mq2, o.idx < 3 ? kso4 : kso2);
          else so = dma_open<sb, o.idx * 64 * wrs + (o.kind == OW ? 0u : wu2)>(mq, wso1);
        }
        if constexpr (o.kind == OK) dma_off_sc1<o.i * 1024>(l16, ws, so);
        else dma_off<o.i * 1024>(o.kind == OX ? xvo[o.i] : wvo[o.i], o.kind == OX ? sx : sw, so);
      } else if constexpr (o.kind == OZ) { set_m0(zm); z_post<o.idx>(oob, ws); }
      else if constexpr (o.kind == OPEEK) { fa = fadr(o.idx); flag_peek(fv, ws, fa); }
      else if constexpr (o.kind == OB) ldg_sc1o<(o.idx & 1) * 2048 + o.i * 1024>(bf[o.idx % 14][o.i], l16, ws, bso + (o.idx >> 1) * 4096);
      else if constexpr (o.kind == OA) {
        constexpr int ks = o.idx >= 14 ? o.idx + 42 : o.idx;
        ldg_sc1o<(ks & 3) * 1024>(af[o.idx % (AW + 1)][o.i], o.i ? l16h : l16, ws, hso + (ks >> 2) * 4096);
      } else if constexpr (o.kind == OST) stg_sc1(kvs, l16, ws, WS_KVP + ((kvhead * 16 + m) * 4 + w) * 1024);
      else if constexpr (o.kind == OFLAG) { VMWAIT(K - 1 - T::find(OST, 0, 0)); flag_set(ws, lane == 0 ? WS_FP + ((kvhead * 4 + w) * 16 + m) * 128 : OOB, tag); }
      if constexpr (T::need(K + 3) >= 0) pre = z_peek<T::need(K + 3)>(z1);
      constexpr int J = K - LAG;
      if constexpr (J >= 0) {
        constexpr Op oj = T::t.v[J];
        if constexpr (oj.kind == OX && oj.i == 3) {
          if constexpr (J == 3) {
            asm volatile("s_waitcnt vmcnt(%1)" : "+v"(ev) : "n"(LAG) : "memory");
            tag = (((u64)rfl(ev.y) << 32) | rfl(ev.x)) + 1;
          } else VMWAIT(LAG);
#pragma unroll
          for (int kk = 0; kk < 4; ++kk) xf[4 * oj.idx + kk] = lds128(slot_of(OX, oj.idx) * 16384 + (xb ^ (kk * 32)));
          FSTAMP(16 + oj.idx);
        }
      }
      SB();
    };
    auto ops = [&](auto ac, auto bc) { sfor<decltype(bc)::v - decltype(ac)::v>([&](auto kc) { op(IC<decltype(ac)::v + decltype(kc)::v>{}); }); };
#define OPS(a, b) ops(IC<(a)>{}, IC<(b)>{})
#define MK(m) (T::t.mark[m])
#define TICK(M, n, N) OPS(MK(M) + (MK((M) + 1) - MK(M)) * (n) / (N), MK(M) + (MK((M) + 1) - MK(M)) * ((n) + 1) / (N))
#define ETICK(n) OPS(T::etick(n), T::etick((n) + 1))
    f32x16 ka, va;
#pragma unroll
    for (int e = 0; e < 16; ++e) { ka[e] = 0.f; va[e] = r == 28 ? 1.f : 0.f; }
    __builtin_amdgcn_s_setprio(PRIO2);
    OPS(0, MK(M_KV0));
    STAMP(1);
    {
      u32 ak[4];
#pragma unroll
      for (int t = 0; t < 4; ++t) { ak[t] = slot_of(OK, t) * 16384 + l16; OPAQUE(ak[t]); }
      u32x4 fk[3], fw[3];
      u32 pk = 0;
      auto reads = [&](int ks) { fk[ks % 3] = lds128(ak[ks >> 3] + (ks & 7) * 2048); fw[ks % 3] = lds128(ak[ks >> 3] + (ks & 7) * 2048 + 1024); };
      z_wait<zidx(OK, 0)>(z2);
      reads(0); reads(1); reads(2);
      SB();
      sfor<28>([&](auto kc) {
        constexpr int ks = decltype(kc)::v, t = ks >> 3;
        ka = mfma(fk[ks % 3], xf[ks], ka);
        SB();
        asm volatile("" : "+v"(ka));
        TICK(M_KV0, ks, 28);
        va = mfma(xf[ks], fw[ks % 3], va);
        SB();
        asm volatile("" : "+v"(va));
        if constexpr ((ks & 7) == 3 && t < 3) pk = z_peek<zidx(OK, t + 1)>(z2);
        if constexpr ((ks & 7) == 5) z_wait_pre<zidx(OK, t + 1)>(z2, pk);
        if constexpr (ks + 3 < 28) reads(ks + 3);
        SB();
      });
    }
    STAMP(3);
    {
      ETICK(0);
      u32x4 vf[2] = {pack8(va, 0), pack8(va, 1)};
      asm volatile("" : "+v"(vf[0]), "+v"(vf[1]));
      ETICK(1);
      float mx = max3(max3(max3(ka[0], ka[1], ka[2]), max3(ka[3], ka[4], ka[5]), max3(ka[6], ka[7], ka[8])),
                      max3(max3(ka[9], ka[10], ka[11]), max3(ka[12], ka[13], ka[14]), ka[15]), ka[15]);
      asm volatile("" : "+v"(mx));
      ETICK(2);
      float mn = min3(min3(min3(ka[0], ka[1], ka[2]), min3(ka[3], ka[4], ka[5]), min3(ka[6], ka[7], ka[8])),
                      min3(min3(ka[9], ka[10], ka[11]), min3(ka[12], ka[13], ka[14]), ka[15]), ka[15]);
      asm volatile("" : "+v"(mn));
      ETICK(3);
      { float a, c; both_halves(mx, a, c); mx = max3(a, c, c); both_halves(mn, a, c); mn = min3(a, c, c); }
      asm volatile("" : "+v"(mx), "+v"(mn));
      ETICK(4);
      constexpr u32 L_PHI = slot_of(OK, 1) * 16384, L_VF = slot_of(OK, 2) * 16384;
      const u32 wa = L_PHI + w * 4096 + r * 64, sw8 = (h ^ ((r >> 1) & 7)) * 8;
      const float keep = h ? 0.f : 1.f;
      u32x2 ph[2][4];
      sfor<2>([&](auto sc) {
        constexpr int sg = decltype(sc)::v, E0 = 5 + 9 * sg;
        f32x16 e;
        float ps[4];
        sfor<4>([&](auto gc) {
          constexpr int g4 = decltype(gc)::v;
#pragma unroll
          for (int i = 4 * g4; i < 4 * g4 + 4; ++i) e[i] = __builtin_amdgcn_exp2f(sg == 0 ? ka[i] - mx : mn - ka[i]);
          ps[g4] = (e[4 * g4] + e[4 * g4 + 1]) + (e[4 * g4 + 2] + e[4 * g4 + 3]);
          asm volatile("" : "+v"(ps[g4]));
          ETICK(E0 + g4);
        });
        float a, c; both_halves(__builtin_fmaf(ps[3], keep, ps[2]) + (ps[0] + ps[1]), a, c);
        float rs = __builtin_amdgcn_rcpf(a + c);
        asm volatile("" : "+v"(rs));
        ETICK(E0 + 4);
        if constexpr (sg == 1) pre = z_peek<zidx(OU, 6)>(z2);
        sfor<4>([&](auto gc) {
          constexpr int g4 = decltype(gc)::v;
          ph[sg][g4] = u32x2{cvtpk(e[4 * g4] * rs, e[4 * g4 + 1] * rs), cvtpk(e[4 * g4 + 2] * rs, e[4 * g4 + 3] * rs)};
          asm volatile("" : "+v"(ph[sg][g4]));
          ETICK(E0 + 5 + g4);
        });
      });
      z_wait_pre<zidx(OU, 6)>(z2, pre);
      sts128(L_VF + w * 2048 + l16, vf[0]); sts128(L_VF + w * 2048 + 1024 + l16, vf[1]);
      ETICK(23);
#pragma unroll
      for (int i = 0; i < 8; ++i) sts64(wa + (i >> 2) * 2048 + (sw8 ^ ((i & 3) * 16)), ph[i >> 2][i & 3]);
      ETICK(24);
      lds_post<C_SYNC>(y);
      ETICK(25); ETICK(26);
      lds_wait<C_SYNC, 1>(y);
      const u32 t0 = 4 * h + q, ra = L_PHI + (w >> 1) * 2048 + t0 * 64;
      u32 a0 = ra + (((4 * cb + pp) ^ (t0 >> 1)) * 8), a1 = ra + 512 + (((4 * cb + pp) ^ ((t0 >> 1) + 4)) * 8), av = L_VF + l16;
      OPAQUE(a0); OPAQUE(a1); OPAQUE(av);
      f32x16 kp;
#pragma unroll
      for (int e = 0; e < 16; ++e) kp[e] = 0.f;
      sfor<2>([&](auto uc) {
        constexpr int u2 = decltype(uc)::v;
        u32x4 pf[4], vv[4];
#pragma unroll
        for (int i = 0; i < 4; ++i) {
          const int k8 = u2 * 4 + i, off = (k8 >> 1) * 4096 + (k8 & 1) * 1024;
          pf[i] = trfrag(a0 + off, a1 + off); vv[i] = lds128(av + k8 * 1024);
        }
        SB();
        ETICK(27 + u2);
#pragma unroll
        for (int i = 0; i < 4; ++i) kp = mfma(pf[i], vv[i], kp);
        asm volatile("" : "+v"(kp));
      });
      if (w & 1) kvs = pack8(kp, 1); else kvs = pack8(kp, 0);
      OPS(MK(M_ST), MK(M_ST) + 1);
    }
    STAMP(4);
    if constexpr (!isH) {
#pragma unroll
      for (int g4 = 0; g4 < 4; ++g4)
        stg_sc1(__builtin_bit_cast(u32x4, f32x4{va[4 * g4], va[4 * g4 + 1], va[4 * g4 + 2], va[4 * g4 + 3]}), (half || r != 30) ? OOB : h * 16, ws, WS_G + rt32 * 128 + g4 * 32);
      VMWAIT(0);
      flag_set(ws, lane == 0 ? WS_FP + ((kvhead * 4 + w) * 16 + m) * 128 : (lane == 1 && !half) ? WS_FQ + (rt32 * 8 + 4) * 128 : OOB, tag);
      const int qw = (m * 2 + half) * 4 + w, hd = qw >> 3, pc = qw & 7;
      flag_wait(ws, WS_FP + ((hd * 4 + (pc >> 1)) * 16 + (lane >> 2)) * 128, tag);
      STAMP(6);
      u32x4 pv[8];
#pragma unroll
      for (int i = 0; i < 8; ++i) ldg_sc1(pv[i], h * 4096 + r * 16, ws, WS_KVP + (hd * 16 + 2 * i) * 4096 + pc * 512);
      asm volatile("s_waitcnt vmcnt(0)" : "+v"(pv[0]), "+v"(pv[1]), "+v"(pv[2]), "+v"(pv[3]), "+v"(pv[4]), "+v"(pv[5]), "+v"(pv[6]), "+v"(pv[7])::"memory");
      STAMP(8);
      u32x4 o;
#pragma unroll
      for (int e = 0; e < 4; ++e) {
        float lo[8], hi[8];
#pragma unroll
        for (int i = 0; i < 8; ++i) { lo[i] = u2f(pv[i][e] << 16); hi[i] = u2f(pv[i][e] & 0xffff0000u); }
        float a, c, d, f;
        both_halves(((lo[0] + lo[1]) + (lo[2] + lo[3])) + ((lo[4] + lo[5]) + (lo[6] + lo[7])), a, c);
        both_halves(((hi[0] + hi[1]) + (hi[2] + hi[3])) + ((hi[4] + hi[5]) + (hi[6] + hi[7])), d, f);
        o[e] = cvtpk(a + c, d + f);
        if (r == 29) o[e] = 0x3f803f80u;
        if (e >= 2 && (pc & 3) == 3) o[e] = 0u;
      }
      stg_sc1(o, h ? OOB : l16, ws, WS_KVF + hd * 4096 + pc * 512);
      VMWAIT(0);
      flag_set(ws, lane == 0 ? WS_FF + (hd * 8 + pc) * 128 : OOB, tag);
      STAMP(7);
      STAMP_FLUSH();
    } else {
      f32x16 acc[2][2];
#pragma unroll
      for (int i = 0; i < 4; ++i)
#pragma unroll
        for (int e = 0; e < 16; ++e) acc[i >> 1][i & 1][e] = 0.f;
      OPS(MK(M_ST) + 1, MK(M_JOB));
      __builtin_amdgcn_s_setprio(PRIO3);
      sfor<28>([&](auto nc) {
        constexpr int n = decltype(nc)::v, P = MK(M_JOB + n), NL = MK(M_JOB + n + 1) - P;
        constexpr int need = cmax(T::find(OA, n, 1), T::find(OB, n, 1));
        static_assert(need < P, "stream order");
        asm volatile("s_waitcnt vmcnt(%4)" : "+v"(af[n % (AW + 1)][0]), "+v"(af[n % (AW + 1)][1]), "+v"(bf[n % 14][0]), "+v"(bf[n % 14][1]) : "n"(P - 1 - need) : "memory");
        sfor<4>([&](auto uc) {
          constexpr int u = decltype(uc)::v, c = u >> 1, t = u & 1;
          acc[c][t] = mfma(bf[n % 14][c], af[n % (AW + 1)][t], acc[c][t]);
          SB();
          asm volatile("" : "+v"(acc[c][t]));
          OPS(P + NL * u / 4, P + NL * (u + 1) / 4);
        });
        if constexpr (n == 12) STAMP(6);
      });
      STAMP(10);
      lds_wait<C_PROG, 1>(y);
      dump_tile(acc, w, lane);
    }
  }
  if (isH) {
    const int tok = tid >> 3, pc = tid & 7;
    f32x4 s0 = {0.f, 0.f, 0.f, 0.f}, s1 = s0;
#pragma unroll
    for (int u = 0; u < 4; ++u) {
      s0 += __builtin_bit_cast(f32x4, lds128(u * 17408 + tok * 272 + pc * 32));
      s1 += __builtin_bit_cast(f32x4, lds128(u * 17408 + tok * 272 + pc * 32 + 16));
    }
    const float omg = *(LAS float*)(size_t)(L_GW + tok * 4);
    s0 *= omg; s1 *= omg;
#pragma unroll
    for (int u = 4; u < 8; ++u) {
      s0 += __builtin_bit_cast(f32x4, lds128(u * 17408 + tok * 272 + pc * 32));
      s1 += __builtin_bit_cast(f32x4, lds128(u * 17408 + tok * 272 + pc * 32 + 16));
    }
    *(u32x4*)(p.out + (rt64 * 64 + tok) * 896 + ct2 * 128 + pc * 16) = u32x4{cvtpk(s0.x, s0.y), cvtpk(s0.z, s0.w), cvtpk(s1.x, s1.y), cvtpk(s1.z, s1.w)};
    STAMP(9);
    STAMP_FLUSH();
  }
}

__global__ __launch_bounds__(512, 2) void fused(Params p) {
  asm volatile(".subsection 2\n\t.fill 256, 4, 0xBF800000\n\t.subsection 0");
  const int wave = rfl(threadIdx.x >> 6);
  *(LAS u32*)(size_t)(L_Z + (threadIdx.x >> 6) * 512 + 256 + (threadIdx.x & 63) * 4) = 1u;
  *(LAS u32*)(size_t)(L_CNT + (threadIdx.x & 15) * 4) = 0u;
  asm volatile("s_waitcnt lgkmcnt(0)\n\ts_barrier" ::: "memory");
  if (!((blockIdx.x & 1) && blockIdx.x >= 192)) {
    if (wave < 4) body<true, true>(p, wave); else body<false, true>(p, wave);
  } else {
    if (wave < 4) body<true, false>(p, wave); else body<false, false>(p, wave);
  }
}
}
#if defined(__HIP_DEVICE_COMPILE__)
#pragma clang attribute pop
#endif

extern "C" void kernel_launch(void* const* d_in, const int* in_sizes, int n_in, void* d_out, int out_size, void* d_ws, size_t ws_size,
                              hipStream_t stream) {
  hk::Params p;
  p.x = (const char*)d_in[0]; p.wqkv = (const char*)d_in[1]; p.wphi = (const char*)d_in[2]; p.wo = (const char*)d_in[3];
  p.wg = (const char*)d_in[5]; p.wu = (const char*)d_in[6]; p.wd = (const char*)d_in[7]; p.gw = (const char*)d_in[8];
  p.out = (char*)d_out; p.ws = (char*)d_ws;
  (void)hipFuncSetAttribute((const void*)hk::fused, hipFuncAttributeMaxDynamicSharedMemorySize, hk::L_TOTAL);
  hk::fused<<<256, 512, hk::L_TOTAL, stream>>>(p);
}
```

```cpp
#include <hip/hip_runtime.h>
#include <stdint.h>

#define NOWAIT 0
#define SLEEPN "1"
#define PRIO0 2
#define PRIOE 3
#define NOBEPI 0
#define PFM 15
#define KVT 28
#define T2S 3
#define QAS 4
#define PRIOQ 3
#define PAUSE 0
#define LW 7
#define PRIOF 3
#define PSL 2
#define QPK -1
#define PRED 8
#define OSL 8
#define ODIR 4
#define OUTSC 0
#define PRIO2 2
#define PRIO1 1
#define PRIO3 1
#define REDAT 1
#define STAMPS 0

#if defined(__HIP_DEVICE_COMPILE__)
#pragma clang attribute push(__attribute__((target("no-packed-fp32-ops"))), apply_to = function)
#endif
namespace hk {
typedef unsigned u32;
typedef unsigned long long u64;
typedef __bf16 bf16x8 __attribute__((ext_vector_type(8)));
typedef __bf16 bf16x2 __attribute__((ext_vector_type(2)));
typedef short s16x4 __attribute__((ext_vector_type(4)));
typedef short s16x8 __attribute__((ext_vector_type(8)));
typedef u32 u32x4 __attribute__((ext_vector_type(4)));
typedef u32 u32x2 __attribute__((ext_vector_type(2)));
typedef float f32x16 __attribute__((ext_vector_type(16)));
typedef float f32x4 __attribute__((ext_vector_type(4)));
typedef float f32x2 __attribute__((ext_vector_type(2)));
typedef __amdgpu_buffer_rsrc_t srd_t;
#define LAS __attribute__((address_space(3)))
#define DI __device__ __forceinline__

constexpr int S = 2048, D = 448, DF = 1792;
constexpr u32 WS_E = 0;
constexpr u32 WS_FK = 4096;
constexpr u32 WS_FQ = 131072;
constexpr u32 WS_FH = 393216;
constexpr u32 WS_KVF = 655360;
constexpr u32 WS_KVP = 720896;
constexpr u32 WS_O = WS_KVP + 16 * 16 * 4096;
constexpr u32 WS_Q = WS_O + 64 * 32 * 1024;
constexpr u32 WS_H = WS_Q + 64 * 32 * 1024;
constexpr u32 WS_KT = WS_H + 64 * 112 * 1024;
constexpr u32 WS_END = WS_KT + 16 * 56 * 1024;
constexpr u32 WS_STAMP = 24u << 20;
constexpr u32 OOB = 0x7ff00000u;
constexpr u32 L_K3 = 9 * 16384;
constexpr u32 L_WPHI = L_K3 + 8192;
constexpr u32 L_GW = L_WPHI + 2048;
constexpr u32 L_Z = L_GW + 1024;
constexpr u32 L_CNT = L_Z + 4096;
constexpr u32 L_DUM = L_CNT + 64;
constexpr u32 L_TOTAL = 163840;
enum { C_PROG = 0, C_SYNC = 16, C_KVF = 32 };
constexpr u32 L_KVF = 143360;

struct Params {
  const char *x, *wqkv, *wphi, *wo, *wg, *wu, *wd, *gw;
  char *out, *ws;
};

DI u32 f2u(float f) { return __builtin_bit_cast(u32, f); }
DI float u2f(u32 u) { return __builtin_bit_cast(float, u); }
DI u32 min(u32 a, u32 b) { return a < b ? a : b; }
DI u32 max(u32 a, u32 b) { return a > b ? a : b; }
DI u32 rfl(u32 v) { return __builtin_amdgcn_readfirstlane(v); }
DI srd_t mksrd(const void* p, u32 bytes) { return __builtin_amdgcn_make_buffer_rsrc((void*)p, 0, bytes, 0x00020000); }
DI u32 cvtpk(float lo, float hi) { f32x2 v = {lo, hi}; return __builtin_bit_cast(u32, __builtin_convertvector(v, bf16x2)); }
DI f32x16 mfma(u32x4 a, u32x4 b, f32x16 c) {
  return __builtin_amdgcn_mfma_f32_32x32x16_bf16(__builtin_bit_cast(bf16x8, a), __builtin_bit_cast(bf16x8, b), c, 0, 0, 0);
}
DI u32x2 tr64(u32 addr) { return __builtin_bit_cast(u32x2, __builtin_amdgcn_ds_read_tr16_b64_v4i16((LAS s16x4*)(size_t)addr)); }
DI u32x4 trfrag(u32 a0, u32 a1) { u32x2 lo = tr64(a0), hi = tr64(a1); return u32x4{lo.x, lo.y, hi.x, hi.y}; }
DI u32x4 lds128(u32 addr) { return *(LAS u32x4*)(size_t)addr; }
DI void sts128(u32 addr, u32x4 v) { *(LAS u32x4*)(size_t)addr = v; }
DI void sts64(u32 addr, u32x2 v) { *(LAS u32x2*)(size_t)addr = v; }
#define MEMFENCE() asm volatile("" ::: "memory")
#define VMWAIT(n) asm volatile("s_waitcnt vmcnt(%0)" ::"n"(n) : "memory")
DI void dma_buf(u32 ldsaddr, u32 voff, srd_t srd, u32 soff) {
  asm volatile("s_mov_b32 m0, %0\n\ts_nop 0\n\tbuffer_load_dwordx4 %1, %2, %3 offen lds" ::"s"(ldsaddr), "v"(voff), "s"(srd), "s"(soff) : "memory");
}
template <u32 IMM, u32 SIMM> DI u32 dma_open(u32 base, u32 sbase) {
  u32 so;
  asm volatile("s_add_u32 m0, %1, %2\n\ts_add_u32 %0, %3, %4" : "=s"(so) : "s"(base), "n"(IMM), "s"(sbase), "n"(SIMM) : "scc", "memory");
  return so;
}
template <int OFF> DI void dma_off(u32 voff, srd_t srd, u32 soff) {
  asm volatile("buffer_load_dwordx4 %0, %1, %2 offen offset:%3 lds" ::"v"(voff), "s"(srd), "s"(soff), "n"(OFF) : "memory");
}
template <int OFF> DI void dma_off_sc1(u32 voff, srd_t srd, u32 soff) {
  asm volatile("buffer_load_dwordx4 %0, %1, %2 offen offset:%3 sc1 lds" ::"v"(voff), "s"(srd), "s"(soff), "n"(OFF) : "memory");
}
template <int OFF> DI void ldg_sc1o(u32x4& dst, u32 voff, srd_t srd, u32 soff) {
  asm volatile("buffer_load_dwordx4 %0, %1, %2, %3 offen offset:%4 sc1" : "=v"(dst) : "v"(voff), "s"(srd), "s"(soff), "n"(OFF) : "memory");
}
DI void ldg(u32x4& dst, u32 voff, srd_t srd, u32 soff) {
  asm volatile("buffer_load_dwordx4 %0, %1, %2, %3 offen" : "=v"(dst) : "v"(voff), "s"(srd), "s"(soff) : "memory");
}
template <int BIT, int OFF> DI void ldg2_if(u32x4& a, u32x4& b, u32 mask, u32 voff, srd_t srd, u32 soff) {
  asm volatile("s_bitcmp1_b32 %2, %3\n\ts_cbranch_scc0 .Lsk%=\n\tbuffer_load_dwordx4 %0, %4, %5, %6 offen offset:%7 sc1\n\tbuffer_load_dwordx4 %1, %4, %5, %6 offen offset:%8 sc1\n.Lsk%=:"
               : "+v"(a), "+v"(b) : "s"(mask), "n"(BIT), "v"(voff), "s"(srd), "s"(soff), "n"(OFF), "n"(OFF + 1024) : "scc", "memory");
}
DI void touch(u32& sink, u32 voff, srd_t srd) {
  asm volatile("buffer_load_dword %0, %1, %2, 0 offen" : "+v"(sink) : "v"(voff), "s"(srd) : "memory");
}
DI void ldg_sc1(u32x4& dst, u32 voff, srd_t srd, u32 soff) {
  asm volatile("buffer_load_dwordx4 %0, %1, %2, %3 offen sc1" : "=v"(dst) : "v"(voff), "s"(srd), "s"(soff) : "memory");
}
DI void stg_sc1(u32x4 v, u32 voff, srd_t srd, u32 soff) {
  asm volatile("buffer_store_dwordx4 %0, %1, %2, %3 offen sc1\n\ts_nop 1" ::"v"(v), "v"(voff), "s"(srd), "s"(soff) : "memory");
}
DI void stg64_sc1(u32x2 v, u32 voff, srd_t srd, u32 soff) {
  asm volatile("buffer_store_dwordx2 %0, %1, %2, %3 offen sc1" ::"v"(v), "v"(voff), "s"(srd), "s"(soff) : "memory");
}
DI void stg32_sc1(u32 v, u32 voff, srd_t srd, u32 soff) {
  asm volatile("buffer_store_dword %0, %1, %2, %3 offen sc1" ::"v"(v), "v"(voff), "s"(srd), "s"(soff) : "memory");
}
struct Sync { u32 pa, ra, one; };
template <int C> DI void lds_post(const Sync& y) { asm volatile("ds_add_u32 %0, %1 offset:%2" ::"v"(y.pa), "v"(y.one), "n"(C) : "memory"); }
template <int C> DI u32 lds_peek(const Sync& y) { return *(volatile LAS u32*)(size_t)(y.ra + C); }
template <int C, int T> DI void lds_wait_pre(u32 a, u32 v) {
  u32 cnt;
  asm volatile("v_cmp_gt_u32 vcc, %3, %0\n\ts_cbranch_vccnz .Lsl%=\n.Lse%=:\n\t.subsection 1\n"
               ".Lsl%=:\n\ts_mov_b32 %1, 0x40000\n.Lsp%=:\n\tds_read_b32 %0, %2 offset:%4\n\ts_waitcnt lgkmcnt(0)\n\tv_cmp_gt_u32 vcc, %3, %0\n\ts_cbranch_vccz .Lse%=\n\t"
               "s_sub_u32 %1, %1, 1\n\ts_cbranch_scc0 .Lsp%=\n\ts_branch .Lse%=\n\t.subsection 0"
               : "+v"(v), "=&s"(cnt) : "v"(a), "n"(T), "n"(C) : "vcc", "scc", "memory");
}
template <int C, int T> DI void lds_wait(const Sync& y) { lds_wait_pre<C, T>(y.ra, lds_peek<C>(y)); }
template <int K> DI void z_post(u32 oob, srd_t srd) {
  asm volatile("buffer_load_dword %0, %1, 0 offen offset:%2 lds" ::"v"(oob), "s"(srd), "n"(4 * K + 4) : "memory");
}
DI void set_m0(u32 v) { asm volatile("s_mov_b32 m0, %0\n\ts_nop 0" ::"s"(v) : "memory"); }
template <int K> DI u32 z_peek(u32 za) { return *(volatile LAS u32*)(size_t)(za + 4 * K); }
template <int K> DI void z_wait_pre(u32 za, u32 v) {
  u32 cnt;
  asm volatile("v_cmp_ne_u32 vcc, 0, %0\n\ts_cbranch_vccnz .Lsl%=\n.Lse%=:\n\t.subsection 1\n"
               ".Lsl%=:\n\ts_mov_b32 %1, 0x40000\n.Lsp%=:\n\tds_read_b32 %0, %2 offset:%3\n\ts_waitcnt lgkmcnt(0)\n\tv_cmp_ne_u32 vcc, 0, %0\n\ts_cbranch_vccz .Lse%=\n\t"
               "s_sub_u32 %1, %1, 1\n\ts_cbranch_scc0 .Lsp%=\n\ts_branch .Lse%=\n\t.subsection 0"
               : "+v"(v), "=&s"(cnt) : "v"(za), "n"(4 * K) : "vcc", "scc", "memory");
}
template <int K> DI void z_wait(u32 za) { z_wait_pre<K>(za, z_peek<K>(za)); }
DI void flag_peek(u64& v, srd_t ws, u32 voff) {
  asm volatile("buffer_load_dwordx2 %0, %1, %2, 0 offen sc1" : "=v"(v) : "v"(voff), "s"(ws) : "memory");
}
template <int N> DI void flag_check(u64 v, srd_t ws, u32 voff, u64 tag) {
  u32 cnt;
  asm volatile("s_waitcnt vmcnt(%5)\n\t"
               "v_cmp_ne_u64 vcc, %4, %0\n\ts_cbranch_vccnz .Lfl%=\n.Lfe%=:\n\t.subsection 1\n"
               ".Lfl%=:\n\ts_mov_b32 %1, 0x8000\n"
               ".Lfp%=:\n\tbuffer_load_dwordx2 %0, %2, %3, 0 offen sc1\n\ts_waitcnt vmcnt(0)\n\tv_cmp_ne_u64 vcc, %4, %0\n\ts_cbranch_vccz .Lfe%=\n\t"
               "s_sleep " SLEEPN "\n\ts_sub_u32 %1, %1, 1\n\ts_cbranch_scc0 .Lfp%=\n\ts_branch .Lfe%=\n.Lfz%=:\n\t.subsection 0"
               : "+v"(v), "=&s"(cnt) : "v"(voff), "s"(ws), "s"(tag), "n"(N) : "vcc", "scc", "memory");
}
DI void flag_wait(srd_t ws, u32 voff, u64 tag) { u64 v; flag_peek(v, ws, voff); flag_check<0>(v, ws, voff, tag); }
DI void flag_set(srd_t ws, u32 voff, u64 tag) {
  asm volatile("buffer_store_dwordx2 %0, %1, %2, 0 offen sc1" ::"v"(tag), "v"(voff), "s"(ws) : "memory");
}
DI u32x4 not_ones(u32x4 v) { return u32x4{min(v.x, 0xfffffffeu), min(v.y, 0xfffffffeu), min(v.z, 0xfffffffeu), min(v.w, 0xfffffffeu)}; }
template <int N> DI bool arrived(const u32x4 (&v)[N]) {
  u32 m = 0;
#pragma unroll
  for (int i = 0; i < N; ++i) m = max(max(max(m, v[i].x), v[i].y), max(v[i].z, v[i].w));
  return NOWAIT || __builtin_amdgcn_ballot_w64(m == 0xffffffffu) == 0;
}
template <bool FORCE, int NZ> DI void flag_try(u32& done, srd_t ws, u32 voff, u64 tag) {
  u32 t;
#define FT_TAIL "s_cbranch_scc0 .Lfp%=\n.Lfe%=:\n\t.subsection 1\n.Lfp%=:\n\ts_waitcnt vmcnt(%5)\n\tbuffer_store_dwordx2 %2, %3, %4, 0 offen sc1\n\t" \
                "s_mov_b32 %0, 64\n\ts_branch .Lfe%=\n.Lfz%=:\n\t.subsection 0"
  if (FORCE) asm volatile("s_cmp_lg_u32 %0, 0\n\t" FT_TAIL : "+s"(done), "=&s"(t) : "v"(tag), "v"(voff), "s"(ws), "n"(NZ) : "scc", "memory");
  else asm volatile("s_getreg_b32 %1, hwreg(HW_REG_IB_STS, 0, 4)\n\ts_or_b32 %1, %1, %0\n\ts_cmp_gt_u32 %1, %5\n\t" FT_TAIL
                    : "+s"(done), "=&s"(t) : "v"(tag), "v"(voff), "s"(ws), "n"(NZ) : "scc", "memory");
}
#define SB() __builtin_amdgcn_sched_barrier(0)
#define OPAQUE(x) asm volatile("" : "+v"(x))
DI void interleave4() {
#pragma unroll
  for (int i = 0; i < 4; ++i) { __builtin_amdgcn_sched_group_barrier(0x8, 1, 0); __builtin_amdgcn_sched_group_barrier(0x100, 2, 0); }
}
DI float swap_hi(float lo_wants, int h) {
  auto r = __builtin_amdgcn_permlane32_swap(f2u(lo_wants), f2u(lo_wants), false, false);
  (void)h;
  return u2f(r[1]);
}
DI void both_halves(float v, float& lo, float& hi) {
  auto r = __builtin_amdgcn_permlane32_swap(f2u(v), f2u(v), false, false);
  lo = u2f(r[0]); hi = u2f(r[1]);
}
#define STAMP(i) do {} while (0)
#define STAMP_FLUSH() do {} while (0)
#define STAMP_ARG
#define STAMP_PASS

#define FSTAMP(i) do {} while (0)
constexpr float LOG2E = 1.4426950408889634f;

template <int I> struct IC { static constexpr int v = I; };
template <int N, class F, int I = 0> DI void sfor(F f) { if constexpr (I < N) { f(IC<I>{}); sfor<N, F, I + 1>(f); } }

enum { OX, OW, OU, OK, OZ, OPEEK, OB, OA, OST, OFLAG, OOM };
struct Op { int kind, idx, i; };
enum { M_KV0, M_EPI, M_ST, M_JOB };
struct Tab { Op v[300]; int mark[48]; int n; };
#define AWV 4
constexpr int AW = AWV;
constexpr int zidx(bool H, int kind, int idx) { return kind == OW ? idx : kind == OK ? (H ? 7 : 14) + idx : (H ? 11 : 7) + idx; }
constexpr int rpos(bool H, int kind, int idx) { return kind == OX ? 2 * idx : kind == OW ? 2 * idx + 1 : kind == OK ? (H ? 14 : 21) + idx : (H ? 17 : 14) + idx; }
constexpr u32 slot_of(bool H, int kind, int idx) { return kind == OK && idx == 3 ? 9 : rpos(H, kind, idx) % 9; }
constexpr Tab make_tab(bool H) {
  Tab t{}; int n = 0;
  auto I4 = [&](int kind, int idx, int lo = 0, int hi = 4) {
    for (int i = lo; i < hi; ++i) t.v[n++] = Op{kind, idx, i};
    if (kind != OX && kind <= OK && hi == (kind == OK && idx == 3 ? 2 : 4)) t.v[n++] = Op{OZ, zidx(H, kind, idx), 0};
  };
  for (int s = 0; s < 7; ++s) { if (s == 6 && H) t.v[n++] = Op{OPEEK, 0, 0}; I4(OX, s); I4(OW, s); }
  if (!H) for (int s = 0; s < 7; ++s) { if (s == 5) t.v[n++] = Op{OPEEK, 0, 0}; I4(OU, s); }
  I4(OK, 0); I4(OK, 1); I4(OK, 2); I4(OK, 3, 0, 2);
  t.mark[M_KV0] = n;
  t.mark[M_EPI] = -1;
  if (H) for (int u = 0; u < 28; ++u) { if (u == KVT) t.mark[M_EPI] = n; I4(OU, u >> 2, u & 3, (u & 3) + 1); }
  else t.v[n++] = Op{OZ, 18, 0};
  if (t.mark[M_EPI] < 0) t.mark[M_EPI] = n;
  if (H) for (int j = 0; j < 14 - NOBEPI; ++j) { if (j == 11) t.v[n++] = Op{OPEEK, 2, 0}; I4(OB, j, 0, 2); }
  t.mark[M_ST] = n; t.v[n++] = Op{OST, 0, 0};
  if (H) {
    for (int j = 14 - NOBEPI; j < 14; ++j) { if (j == 11) t.v[n++] = Op{OPEEK, 2, 0}; I4(OB, j, 0, 2); }
    const int ord[17] = {31, 32, 33, 34, 35, 19, 20, 21, 22, 23, 24, 25, 26, 28, 29, 30, 27};
    int nd = 0, na = AW;
    for (int j = 0; j < AW; ++j) I4(OA, j, 0, 2);
    for (; nd < PRED; ++nd) I4(OB, ord[nd], 0, 2);
    for (int j = 0; j < 28; ++j) {
      t.mark[M_JOB + j] = n;
      if (j == 12) t.v[n++] = Op{OPEEK, 3, 0};
      for (int k = 0; k < 2; ++k) {
        if (na < 14) { I4(OA, na, 0, 2); ++na; }
        else if (nd < 17) { I4(OB, ord[nd], 0, 2); if (++nd == 16) t.v[n++] = Op{OZ, 18, 0}; }
        else if (nd < 22) { I4(OB, nd - 3, 0, 2); ++nd; }
      }
      if (j == 13) for (int k = 14; k < 14 + LW; ++k) I4(OA, k, 0, 2);
      if (j >= 14 && j + LW < 28) I4(OA, j + LW, 0, 2);
    }
    t.mark[M_JOB + 28] = n;
  }
  t.n = n;
  return t;
}
template <bool H> struct TB {
  static constexpr Tab t = make_tab(H);
  static constexpr int find(int kind, int idx, int i) { for (int k = 0; k < t.n; ++k) if (t.v[k].kind == kind && t.v[k].idx == idx && t.v[k].i == i) return k; return 1000; }
  static constexpr int etick(int n) {
    int k = t.mark[M_EPI];
    for (int i = 0; i < n && k < t.mark[M_ST]; ++i) { ++k; while (k < t.mark[M_ST] && (t.v[k].kind == OZ || t.v[k - 1].kind == OPEEK)) ++k; }
    return n >= 29 ? t.mark[M_ST] : k;
  }
  static constexpr int need0(int k) {
    if (k >= t.n || t.v[k].kind > OK || t.v[k].i != 0 || slot_of(H, t.v[k].kind, t.v[k].idx) == 9) return -1;
    const int p = rpos(H, t.v[k].kind, t.v[k].idx);
    return p < 9 || (p > 22 && H) ? -1 : (p - 9) >> 1;
  }
  static constexpr int need(int k) { for (int i = 0; i < k && i < t.n; ++i) if (need0(i) >= need0(k)) return -1; return need0(k); }
};
static_assert(KVT >= 20 && KVT <= 28, "signals of U2 / U6 double as progress marks of the k/v GEMM");
constexpr u32 L_R2 = 0, L_R1 = 9 * 8192;
static_assert(PRED >= 6 && PRED <= 13, "");
constexpr int LAG = 12;
constexpr int cmax(int a, int b) { return a > b ? a : b; }

template <bool HG, bool FIRST, class Hook>
DI void unit_gemm(u32x4 (&xf)[28], f32x16 (&acc)[4], int lane, int w, u32 za, Hook hook STAMP_ARG) {
  const u32 h = lane >> 5, mat = (lane >> 4) & 1, q = (lane & 15) >> 2, pp = lane & 3;
  const u32 vb0 = HG ? mat * 8192 + (8 * h + q) * 128 + ((q & 2) | mat) * 32 + 8 * pp : (8 * h + q) * 256 + mat * 128 + q * 32 + 8 * pp;
  constexpr u32 KS = HG ? 2048 : 4096;
  constexpr int KIND = FIRST ? OW : OU;
  const u32 r = lane & 31;
  const u32 xb = w * 4096 + r * 128 + ((h ^ ((r >> 1) & 7)) * 16);
  u32 vb[2][4], xa[2][4], pre = 0;
#pragma unroll
  for (int c = 0; c < 8; ++c) {
    vb[c >> 2][c & 3] = (vb0 ^ ((c & 3) * 32)) + (c < 4 && !FIRST ? 131072 : (c >> 2) * 65536); OPAQUE(vb[c >> 2][c & 3]);
    if (FIRST) { xa[c >> 2][c & 3] = (xb ^ ((c & 3) * 32)) + (c >> 2) * 81920; OPAQUE(xa[c >> 2][c & 3]); }
  }
  u32x4 fr[4];
  auto rdw = [&](int s, int n) {
    const int slot = slot_of(HG, KIND, s);
    const u32 a = vb[slot >> 2 & 1][n & 3] + (slot & 3) * 16384 + (n >> 2) * KS;
    fr[n & 3] = trfrag(a, a + KS / 4);
  };
  auto rdx = [&](int s, int kk) { const int slot = slot_of(HG, OX, s); xf[4 * s + kk] = lds128(slot < 5 ? xa[0][kk] + slot * 16384 : xa[1][kk] + (slot - 5) * 16384); };
#pragma unroll
  for (int c = 0; c < 4; ++c)
#pragma unroll
    for (int e = 0; e < 16; ++e) acc[c][e] = 0.f;
  z_wait<zidx(HG, KIND, 0)>(za);
  STAMP(FIRST ? 16 : 32);
  if (FIRST) { rdx(0, 0); rdx(0, 1); rdx(0, 2); }
#pragma unroll
  for (int n = 0; n < 4; ++n) rdw(0, n);
  SB();
  sfor<112>([&](auto gc) {
    constexpr int G = decltype(gc)::v, s = G >> 4, n = G & 15;
    acc[n & 3] = mfma(fr[n & 3], xf[4 * s + (n >> 2)], acc[n & 3]);
    SB();
    asm volatile("" : "+v"(acc[n & 3]));
    if constexpr (n == 12 && s < 6) z_wait_pre<zidx(HG, KIND, s + 1)>(za, pre);
    if constexpr (n == 12 && s < 6 && FIRST) rdx(s + 1, 0);
    if constexpr (!FIRST && slot_of(HG, KIND, s) == 8 && n >= 12) { vb[0][n & 3] -= 131072; OPAQUE(vb[0][n & 3]); }
    if constexpr (n < 12) rdw(s, n + 4); else if constexpr (s < 6) rdw(s + 1, n - 12);
    if constexpr (FIRST) {
      if constexpr (n == 0) rdx(s, 3);
      if constexpr (n >= 13 && n < 15 && s < 6) rdx(s + 1, n - 12);
    }
    if constexpr (n == 8 && s < 6) pre = z_peek<zidx(HG, KIND, s + 1)>(za);
    hook(gc);
    SB();
  });
}

DI void load_wphi(u32x4 (&wp)[2], u32 base, int lane) {
  const u32 h = lane >> 5, fb = (lane >> 4) & 1, q = (lane & 15) >> 2, pp = lane & 3;
  u32 col = 16 * fb + 4 * pp; if (col == 28) col = 24;
  const u32 a = base + (4 * h + q) * 56 + col * 2;
  wp[0] = trfrag(a, a + 8 * 56);
  u32x2 lo = tr64(a + 16 * 56), hi = tr64(base + (24 + q) * 56 + col * 2);
  wp[1] = u32x4{lo.x, lo.y, h ? 0u : hi.x, h ? 0u : hi.y};
}
DI u32x4 pack8(const f32x16& v, int s) {
  return u32x4{cvtpk(v[8 * s], v[8 * s + 1]), cvtpk(v[8 * s + 2], v[8 * s + 3]), cvtpk(v[8 * s + 4], v[8 * s + 5]), cvtpk(v[8 * s + 6], v[8 * s + 7])};
}
DI float sigmoid(float t) { return __builtin_amdgcn_rcpf(1.f + __builtin_amdgcn_exp2f(-LOG2E * t)); }
DI float vmax(float a, float b) { float r; asm("v_max_f32 %0, %1, %2" : "=v"(r) : "v"(a), "v"(b)); return r; }
DI float vmin(float a, float b) { float r; asm("v_min_f32 %0, %1, %2" : "=v"(r) : "v"(a), "v"(b)); return r; }
DI float max3(float a, float b, float c) { return __builtin_fmaxf(__builtin_fmaxf(a, b), c); }
DI float min3(float a, float b, float c) { return __builtin_fminf(__builtin_fminf(a, b), c); }
template <class Hook> DI void feature_exp(const u32x4 (&t)[2], const u32x4 (&wp)[2], f32x16& ep, f32x16& en, Hook hook) {
  f32x16 pz;
#pragma unroll
  for (int e = 0; e < 16; ++e) pz[e] = 0.f;
  pz = mfma(wp[0], t[0], pz);
  pz = mfma(wp[1], t[1], pz);
  float mx = max3(max3(max3(pz[0], pz[1], pz[2]), max3(pz[3], pz[4], pz[5]), max3(pz[6], pz[7], pz[8])),
                  max3(max3(pz[9], pz[10], pz[11]), max3(pz[12], pz[13], pz[14]), pz[15]), pz[15]);
  float mn = min3(min3(min3(pz[0], pz[1], pz[2]), min3(pz[3], pz[4], pz[5]), min3(pz[6], pz[7], pz[8])),
                  min3(min3(pz[9], pz[10], pz[11]), min3(pz[12], pz[13], pz[14]), pz[15]), pz[15]);
  float a, b;
  both_halves(mx, a, b); mx = __builtin_fmaxf(a, b) * LOG2E;
  both_halves(mn, a, b); mn = __builtin_fminf(a, b) * LOG2E;
  asm volatile("" : "+v"(mx), "+v"(mn));
  hook();
#pragma unroll
  for (int e = 0; e < 16; ++e) ep[e] = __builtin_amdgcn_exp2f(__builtin_fmaf(pz[e], LOG2E, -mx));
  asm volatile("" : "+v"(ep));
  hook();
#pragma unroll
  for (int e = 0; e < 16; ++e) en[e] = __builtin_amdgcn_exp2f(__builtin_fmaf(pz[e], -LOG2E, mn));
  asm volatile("" : "+v"(en));
  hook();
}
template <class Hook> DI void reduce_kv(int hd, int pc, srd_t ws, int lane, Hook hook STAMP_ARG) {
  const u32 h = lane >> 5, r = lane & 31, l16 = lane * 16;
  u32x4 pv[8];
  for (int it = 0; it < 20000; ++it) {
#pragma unroll
    for (int i = 0; i < 8; ++i) ldg_sc1(pv[i], h * 4096 + r * 16, ws, WS_KVP + (hd * 16 + 2 * i) * 4096 + pc * 512);
    asm volatile("s_waitcnt vmcnt(0)" : "+v"(pv[0]), "+v"(pv[1]), "+v"(pv[2]), "+v"(pv[3]), "+v"(pv[4]), "+v"(pv[5]), "+v"(pv[6]), "+v"(pv[7])::"memory");
    hook();
    if (arrived(pv)) break;
  }
  STAMP(11);
  u32x4 o;
#pragma unroll
  for (int e = 0; e < 4; ++e) {
    float lo[8], hi[8];
#pragma unroll
    for (int i = 0; i < 8; ++i) { lo[i] = u2f(pv[i][e] << 16); hi[i] = u2f(pv[i][e] & 0xffff0000u); }
    float a, c, d, f;
    both_halves(((lo[0] + lo[1]) + (lo[2] + lo[3])) + ((lo[4] + lo[5]) + (lo[6] + lo[7])), a, c);
    both_halves(((hi[0] + hi[1]) + (hi[2] + hi[3])) + ((hi[4] + hi[5]) + (hi[6] + hi[7])), d, f);
    o[e] = cvtpk(a + c, d + f);
    if (r == 29) o[e] = 0x3f803f80u;
    if (e >= 2 && (pc & 3) == 3) o[e] = 0u;
  }
  stg_sc1(not_ones(o), h ? OOB : l16, ws, WS_KVF + hd * 4096 + pc * 512);
}
template <bool H, class Epi, class Mid>
DI void task_o(int head, int rt32, int w, srd_t ws, u64 tag, int lane, const Sync& y, u32 fdone, const f32x16& own, Epi epi, Mid mid STAMP_ARG) {
  const int h = lane >> 5;
  const u32 l16 = lane * 16, fqa = WS_FQ + (rt32 * 4 + (head >> 2)) * 128;
  u32x4 qf[2], wp[2], kf[4], k1[1];
  auto ldq = [&](u32 vo) {
    ldg_sc1o<0>(qf[0], vo, ws, WS_Q + (rt32 * 32 + head * 2) * 1024);
    ldg_sc1o<1024>(qf[1], vo, ws, WS_Q + (rt32 * 32 + head * 2) * 1024);
  };
  auto ldk = [&]() { ldg_sc1(k1[0], l16, ws, WS_KVF + head * 4096 + w * 1024); };
  u64 fq = 0;
  bool early = false;
  epi([&](int c) {
    if (H && c == QPK) flag_peek(fq, ws, fqa);
    if (H && c == QPK + 2) {
      asm volatile("s_waitcnt vmcnt(2)" : "+v"(fq)::"memory");
      early = __builtin_amdgcn_ballot_w64(fq != tag) == 0;
      ldq(early ? l16 : OOB);
    }
  });
  STAMP(5);
  __builtin_amdgcn_s_setprio(3);
  u32 done = 0;
  if (H) {
    asm volatile("s_waitcnt vmcnt(%2)" : "+v"(qf[0]), "+v"(qf[1]) : "n"(1 - QPK) : "memory");
    if (!early) {
      flag_try<true, 0>(done, ws, fdone, tag);
      flag_wait(ws, fqa, tag);
      ldq(l16);
      asm volatile("s_waitcnt vmcnt(0)" : "+v"(qf[0]), "+v"(qf[1])::"memory");
    }
  } else {
    qf[0] = pack8(own, 0); qf[1] = pack8(own, 1);
    __builtin_amdgcn_s_setprio(0);
    if ((rt32 & 28) != 28) mid([&]() { flag_try<true, 0>(done, ws, fdone, tag); }); else flag_try<true, 0>(done, ws, fdone, tag);
  }
  ldk();
  load_wphi(wp, L_WPHI, lane);
  STAMP(6);
  __builtin_amdgcn_s_setprio(H ? PRIOF : 0);
  f32x16 ep, en; feature_exp(qf, wp, ep, en, [&]() { flag_try<false, 1>(done, ws, fdone, tag); });
  u32x4 ef[2][2] = {{pack8(ep, 0), pack8(ep, 1)}, {pack8(en, 0), pack8(en, 1)}};
  asm volatile("" : "+v"(ef[0][0]), "+v"(ef[0][1]), "+v"(ef[1][0]), "+v"(ef[1][1]));
  __builtin_amdgcn_s_setprio(3);
  if (early) { flag_try<true, 1>(done, ws, fdone, tag); VMWAIT(1); } else VMWAIT(0);
  asm volatile("" : "+v"(k1[0])::"memory");
  STAMP(7);
  if (H || (rt32 & 28) == 28) mid([]() {});
  lds_wait<C_SYNC, 1>(y);
  float gate = sigmoid(*(LAS float*)(size_t)(L_GW + 256 + (w * 32 + (lane & 31)) * 4));
  OPAQUE(gate);
  for (int it = 0; it < 20000 && !arrived(k1); ++it) {
    __builtin_amdgcn_s_sleep(PSL);
    ldk();
    asm volatile("s_waitcnt vmcnt(0)" : "+v"(k1[0])::"memory");
  }
  lds_wait<C_PROG, 1>(y);
  sts128(L_KVF + w * 1024 + l16, k1[0]);
  lds_post<C_KVF>(y);
  lds_wait<C_KVF, 1>(y);
#pragma unroll
  for (int i = 0; i < 4; ++i) kf[i] = lds128(L_KVF + i * 1024 + l16);
  STAMP(8);
  f32x16 np, nn;
#pragma unroll
  for (int e = 0; e < 16; ++e) { np[e] = 0.f; nn[e] = 0.f; }
  np = mfma(kf[0], ef[0][0], np); nn = mfma(kf[2], ef[1][0], nn);
  np = mfma(kf[1], ef[0][1], np); nn = mfma(kf[3], ef[1][1], nn);
  const float rp = __builtin_amdgcn_rcpf(np[13]), rn = __builtin_amdgcn_rcpf(nn[13]);
  const float inv = __builtin_amdgcn_rcpf(np[12] * rp + nn[12] * rn + 1e-6f) * gate;
  const float cp = swap_hi(rp * inv, h), cn = swap_hi(rn * inv, h);
  f32x16 o;
#pragma unroll
  for (int e = 0; e < 16; ++e) o[e] = np[e] * cp + nn[e] * cn;
  u32x4 o0 = pack8(o, 0), o1 = pack8(o, 1);
  const float omg = 1.f - gate, oh = u2f(cvtpk(omg, 0.f) << 16);
  o1.z = h ? cvtpk(oh, omg - oh) : o1.z; o1.w = h ? 0u : o1.w;
  stg_sc1(not_ones(o0), l16, ws, WS_O + (rt32 * 32 + head * 2) * 1024);
  stg_sc1(not_ones(o1), l16, ws, WS_O + (rt32 * 32 + head * 2 + 1) * 1024);
}
DI void dump_tile(const f32x16 (&acc)[2][2], u32 base, int lane) {
  const u32 r = lane & 31, a = base + (r >> 3) * 8192 + (r & 7) * 256 + (((lane >> 5) ^ (r & 7)) * 16);
#pragma unroll
  for (int g4 = 0; g4 < 4; ++g4) {
    u32 ag = a ^ (g4 * 32); OPAQUE(ag);
#pragma unroll
    for (int c = 0; c < 2; ++c)
#pragma unroll
      for (int t = 0; t < 2; ++t)
        sts128(ag + t * 32768 + c * 128, __builtin_bit_cast(u32x4, f32x4{acc[c][t][4 * g4], acc[c][t][4 * g4 + 1], acc[c][t][4 * g4 + 2], acc[c][t][4 * g4 + 3]}));
  }
  asm volatile("s_waitcnt lgkmcnt(0)\n\ts_barrier" ::: "memory");
}
template <class Hook> DI void store_q(const f32x16 (&acc)[4], srd_t ws, u32 soff, int lane, Hook hook) {
#pragma unroll
  for (int c = 0; c < 6; ++c) stg_sc1(pack8(acc[c >> 1], c & 1), lane * 16, ws, soff + c * 1024);
}
DI u32 brow_off(int lane) { const u32 row = lane >> 3; return row * 896 + ((lane & 7) ^ (((row >> 1) & 1) * 4)) * 16; }
struct BPath { u32 wa, ra[2]; };
DI BPath make_bpath(u32 base, int lane) {
  const u32 h = lane >> 5, cb = (lane >> 4) & 1, q = (lane & 15) >> 2, pp = lane & 3;
  BPath t;
  t.wa = base + lane * 16; OPAQUE(t.wa);
#pragma unroll
  for (int c = 0; c < 2; ++c) { t.ra[c] = base + (4 * h + q) * 128 + ((c ^ ((q >> 1) & 1)) * 64) + cb * 32 + pp * 8; OPAQUE(t.ra[c]); }
  return t;
}

template <int PE, class Hook> DI void epi_h(const f32x16 (&acc)[4], srd_t ws, u32 soff, int lane, Hook hook) {
  hook(-1);
#pragma unroll
  for (int c = 0; c < 4; ++c) {
    float hv[8];
#pragma unroll
    for (int e = 0; e < 8; ++e) {
      const float x = acc[c][e];
      const float t = __builtin_amdgcn_exp2f(x * __builtin_fmaf(x * x, -0.10294323970f, -2.30220819814f));
      hv[e] = x * __builtin_amdgcn_rcpf(1.f + t) * acc[c][e + 8];
    }
    u32x4 hp = u32x4{cvtpk(hv[0], hv[1]), cvtpk(hv[2], hv[3]), cvtpk(hv[4], hv[5]), cvtpk(hv[6], hv[7])};
    if (PE != 3) { asm volatile("s_setprio 3" : "+v"(hp)); }
    stg_sc1(hp, lane * 16, ws, soff + c * 1024);
    hook(c);
    if (PE != 3) asm volatile("s_setprio %0" ::"n"(PE));
  }
}

template <bool P1, bool isH> DI void body(const Params& p, const int wave) {
  const int tid = threadIdx.x, lane = tid & 63;
  const int w = wave & 3;
  const int b = blockIdx.x, xcd = b & 7, bi = b >> 3;
  const int m = 8 * (xcd >> 2) + (bi & 7), sl = (xcd & 1) * 4 + (bi >> 3), half = (xcd >> 1) & 1;
  const int tl = half * 56 + (xcd & 1) * 32 + bi, rt64 = 16 * (xcd >> 2) + tl / 7, ct2 = tl % 7;
  const int j = 7 * half + sl;
  const int kvhead = 8 * half + sl;
  const int h = lane >> 5;
  const u32 r = lane & 31, l4 = (lane & 3) * 4, l16 = lane * 16;
  const u32 cb = (lane >> 4) & 1, q = (lane & 15) >> 2, pp = lane & 3;
  const srd_t ws = mksrd(p.ws, WS_END), sqkv = mksrd(p.wqkv, D * 3 * D * 2);
  const int rt32 = m * 4 + w;
  STAMP(0);

  if (P1) __builtin_amdgcn_s_setprio(PRIO0);
  const u32 zm = L_Z + wave * 512;
  u32 z1 = L_Z + (lane & 3) * 512 + 256, z2 = z1 + 2048, oob = OOB;
  OPAQUE(z1); OPAQUE(z2); OPAQUE(oob);
  Sync y{lane == 0 ? L_CNT + w * 4 : L_DUM + lane * 4, L_CNT + l4, 1u};
  OPAQUE(y.pa); OPAQUE(y.ra); OPAQUE(y.one);
  u32x2 ev;
  asm volatile("buffer_load_dwordx2 %0, off, %1, 0 sc1" : "=v"(ev) : "s"(ws) : "memory");

  if (P1) {
    u32 fvo, sink = 0, tvo2;
    const srd_t st2 = mksrd(w == 0 ? p.wg : w == 1 ? p.wu : w == 2 ? p.wd : p.wo, w < 3 ? D * DF * 2 : D * D * 2);
    {
      const u32 scr = L_K3 + w * 1024;
      const int km = rt32;
      dma_buf(scr, km < 56 ? (lane >> 2) * 2688 + (lane & 3) * 16 : OOB, sqkv, (km >> 1) * 16 * 2688 + (1 + (km & 1)) * 896 + kvhead * 56);
      u32x4 gwf;
      ldg(gwf, r == 30 ? h * 16 : OOB, mksrd(p.gw, D * 2), (km >> 1) * 32);
      {
        const u32 row = (xcd >> 2) * 224 + bi * 7 + (lane >> 2), c4 = lane & 3;
        const u32 wv = (lane < 28 && !((xcd & 1) && c4 == 3)) ? row * 3584 + (half * 7 + (xcd & 1) * 4 + c4) * 128 : OOB;
        const u32 tvo1 = w < 2 ? ((PFM & 1) ? wv : OOB) : w == 2 ? ((PFM & 4) && lane < 56 ? ((xcd * 32 + bi) * 56 + lane) * 128 : OOB)
                               : ((PFM & 8) && (xcd & 1) && lane < 28 ? row * 2688 + (half * 3 + c4) * 128 : OOB);
        touch(sink, tvo1, w < 2 ? st2 : w == 2 ? mksrd(p.x, S * D * 2) : sqkv);
        tvo2 = w < 2 ? wv + 14 * 128 : w == 2 ? (lane < 49 ? (b * 49 + lane) * 128 : OOB) : (lane < 13 ? (b * 13 + lane) * 128 : OOB);
        if (T2S < 0) touch(sink, tvo2, st2);
      }
      const u32x4 ones = {~0u, ~0u, ~0u, ~0u};
      stg_sc1(ones, l16, ws, WS_KVP + ((kvhead * 16 + m) * 4 + w) * 1024);
      __builtin_amdgcn_s_setprio(3);
      asm volatile("s_waitcnt vmcnt(%2)" : "+v"(ev), "+v"(gwf) : "n"(T2S < 0 ? 3 : 2) : "memory");
      u32x4 kt;
      if (w & 1) {
        const u32 a = scr + (8 * h + q) * 64 + cb * 32 + 8 * pp;
        kt = trfrag(a, a + 256);
        if (r >= 28) kt = gwf;
      } else {
        z_wait<0>(z2);
        u32x4 wp[2]; load_wphi(wp, L_WPHI, lane);
        const u32 a = scr + ((lane & 3) + ((lane >> 1) & 4) + ((lane << 1) & 8)) * 64 + h * 8;
        f32x16 t;
#pragma unroll
        for (int e = 0; e < 16; ++e) t[e] = 0.f;
#pragma unroll
        for (int s = 0; s < 2; ++s) {
          const u32x2 lo = *(LAS u32x2*)(size_t)(a + s * 32), hi = *(LAS u32x2*)(size_t)(a + s * 32 + 16);
          t = mfma(u32x4{lo.x, lo.y, hi.x, hi.y}, wp[s], t);
        }
#pragma unroll
        for (int e = 0; e < 8; ++e) t[e] *= LOG2E;
        kt = pack8(t, 0);
      }
      if (m == 15) kt = ones;
      stg_sc1(kt, m != 14 ? l16 : OOB, ws, m == 15 ? WS_KVF + (kvhead * 4 + w) * 1024 : WS_KT + (kvhead * 56 + km) * 1024);
      fvo = lane == 0 ? WS_FK + (kvhead * 64 + km) * 8 : OOB;
    }
    const u64 tag = (((u64)rfl(ev.y) << 32) | rfl(ev.x)) + 1;
    STAMP(1);
    u32x4 xf[28];
    f32x16 acc[4];
    u32 done = 0;
    set_m0(zm);
    unit_gemm<isH, true>(xf, acc, lane, w, z2, [&](auto gc) {
      constexpr int s = decltype(gc)::v >> 4, n = decltype(gc)::v & 15;
      if constexpr (n == 15) z_post<s>(oob, ws);
      if constexpr (s < 3 && (n == 3 || n == 9)) flag_try<s == 2 && n == 9, s>(done, ws, fvo, tag);
      if constexpr (s == T2S && n == 11) touch(sink, tvo2, st2);
    } STAMP_PASS);
    STAMP(2);
    __builtin_amdgcn_s_setprio(isH ? PRIOE : 3);
    {
      const u32x4 ones = {~0u, ~0u, ~0u, ~0u};
      stg_sc1(ones, l16, ws, WS_O + (rt32 * 32 + kvhead * 2) * 1024);
      stg_sc1(ones, l16, ws, WS_O + (rt32 * 32 + kvhead * 2 + 1) * 1024);
    }
    if (isH) epi_h<PRIOE>(acc, ws, WS_H + (rt32 * 112 + j * 4) * 1024, lane, [](int) {});
    u32x4 qa[8];
    if (!isH) {
#pragma unroll
      for (int c = 0; c < 8; ++c) { qa[c] = pack8(acc[c >> 1], c & 1); OPAQUE(qa[c]); }
    }
    __builtin_amdgcn_s_setprio(isH ? PRIO1 : PRIOQ);
    STAMP(3);
    done = 0;
    fvo = lane != 0 ? OOB : isH ? WS_FH + (rt32 * 16 + j) * 128 : WS_FQ + (rt32 * 4 + half * 2) * 128;
    u64 fk = 0;
    const u32 oh = w * 4 + ((lane >> 3) & 3), ort = rt64 * 2 + h;
    const u32 fka = !isH ? WS_FK + (m * 64 + lane) * 8 : (oh & 7) == 7 ? WS_FQ + (ort * 4 + (oh >> 3) * 2) * 128 : WS_FH + (ort * 16 + oh - (oh >> 3)) * 128;
    if (isH && PAUSE) {
      flag_try<true, 0>(done, ws, fvo, tag);
      for (int it = 0; it < 4000; ++it) {
        if (rfl(*(volatile LAS u32*)(size_t)(L_CNT + C_SYNC + w * 4)) >= PAUSE) break;
        __builtin_amdgcn_s_sleep(1);
      }
    }
    unit_gemm<isH, false>(xf, acc, lane, w, z2, [&](auto gc) {
      constexpr int s = decltype(gc)::v >> 4, n = decltype(gc)::v & 15;
      if constexpr (isH && s < 4 && (n == 3 || n == 9)) flag_try<s == 3 && n == 9, 0>(done, ws, fvo, tag);
      if constexpr (s == 4 && n == 15) lds_post<C_PROG>(y);
      if constexpr (!isH && s == 0 && n == 15) z_post<7>(oob, ws);
      if constexpr (!isH && s == 1 && n == 5) flag_peek(fk, ws, fka);
      if constexpr (!isH && s == 3 && n == 5) flag_check<0>(fk, ws, fka, tag);
      if constexpr (!isH && (s == QAS || s == QAS + 1) && (n & 3) == 1) stg_sc1(qa[(s - QAS) * 4 + (n >> 2)], l16, ws, WS_Q + (rt32 * 32 + half * 16 + (s - QAS) * 4 + (n >> 2)) * 1024);
      if constexpr (!isH && s > QAS + 1 && (n == 3 || n == 9)) flag_try<s == 6 && n == 9, 0>(done, ws, fvo, tag);
    } STAMP_PASS);
    OPAQUE(sink);
    lds_post<C_PROG>(y);
    STAMP(4);
    __builtin_amdgcn_s_setprio(isH ? PRIOF : 3);
    if (isH) flag_peek(fk, ws, fka);
    u32x4 bo[8][2];
    task_o<isH>(kvhead, rt32, w, ws, tag, lane, y, fvo + (isH ? 64 * 16 * 128 : 128), acc[3], [&](auto hook) {
      if (isH) epi_h<PRIOF>(acc, ws, WS_H + (rt32 * 112 + (14 + j) * 4) * 1024, lane, hook);
      else store_q(acc, ws, WS_Q + (rt32 * 32 + half * 16 + 8) * 1024, lane, hook);
    }, [&](auto hook) { if (!isH) reduce_kv(m, half * 4 + w, ws, lane, hook STAMP_PASS); else flag_check<1>(fk, ws, fka, tag); } STAMP_PASS);
    STAMP(10);
    if (!isH) STAMP_FLUSH();
    if (isH) {
      z_wait<18>(L_Z + 2048 + w * 512 + 256);
      {
        const BPath bp = make_bpath(L_R1 + w * 2048, lane);
#pragma unroll
        for (int i = 0; i < 16; ++i) bo[i >> 1][i & 1] = trfrag(bp.ra[i & 1] + (i >> 1) * 8192, bp.ra[i & 1] + (i >> 1) * 8192 + 1024);
      }
      f32x16 oc[2][2];
#pragma unroll
      for (int i = 0; i < 4; ++i)
#pragma unroll
        for (int e = 0; e < 16; ++e) oc[i >> 1][i & 1][e] = 0.f;
      asm volatile("" : "+v"(oc[0][0]), "+v"(oc[0][1]), "+v"(oc[1][0]), "+v"(oc[1][1]));
      u32x4 ao[16];
      const u32 l16o = l16 + 32 * 1024, oso = WS_O + (rt64 * 2 * 32 + w * 8) * 1024;
#pragma unroll
      for (int i = 0; i < 16; ++i) asm volatile("" : "=v"(ao[i]));
      STAMP(14);
      u32 miss = 0xff, go = 0, next = 0;
      const u32 wv = ((lane >> 3) & 1) * 32768 + (lane >> 4) * 2048 + 1024 + (((lane >> 3) + w) & 7) * 128;
      for (int it = 0; it < 20000 && next < 4; ++it) {
        u32 v;
        sfor<8>([&](auto ic) {
          constexpr int i = decltype(ic)::v, hh = i >> 1, t = i & 1;
          ldg2_if<i, (hh & 1) * 2048>(ao[4 * hh + t], ao[4 * hh + 2 + t], go, t ? l16o : l16, ws, oso + (hh >> 1) * 4096);
        });
        asm volatile("buffer_load_dword %0, %1, %2, %3 offen sc1" : "=v"(v) : "v"((miss & ~go) ? wv : OOB), "s"(ws), "s"(oso) : "memory");
        asm volatile("s_waitcnt vmcnt(0)" : "+v"(ao[0]), "+v"(ao[1]), "+v"(ao[2]), "+v"(ao[3]), "+v"(ao[4]), "+v"(ao[5]), "+v"(ao[6]), "+v"(ao[7]), "+v"(ao[8]), "+v"(ao[9]),
                     "+v"(ao[10]), "+v"(ao[11]), "+v"(ao[12]), "+v"(ao[13]), "+v"(ao[14]), "+v"(ao[15]), "+v"(v)::"memory");
        const u64 em = __builtin_amdgcn_ballot_w64(v == 0xffffffffu) & 0x0101010101010101ull;
        const u32 empty = (u32)((em * 0x0102040810204080ull) >> 56);
        sfor<4>([&](auto hc) {
          constexpr int hh = decltype(hc)::v;
          if ((go >> (2 * hh)) & 3) {
#pragma unroll
            for (int t = 0; t < 2; ++t) {
              const u32x4 two[2] = {ao[4 * hh + t], ao[4 * hh + 2 + t]};
              if (arrived(two)) miss &= ~(go & (1u << (2 * hh + t)));
            }
          }
          if (next == hh && !((miss >> (2 * hh)) & 3)) {
#pragma unroll
            for (int u = 0; u < 8; ++u) oc[(u >> 1) & 1][u & 1] = mfma(bo[2 * hh + (u >> 2)][(u >> 1) & 1], ao[4 * hh + (u >> 2) * 2 + (u & 1)], oc[(u >> 1) & 1][u & 1]);
            next = hh + 1;
          }
        });
        go = __builtin_popcount(miss) <= ODIR ? miss : miss & ~empty;
      }
#pragma unroll
      for (int t = 0; t < 2; ++t) *(LAS float*)(size_t)(h ? L_GW + (t * 32 + r) * 4 : L_DUM + r * 4) = u2f(ao[2 + t].z << 16) + u2f(ao[2 + t].z & 0xffff0000u);
      STAMP(15);
      dump_tile(oc, L_R1 + w * 2048, lane);
      flag_set(ws, (b | w | lane) ? OOB : WS_E, tag);
    }
  } else {
    using T = TB<isH>;
    const srd_t sx = mksrd(p.x, S * D * 2);
    {
      const srd_t sp = mksrd(p.wphi, 16 * 1568);
      if (w < 2) dma_buf(L_WPHI + w * 1024, l16, sp, kvhead * 1568 + w * 1024);
    }
    constexpr u32 wrs = isH ? 3584 : 2688, wu2 = isH ? 14 * 128 : 4 * 56;
    u32 xvo[4], wvo[4];
#pragma unroll
    for (int i = 0; i < 4; ++i) {
      xvo[i] = (lane >> 3) * 896 + (((lane & 7) ^ ((4 * i + (lane >> 4)) & 7)) * 16) + i * (8 * 896 - 1024);
      if (isH) wvo[i] = (lane >> 3) * 3584 + ((((lane >> 1) & 3) ^ (((lane >> 3) & 2) | (w >> 1))) * 32) + (lane & 1) * 16 + i * (8 * 3584 - 1024);
      else     wvo[i] = (lane >> 4) * 2688 + ((((lane >> 1) & 3) ^ (lane >> 4)) * 56) + ((lane >> 3) & 1) * 32 + (lane & 1) * 16 + i * (4 * 2688 - 1024);
    }
    const srd_t sw = isH ? mksrd(w < 2 ? p.wg : p.wu, D * DF * 2) : sqkv;
    const u32 xso = (m * 128 + w * 32) * 896;
    const u32 wso1 = isH ? (w & 1) * 32 * 3584 + j * 128 : w * 16 * 2688 + 8 * half * 56;
    const u32 mq = w * 4096, mq2 = w * 2048;
    const u32 kso4 = WS_KT + kvhead * 56 * 1024 + mq, kso2 = WS_KT + kvhead * 56 * 1024 + mq2;
    const u32 l16h = l16 + 112 * 1024;
    const u32 hso = WS_H + (rt64 * 2 * 112 + w * 14) * 1024, bso = w * 14 * 16 * 896 + ct2 * 128;
    const srd_t swd = mksrd(p.wd, D * DF * 2);
    const u32 bvo = brow_off(lane);
    auto fadr = [&](int k) -> u32 {
      return k == 0 ? WS_FK + (kvhead * 64 + lane) * 8 : WS_FH + (((k - 2) * 64 + rt64 * 2 + h) * 16 + ((w * 14) >> 2) + ((lane >> 3) & 3)) * 128;
    };
    u32 so = 0, pre = 0, fa = 0;
    u64 fv = 0, tag = 0;
    u32x4 xf[28], af[28][2], bf[19][2], kvs;
    const srd_t swo = mksrd(p.wo, D * D * 2);
    const u32 bvo1 = bvo + 8 * 896 - 1024, bvop = lane < 32 ? bvo1 : OOB;
    const u32 xb = w * 4096 + r * 128 + ((h ^ ((r >> 1) & 7)) * 16);
    constexpr u32 L_PHI = slot_of(isH, OK, 1) * 16384, L_VF = slot_of(isH, OK, 2) * 16384;
    struct { u32 pw[4], pr[2], vw, vr, lr, lw, so; } ea;
    float keep;
    auto op = [&](auto kc) {
      constexpr int K = decltype(kc)::v;
      constexpr Op o = T::t.v[K];
      if constexpr (T::need(K) >= 0) z_wait_pre<T::need(K)>(z1, pre);
      if constexpr (isH && o.kind == OU && o.idx == 6 && o.i == 0) z_wait<zidx(isH, OU, 3)>(z2);
      constexpr int chk = o.i != 0 ? -1 : (o.kind == OK && o.idx == 0) ? 0 : (o.kind == OA && o.idx == 0) ? 2
                        : (o.kind == OA && o.idx == 14) ? 3 : -1;
      if constexpr (chk >= 0) { flag_check<K - 1 - T::find(OPEEK, chk, 0)>(fv, ws, fa, tag); STAMP(11 + chk); }
      if constexpr (o.kind <= OK) {
        constexpr u32 sb = slot_of(isH, o.kind, o.idx) * 16384;
        if constexpr (o.i == 0) {
          if constexpr (o.kind == OX) so = dma_open<sb, o.idx * 128>(mq, xso);
          else if constexpr (o.kind == OK) so = dma_open<sb, o.idx * 16384>(o.idx < 3 ? mq : mq2, o.idx < 3 ? kso4 : kso2);
          else so = dma_open<sb, o.idx * 64 * wrs + (o.kind == OW ? 0u : wu2)>(mq, wso1);
        }
        if constexpr (o.kind == OK) dma_off_sc1<o.i * 1024>(l16, ws, so);
        else dma_off<o.i * 1024>(o.kind == OX ? xvo[o.i] : wvo[o.i], o.kind == OX ? sx : sw, so);
      } else if constexpr (o.kind == OZ) { set_m0(zm); z_post<o.idx>(oob, ws); }
      else if constexpr (o.kind == OPEEK) { fa = fadr(o.idx); flag_peek(fv, ws, fa); }
      else if constexpr (o.kind == OB) {
        if constexpr (o.idx < 19) ldg(bf[o.idx][o.i], bvo, swd, bso + ((o.idx >= 14 ? o.idx + 42 : o.idx) * 16 + o.i * 8) * 896);
        else {
          if constexpr (o.idx == 31 && o.i == 0) lds_wait<C_SYNC, 3>(y);
          if constexpr (o.idx == 35 && o.i == 0) lds_wait<C_PROG, 1>(y);
          if constexpr (o.idx == 28 && o.i == 0) lds_wait<C_PROG, 2>(y);
          if constexpr (o.i == 0) {
            if constexpr (o.idx < 28) so = dma_open<L_R2 + (o.idx - 19) * 8192, (o.idx + 42) * 16 * 896>(mq2, bso);
            else so = dma_open<L_R1 + (o.idx - 28) * 8192, (((o.idx - 28) >> 1) * 28 + (o.idx & 1) * 16) * 896>(mq2, w * 112 * 896 + ct2 * 128);
          }
          dma_off<o.i * 1024>(o.i == 0 ? bvo : (o.idx >= 28 && (o.idx & 1)) ? bvop : bvo1, o.idx < 28 ? swd : swo, so);
        }
      }
      else if constexpr (o.kind == OA) {
        constexpr int ks = o.idx >= 14 ? o.idx + 42 : o.idx;
        ldg_sc1o<(ks & 3) * 1024>(af[o.idx][o.i], o.i ? l16h : l16, ws, hso + (ks >> 2) * 4096);
      } else if constexpr (o.kind == OST) stg_sc1(not_ones(kvs), ea.so, ws, WS_KVP + ((kvhead * 16 + m) * 4 + w) * 1024);
      if constexpr (T::need(K + 3) >= 0) pre = z_peek<T::need(K + 3)>(z1);
      constexpr int J = K - LAG;
      if constexpr (J >= 0) {
        constexpr Op oj = T::t.v[J];
        if constexpr (oj.kind == OX && oj.i == 3) {
          if constexpr (J == 3) {
            asm volatile("s_waitcnt vmcnt(%1)" : "+v"(ev) : "n"(LAG) : "memory");
            tag = (((u64)rfl(ev.y) << 32) | rfl(ev.x)) + 1;
          } else VMWAIT(LAG);
#pragma unroll
          for (int kk = 0; kk < 4; ++kk) xf[4 * oj.idx + kk] = lds128(slot_of(isH, OX, oj.idx) * 16384 + (xb ^ (kk * 32)));
          FSTAMP(16 + oj.idx);
        }
      }
      SB();
    };
    auto ops = [&](auto ac, auto bc) { sfor<decltype(bc)::v - decltype(ac)::v>([&](auto kc) { op(IC<decltype(ac)::v + decltype(kc)::v>{}); }); };
#define OPS(a, b) ops(IC<(a)>{}, IC<(b)>{})
#define MK(m) (T::t.mark[m])
#define TICK(M, n, N) OPS(MK(M) + (MK((M) + 1) - MK(M)) * (n) / (N), MK(M) + (MK((M) + 1) - MK(M)) * ((n) + 1) / (N))
#define ETICK(n) OPS(T::etick(n), T::etick((n) + 1))
    f32x16 ka, va;
#pragma unroll
    for (int e = 0; e < 16; ++e) { ka[e] = 0.f; va[e] = r == 28 ? 1.f : 0.f; }
    __builtin_amdgcn_s_setprio(PRIO2);
    OPS(0, 36);
    {
      const u32 t0 = 4 * cb + q, ra = L_PHI + (w >> 1) * 2048 + (16 * h + t0) * 64;
#pragma unroll
      for (int g4 = 0; g4 < 4; ++g4) { ea.pw[g4] = L_PHI + w * 4096 + r * 64 + (((2 * g4 + h) ^ ((r >> 1) & 7)) * 8); OPAQUE(ea.pw[g4]); }
      ea.pr[0] = ra + (((4 * (w & 1) + pp) ^ (t0 >> 1)) * 8); ea.pr[1] = ra + 512 + (((4 * (w & 1) + pp) ^ ((t0 >> 1) + 4)) * 8);
      ea.vw = L_VF + w * 2048 + l16; ea.vr = L_VF + h * 1024 + ((lane & 15) + 32 * cb) * 16;
      ea.lr = L_VF + w * 2048 + (r >> 4) * 1024 + (30 + 32 * ((r >> 2) & 1)) * 16 + (((r >> 3) & 1) * 4 + (r & 3)) * 2;
      ea.lw = L_GW + 256 + (w * 32 + r) * 4; ea.so = (h * 16 + (lane & 15) + 32 * cb) * 16;
      keep = h ? 0.f : 1.f;
      OPAQUE(ea.pr[0]); OPAQUE(ea.pr[1]); OPAQUE(ea.vw); OPAQUE(ea.vr); OPAQUE(ea.lr); OPAQUE(ea.lw); OPAQUE(ea.so); OPAQUE(keep);
      asm volatile("" : "+v"(va));
    }
    OPS(36, MK(M_KV0));
    STAMP(1);
    {
      u32 ak[4];
#pragma unroll
      for (int t = 0; t < 4; ++t) { ak[t] = slot_of(isH, OK, t) * 16384 + l16; OPAQUE(ak[t]); }
      u32x4 fk[3], fw[3];
      u32 pk = 0;
      auto reads = [&](int ks) { fk[ks % 3] = lds128(ak[ks >> 3] + (ks & 7) * 2048); fw[ks % 3] = lds128(ak[ks >> 3] + (ks & 7) * 2048 + 1024); };
      z_wait<zidx(isH, OK, 0)>(z2);
      reads(0); reads(1); reads(2);
      SB();
      sfor<28>([&](auto kc) {
        constexpr int ks = decltype(kc)::v, t = ks >> 3;
        ka = mfma(fk[ks % 3], xf[ks], ka);
        SB();
        asm volatile("" : "+v"(ka));
        TICK(M_KV0, ks, 28);
        va = mfma(xf[ks], fw[ks % 3], va);
        SB();
        asm volatile("" : "+v"(va));
        if constexpr ((ks & 7) == 3 && t < 3) pk = z_peek<zidx(isH, OK, t + 1)>(z2);
        if constexpr ((ks & 7) == 5) z_wait_pre<zidx(isH, OK, t + 1)>(z2, pk);
        if constexpr (ks + 3 < 28) reads(ks + 3);
        SB();
      });
    }
    STAMP(3);
    {
      ETICK(0);
      float mx = max3(max3(max3(ka[0], ka[1], ka[2]), max3(ka[3], ka[4], ka[5]), max3(ka[6], ka[7], ka[8])),
                      max3(max3(ka[9], ka[10], ka[11]), max3(ka[12], ka[13], ka[14]), ka[15]), ka[15]);
      asm volatile("" : "+v"(mx));
      ETICK(1);
      float mn = min3(min3(min3(ka[0], ka[1], ka[2]), min3(ka[3], ka[4], ka[5]), min3(ka[6], ka[7], ka[8])),
                      min3(min3(ka[9], ka[10], ka[11]), min3(ka[12], ka[13], ka[14]), ka[15]), ka[15]);
      asm volatile("" : "+v"(mn));
      ETICK(2);
      { float a, c; both_halves(mx, a, c); mx = vmax(a, c); both_halves(mn, a, c); mn = vmin(a, c); }
      asm volatile("" : "+v"(mx), "+v"(mn));
      ETICK(3);
      u32x4 vf[2] = {pack8(va, 0), pack8(va, 1)}, vv[4][2];
      asm volatile("" : "+v"(vf[0]), "+v"(vf[1]));
      ETICK(4);
      u32 lg = 0;
      sfor<2>([&](auto sc) {
        constexpr int sg = decltype(sc)::v, E0 = 5 + 10 * sg;
        f32x16 e;
        float ps[4];
        sfor<4>([&](auto gc) {
          constexpr int g4 = decltype(gc)::v;
#pragma unroll
          for (int i = 4 * g4; i < 4 * g4 + 4; ++i) e[i] = __builtin_amdgcn_exp2f(sg == 0 ? ka[i] - mx : mn - ka[i]);
          if constexpr (g4 > 0) { ps[g4 - 1] = (e[4 * g4 - 4] + e[4 * g4 - 3]) + (e[4 * g4 - 2] + e[4 * g4 - 1]); asm volatile("" : "+v"(ps[g4 - 1])); }
          float t = e[4 * g4 + 3]; asm volatile("" : "+v"(t)); e[4 * g4 + 3] = t;
          if constexpr (g4 == 2) pre = sg == 0 ? z_peek<(isH ? zidx(isH, OU, 6) : 18)>(z2) : lds_peek<C_SYNC>(y);
          ETICK(E0 + g4);
        });
        ps[3] = (e[12] + e[13]) + (e[14] + e[15]);
        float a, c; both_halves(__builtin_fmaf(ps[3], keep, ps[2]) + (ps[0] + ps[1]), a, c);
        float rs = __builtin_amdgcn_rcpf(a + c);
        asm volatile("" : "+v"(rs));
        ETICK(E0 + 4);
        if constexpr (sg == 0) {
          z_wait_pre<(isH ? zidx(isH, OU, 6) : 18)>(z2, pre);
          sts128(ea.vw, vf[0]); sts128(ea.vw + 1024, vf[1]);
          lg = *(LAS unsigned short*)(size_t)ea.lr;
        } else {
          lds_wait_pre<C_SYNC, 1>(y.ra, pre);
#pragma unroll
          for (int i = 0; i < 8; ++i) vv[i >> 1][i & 1] = lds128(ea.vr + (i >> 1) * 2048 + (i & 1) * 256);
        }
        SB();
        ETICK(E0 + 5);
        sfor<4>([&](auto gc) {
          constexpr int g4 = decltype(gc)::v;
          sts64(ea.pw[g4] + sg * 2048, u32x2{cvtpk(e[4 * g4] * rs, e[4 * g4 + 1] * rs), cvtpk(e[4 * g4 + 2] * rs, e[4 * g4 + 3] * rs)});
          ETICK(E0 + 6 + g4);
        });
        if constexpr (sg == 0) *(LAS u32*)(size_t)ea.lw = lg << 16;
        lds_post<C_SYNC>(y);
      });
      pre = lds_peek<C_SYNC>(y);
      ETICK(25);
      lds_wait_pre<C_SYNC, 2>(y.ra, pre);
      u32x4 pf[4];
#pragma unroll
      for (int u = 0; u < 4; ++u) pf[u] = trfrag(ea.pr[0] + u * 4096, ea.pr[1] + u * 4096);
      lds_post<C_SYNC>(y);
      SB();
      ETICK(26);
      f32x4 kp[2] = {{0.f, 0.f, 0.f, 0.f}, {0.f, 0.f, 0.f, 0.f}};
#pragma unroll
      for (int i = 0; i < 8; ++i)
        kp[i & 1] = __builtin_amdgcn_mfma_f32_16x16x32_bf16(__builtin_bit_cast(bf16x8, pf[i >> 1]), __builtin_bit_cast(bf16x8, vv[i >> 1][i & 1]), kp[i & 1], 0, 0, 0);
      asm volatile("" : "+v"(kp[0]), "+v"(kp[1]));
      ETICK(27);
      const auto sx0 = __builtin_amdgcn_permlane32_swap(cvtpk(kp[0].x, kp[0].y), cvtpk(kp[1].x, kp[1].y), false, false);
      const auto sx1 = __builtin_amdgcn_permlane32_swap(cvtpk(kp[0].z, kp[0].w), cvtpk(kp[1].z, kp[1].w), false, false);
      kvs = u32x4{sx0[0], sx1[0], sx0[1], sx1[1]};
      ETICK(28);
      OPS(MK(M_ST), MK(M_ST) + 1);
    }
    STAMP(4);
    if constexpr (!isH) {
      STAMP(7);
      STAMP_FLUSH();
    } else {
      f32x16 acc[2][2];
#pragma unroll
      for (int i = 0; i < 4; ++i)
#pragma unroll
        for (int e = 0; e < 16; ++e) acc[i >> 1][i & 1][e] = 0.f;
      BPath bp = make_bpath(L_K3 + w * 2048, lane);
      u32x4 tf[2][2];
      asm volatile("s_waitcnt vmcnt(%2)" : "+v"(bf[0][0]), "+v"(bf[0][1]) : "n"(MK(M_ST) - T::find(OB, 0, 1)) : "memory");
      sts128(bp.wa, bf[0][0]); sts128(bp.wa + 1024, bf[0][1]);
      tf[0][0] = trfrag(bp.ra[0], bp.ra[0] + 1024); tf[0][1] = trfrag(bp.ra[1], bp.ra[1] + 1024);
      OPS(MK(M_ST) + 1, MK(M_JOB));
      __builtin_amdgcn_s_setprio(PRIO3);
      sfor<28>([&](auto nc) {
        constexpr int n = decltype(nc)::v, P = MK(M_JOB + n), NL = MK(M_JOB + n + 1) - P, nb = n < 27 ? n + 1 : n;
        constexpr int need = cmax(T::find(OA, n, 1), T::find(OB, nb, 1));
        static_assert(need < P, "stream order");
        if constexpr (nb < 19) asm volatile("s_waitcnt vmcnt(%4)" : "+v"(af[n][0]), "+v"(af[n][1]), "+v"(bf[nb][0]), "+v"(bf[nb][1]) : "n"(P - 1 - need) : "memory");
        else asm volatile("s_waitcnt vmcnt(%2)" : "+v"(af[n][0]), "+v"(af[n][1]) : "n"(P - 1 - need) : "memory");
        sfor<4>([&](auto uc) {
          constexpr int u = decltype(uc)::v, c = u >> 1, t = u & 1;
          acc[c][t] = mfma(tf[n & 1][c], af[n][t], acc[c][t]);
          SB();
          asm volatile("" : "+v"(acc[c][t]));
          if constexpr (nb < 19) {
            if constexpr (u == 0) { sts128(bp.wa, bf[nb][0]); sts128(bp.wa + 1024, bf[nb][1]); }
            if constexpr (u == 1) { tf[nb & 1][0] = trfrag(bp.ra[0], bp.ra[0] + 1024); tf[nb & 1][1] = trfrag(bp.ra[1], bp.ra[1] + 1024); }
            SB();
          } else if constexpr (n < 27 && u < 2) {
            if constexpr (nb == 19) { bp.ra[u] += L_R2 - L_K3; OPAQUE(bp.ra[u]); }
            if constexpr (nb == 23) { bp.ra[u] += 4 * 8192; OPAQUE(bp.ra[u]); }
            constexpr u32 bo2 = (nb < 23 ? nb - 19 : nb - 23) * 8192;
            tf[nb & 1][u] = trfrag(bp.ra[u] + bo2, bp.ra[u] + bo2 + 1024);
            SB();
          }
          OPS(P + NL * u / 4, P + NL * (u + 1) / 4);
        });
        if constexpr (n == 12) STAMP(6);
      });
      STAMP(10);
      dump_tile(acc, L_R2 + w * 2048, lane);
    }
  }
  if (isH) {
    const int tok = tid >> 3, pc = tid & 7;
    const u32 a = (tok >> 3) * 8192 + (tok & 7) * 256 + (((pc * 2) ^ (tok & 7)) * 16);
    f32x4 v[8][2];
#pragma unroll
    for (int u = 0; u < 8; ++u)
#pragma unroll
      for (int i = 0; i < 2; ++i) v[u][i] = __builtin_bit_cast(f32x4, lds128((u < 4 ? L_R2 + u * 2048 : L_R1 + (u - 4) * 2048) + (a ^ (i * 16))));
    const float omg = *(LAS float*)(size_t)(L_GW + tok * 4);
    const f32x4 s0 = ((v[0][0] + v[1][0]) + (v[2][0] + v[3][0])) * omg + ((v[4][0] + v[5][0]) + (v[6][0] + v[7][0]));
    const f32x4 s1 = ((v[0][1] + v[1][1]) + (v[2][1] + v[3][1])) * omg + ((v[4][1] + v[5][1]) + (v[6][1] + v[7][1]));
    const u32x4 res = {cvtpk(s0.x, s0.y), cvtpk(s0.z, s0.w), cvtpk(s1.x, s1.y), cvtpk(s1.z, s1.w)};
    if (OUTSC) stg_sc1(res, tok * 896 + pc * 16, mksrd(p.out, S * D * 2), rt64 * 64 * 896 + ct2 * 128);
    else *(u32x4*)(p.out + (rt64 * 64 + tok) * 896 + ct2 * 128 + pc * 16) = res;
    STAMP(9);
    STAMP_FLUSH();
  }
}

__global__ __launch_bounds__(512, 2) void fused(Params p) {
  asm volatile(".subsection 2\n\t.fill 256, 4, 0xBF800000\n\t.subsection 0");
  const int wave = rfl(threadIdx.x >> 6);
  *(LAS u32*)(size_t)(L_Z + (threadIdx.x >> 6) * 512 + 256 + (threadIdx.x & 63) * 4) = 1u;
  *(LAS u32*)(size_t)(L_CNT + (threadIdx.x & 15) * 4) = 0u;
  asm volatile("s_waitcnt lgkmcnt(0)\n\ts_barrier" ::: "memory");
  if (!((blockIdx.x & 1) && blockIdx.x >= 192)) {
    if (wave < 4) body<true, true>(p, wave); else body<false, true>(p, wave);
  } else {
    if (wave < 4) body<true, false>(p, wave); else body<false, false>(p, wave);
  }
}
}
#if defined(__HIP_DEVICE_COMPILE__)
#pragma clang attribute pop
#endif

extern "C" void kernel_launch(void* const* d_in, const int* in_sizes, int n_in, void* d_out, int out_size, void* d_ws, size_t ws_size,
                              hipStream_t stream) {
  hk::Params p;
  p.x = (const char*)d_in[0]; p.wqkv = (const char*)d_in[1]; p.wphi = (const char*)d_in[2]; p.wo = (const char*)d_in[3];
  p.wg = (const char*)d_in[5]; p.wu = (const char*)d_in[6]; p.wd = (const char*)d_in[7]; p.gw = (const char*)d_in[8];
  p.out = (char*)d_out; p.ws = (char*)d_ws;
  (void)hipFuncSetAttribute((const void*)hk::fused, hipFuncAttributeMaxDynamicSharedMemorySize, hk::L_TOTAL);
  hk::fused<<<256, 512, hk::L_TOTAL, stream>>>(p);
}
```

```cpp
#include <hip/hip_runtime.h>
#include <stdint.h>

#define NOWAIT 0
#define SLEEPN "1"
#define PRIO0 2
#define PRIOE 3
#define NOBEPI 0
#define PFM 15
#define KVT 28
#define T2S 3
#define QAS 4
#define PRIOQ 3
#define PAUSE 0
#define LW 6
#define PRIOF 3
#define PSL 2
#define QPK -1
#define PRED 8
#define OSL 8
#define ODIR 4
#define OUTSC 0
#define ODLY 16
#define PRIOO 3
#define CODEPF 1
#define PRIO2 2
#define PRIO1 1
#define PRIO3 1
#define REDAT 1
#define STAMPS 0

#if defined(__HIP_DEVICE_COMPILE__)
#pragma clang attribute push(__attribute__((target("no-packed-fp32-ops"))), apply_to = function)
#endif
namespace hk {
typedef unsigned u32;
typedef unsigned long long u64;
typedef __bf16 bf16x8 __attribute__((ext_vector_type(8)));
typedef __bf16 bf16x2 __attribute__((ext_vector_type(2)));
typedef short s16x4 __attribute__((ext_vector_type(4)));
typedef short s16x8 __attribute__((ext_vector_type(8)));
typedef u32 u32x4 __attribute__((ext_vector_type(4)));
typedef u32 u32x2 __attribute__((ext_vector_type(2)));
typedef float f32x16 __attribute__((ext_vector_type(16)));
typedef float f32x4 __attribute__((ext_vector_type(4)));
typedef float f32x2 __attribute__((ext_vector_type(2)));
typedef __amdgpu_buffer_rsrc_t srd_t;
#define LAS __attribute__((address_space(3)))
#define DI __device__ __forceinline__

constexpr int S = 2048, D = 448, DF = 1792;
constexpr u32 WS_E = 0;
constexpr u32 WS_FK = 4096;
constexpr u32 WS_FQ = 131072;
constexpr u32 WS_FH = 393216;
constexpr u32 WS_KVF = 655360;
constexpr u32 WS_KVP = 720896;
constexpr u32 WS_O = WS_KVP + 16 * 16 * 4096;
constexpr u32 WS_Q = WS_O + 64 * 32 * 1024;
constexpr u32 WS_H = WS_Q + 64 * 32 * 1024;
constexpr u32 WS_KT = WS_H + 64 * 112 * 1024;
constexpr u32 WS_END = WS_KT + 16 * 56 * 1024;
constexpr u32 WS_STAMP = 24u << 20;
constexpr u32 OOB = 0x7ff00000u;
constexpr u32 L_K3 = 9 * 16384;
constexpr u32 L_WPHI = L_K3 + 8192;
constexpr u32 L_GW = L_WPHI + 2048;
constexpr u32 L_Z = L_GW + 1024;
constexpr u32 L_CNT = L_Z + 4096;
constexpr u32 L_DUM = L_CNT + 64;
constexpr u32 L_TOTAL = 163840;
enum { C_PROG = 0, C_SYNC = 16, C_KVF = 32 };
constexpr u32 L_KVF = 143360;

struct Params {
  const char *x, *wqkv, *wphi, *wo, *wg, *wu, *wd, *gw;
  char *out, *ws;
};

DI u32 f2u(float f) { return __builtin_bit_cast(u32, f); }
DI float u2f(u32 u) { return __builtin_bit_cast(float, u); }
DI u32 min(u32 a, u32 b) { return a < b ? a : b; }
DI u32 max(u32 a, u32 b) { return a > b ? a : b; }
DI u32 rfl(u32 v) { return __builtin_amdgcn_readfirstlane(v); }
DI srd_t mksrd(const void* p, u32 bytes) { return __builtin_amdgcn_make_buffer_rsrc((void*)p, 0, bytes, 0x00020000); }
DI u32 cvtpk(float lo, float hi) { f32x2 v = {lo, hi}; return __builtin_bit_cast(u32, __builtin_convertvector(v, bf16x2)); }
DI f32x16 mfma(u32x4 a, u32x4 b, f32x16 c) {
  return __builtin_amdgcn_mfma_f32_32x32x16_bf16(__builtin_bit_cast(bf16x8, a), __builtin_bit_cast(bf16x8, b), c, 0, 0, 0);
}
DI u32x2 tr64(u32 addr) { return __builtin_bit_cast(u32x2, __builtin_amdgcn_ds_read_tr16_b64_v4i16((LAS s16x4*)(size_t)addr)); }
DI u32x4 trfrag(u32 a0, u32 a1) { u32x2 lo = tr64(a0), hi = tr64(a1); return u32x4{lo.x, lo.y, hi.x, hi.y}; }
DI u32x4 lds128(u32 addr) { return *(LAS u32x4*)(size_t)addr; }
DI void sts128(u32 addr, u32x4 v) { *(LAS u32x4*)(size_t)addr = v; }
DI void sts64(u32 addr, u32x2 v) { *(LAS u32x2*)(size_t)addr = v; }
#define MEMFENCE() asm volatile("" ::: "memory")
#define VMWAIT(n) asm volatile("s_waitcnt vmcnt(%0)" ::"n"(n) : "memory")
DI void dma_buf(u32 ldsaddr, u32 voff, srd_t srd, u32 soff) {
  asm volatile("s_mov_b32 m0, %0\n\ts_nop 0\n\tbuffer_load_dwordx4 %1, %2, %3 offen lds" ::"s"(ldsaddr), "v"(voff), "s"(srd), "s"(soff) : "memory");
}
template <u32 IMM, u32 SIMM> DI u32 dma_open(u32 base, u32 sbase) {
  u32 so;
  asm volatile("s_add_u32 m0, %1, %2\n\ts_add_u32 %0, %3, %4" : "=s"(so) : "s"(base), "n"(IMM), "s"(sbase), "n"(SIMM) : "scc", "memory");
  return so;
}
template <int OFF> DI void dma_off(u32 voff, srd_t srd, u32 soff) {
  asm volatile("buffer_load_dwordx4 %0, %1, %2 offen offset:%3 lds" ::"v"(voff), "s"(srd), "s"(soff), "n"(OFF) : "memory");
}
template <int OFF> DI void dma_off_sc1(u32 voff, srd_t srd, u32 soff) {
  asm volatile("buffer_load_dwordx4 %0, %1, %2 offen offset:%3 sc1 lds" ::"v"(voff), "s"(srd), "s"(soff), "n"(OFF) : "memory");
}
template <int OFF> DI void ldg_sc1o(u32x4& dst, u32 voff, srd_t srd, u32 soff) {
  asm volatile("buffer_load_dwordx4 %0, %1, %2, %3 offen offset:%4 sc1" : "=v"(dst) : "v"(voff), "s"(srd), "s"(soff), "n"(OFF) : "memory");
}
DI void ldg(u32x4& dst, u32 voff, srd_t srd, u32 soff) {
  asm volatile("buffer_load_dwordx4 %0, %1, %2, %3 offen" : "=v"(dst) : "v"(voff), "s"(srd), "s"(soff) : "memory");
}
template <int BIT, int OFF> DI void ldg2_if(u32x4& a, u32x4& b, u32 mask, u32 voff, srd_t srd, u32 soff) {
  asm volatile("s_bitcmp1_b32 %2, %3\n\ts_cbranch_scc0 .Lsk%=\n\tbuffer_load_dwordx4 %0, %4, %5, %6 offen offset:%7 sc1\n\tbuffer_load_dwordx4 %1, %4, %5, %6 offen offset:%8 sc1\n.Lsk%=:"
               : "+v"(a), "+v"(b) : "s"(mask), "n"(BIT), "v"(voff), "s"(srd), "s"(soff), "n"(OFF), "n"(OFF + 1024) : "scc", "memory");
}
template <int N> DI void landed4(u32x4& a, u32x4& b, u32x4& c, u32x4& d) {
  asm volatile("s_waitcnt vmcnt(%4)" : "+v"(a), "+v"(b), "+v"(c), "+v"(d) : "n"(N) : "memory");
}
DI void pin2(u32x4& a, u32x4& b) { asm volatile("" : "+v"(a), "+v"(b)::"memory"); }
DI void pin1(u32& a) { asm volatile("" : "+v"(a)); }
DI void touch(u32& sink, u32 voff, srd_t srd) {
  asm volatile("buffer_load_dword %0, %1, %2, 0 offen" : "+v"(sink) : "v"(voff), "s"(srd) : "memory");
}
DI void ldg_sc1(u32x4& dst, u32 voff, srd_t srd, u32 soff) {
  asm volatile("buffer_load_dwordx4 %0, %1, %2, %3 offen sc1" : "=v"(dst) : "v"(voff), "s"(srd), "s"(soff) : "memory");
}
DI void stg_sc1(u32x4 v, u32 voff, srd_t srd, u32 soff) {
  asm volatile("buffer_store_dwordx4 %0, %1, %2, %3 offen sc1\n\ts_nop 1" ::"v"(v), "v"(voff), "s"(srd), "s"(soff) : "memory");
}
DI void stg64_sc1(u32x2 v, u32 voff, srd_t srd, u32 soff) {
  asm volatile("buffer_store_dwordx2 %0, %1, %2, %3 offen sc1" ::"v"(v), "v"(voff), "s"(srd), "s"(soff) : "memory");
}
DI void stg32_sc1(u32 v, u32 voff, srd_t srd, u32 soff) {
  asm volatile("buffer_store_dword %0, %1, %2, %3 offen sc1" ::"v"(v), "v"(voff), "s"(srd), "s"(soff) : "memory");
}
struct Sync { u32 pa, ra, one; };
template <int C> DI void lds_post(const Sync& y) { asm volatile("ds_add_u32 %0, %1 offset:%2" ::"v"(y.pa), "v"(y.one), "n"(C) : "memory"); }
template <int C> DI u32 lds_peek(const Sync& y) { return *(volatile LAS u32*)(size_t)(y.ra + C); }
template <int C, int T> DI void lds_wait_pre(u32 a, u32 v) {
  u32 cnt;
  asm volatile("v_cmp_gt_u32 vcc, %3, %0\n\ts_cbranch_vccnz .Lsl%=\n.Lse%=:\n\t.subsection 1\n"
               ".Lsl%=:\n\ts_mov_b32 %1, 0x40000\n.Lsp%=:\n\tds_read_b32 %0, %2 offset:%4\n\ts_waitcnt lgkmcnt(0)\n\tv_cmp_gt_u32 vcc, %3, %0\n\ts_cbranch_vccz .Lse%=\n\t"
               "s_sub_u32 %1, %1, 1\n\ts_cbranch_scc0 .Lsp%=\n\ts_branch .Lse%=\n\t.subsection 0"
               : "+v"(v), "=&s"(cnt) : "v"(a), "n"(T), "n"(C) : "vcc", "scc", "memory");
}
template <int C, int T> DI void lds_wait(const Sync& y) { lds_wait_pre<C, T>(y.ra, lds_peek<C>(y)); }
template <int K> DI void z_post(u32 oob, srd_t srd) {
  asm volatile("buffer_load_dword %0, %1, 0 offen offset:%2 lds" ::"v"(oob), "s"(srd), "n"(4 * K + 4) : "memory");
}
DI void set_m0(u32 v) { asm volatile("s_mov_b32 m0, %0\n\ts_nop 0" ::"s"(v) : "memory"); }
template <int K> DI u32 z_peek(u32 za) { return *(volatile LAS u32*)(size_t)(za + 4 * K); }
template <int K> DI void z_wait_pre(u32 za, u32 v) {
  u32 cnt;
  asm volatile("v_cmp_ne_u32 vcc, 0, %0\n\ts_cbranch_vccnz .Lsl%=\n.Lse%=:\n\t.subsection 1\n"
               ".Lsl%=:\n\ts_mov_b32 %1, 0x40000\n.Lsp%=:\n\tds_read_b32 %0, %2 offset:%3\n\ts_waitcnt lgkmcnt(0)\n\tv_cmp_ne_u32 vcc, 0, %0\n\ts_cbranch_vccz .Lse%=\n\t"
               "s_sub_u32 %1, %1, 1\n\ts_cbranch_scc0 .Lsp%=\n\ts_branch .Lse%=\n\t.subsection 0"
               : "+v"(v), "=&s"(cnt) : "v"(za), "n"(4 * K) : "vcc", "scc", "memory");
}
template <int K> DI void z_wait(u32 za) { z_wait_pre<K>(za, z_peek<K>(za)); }
DI void flag_peek(u64& v, srd_t ws, u32 voff) {
  asm volatile("buffer_load_dwordx2 %0, %1, %2, 0 offen sc1" : "=v"(v) : "v"(voff), "s"(ws) : "memory");
}
template <int N> DI void flag_check(u64 v, srd_t ws, u32 voff, u64 tag) {
  u32 cnt;
  asm volatile("s_waitcnt vmcnt(%5)\n\t"
               "v_cmp_ne_u64 vcc, %4, %0\n\ts_cbranch_vccnz .Lfl%=\n.Lfe%=:\n\t.subsection 1\n"
               ".Lfl%=:\n\ts_mov_b32 %1, 0x8000\n"
               ".Lfp%=:\n\tbuffer_load_dwordx2 %0, %2, %3, 0 offen sc1\n\ts_waitcnt vmcnt(0)\n\tv_cmp_ne_u64 vcc, %4, %0\n\ts_cbranch_vccz .Lfe%=\n\t"
               "s_sleep " SLEEPN "\n\ts_sub_u32 %1, %1, 1\n\ts_cbranch_scc0 .Lfp%=\n\ts_branch .Lfe%=\n.Lfz%=:\n\t.subsection 0"
               : "+v"(v), "=&s"(cnt) : "v"(voff), "s"(ws), "s"(tag), "n"(N) : "vcc", "scc", "memory");
}
DI void flag_wait(srd_t ws, u32 voff, u64 tag) { u64 v; flag_peek(v, ws, voff); flag_check<0>(v, ws, voff, tag); }
DI void flag_set(srd_t ws, u32 voff, u64 tag) {
  asm volatile("buffer_store_dwordx2 %0, %1, %2, 0 offen sc1" ::"v"(tag), "v"(voff), "s"(ws) : "memory");
}
DI u32x4 not_ones(u32x4 v) { return u32x4{min(v.x, 0xfffffffeu), min(v.y, 0xfffffffeu), min(v.z, 0xfffffffeu), min(v.w, 0xfffffffeu)}; }
template <int N> DI bool arrived(const u32x4 (&v)[N]) {
  u32 m = 0;
#pragma unroll
  for (int i = 0; i < N; ++i) m = max(max(max(m, v[i].x), v[i].y), max(v[i].z, v[i].w));
  return NOWAIT || __builtin_amdgcn_ballot_w64(m == 0xffffffffu) == 0;
}
template <bool FORCE, int NZ> DI void flag_try(u32& done, srd_t ws, u32 voff, u64 tag) {
  u32 t;
#define FT_TAIL "s_cbranch_scc0 .Lfp%=\n.Lfe%=:\n\t.subsection 1\n.Lfp%=:\n\ts_waitcnt vmcnt(%5)\n\tbuffer_store_dwordx2 %2, %3, %4, 0 offen sc1\n\t" \
                "s_mov_b32 %0, 64\n\ts_branch .Lfe%=\n.Lfz%=:\n\t.subsection 0"
  if (FORCE) asm volatile("s_cmp_lg_u32 %0, 0\n\t" FT_TAIL : "+s"(done), "=&s"(t) : "v"(tag), "v"(voff), "s"(ws), "n"(NZ) : "scc", "memory");
  else asm volatile("s_getreg_b32 %1, hwreg(HW_REG_IB_STS, 0, 4)\n\ts_or_b32 %1, %1, %0\n\ts_cmp_gt_u32 %1, %5\n\t" FT_TAIL
                    : "+s"(done), "=&s"(t) : "v"(tag), "v"(voff), "s"(ws), "n"(NZ) : "scc", "memory");
}
#define SB() __builtin_amdgcn_sched_barrier(0)
#define OPAQUE(x) asm volatile("" : "+v"(x))
DI void interleave4() {
#pragma unroll
  for (int i = 0; i < 4; ++i) { __builtin_amdgcn_sched_group_barrier(0x8, 1, 0); __builtin_amdgcn_sched_group_barrier(0x100, 2, 0); }
}
DI float swap_hi(float lo_wants, int h) {
  auto r = __builtin_amdgcn_permlane32_swap(f2u(lo_wants), f2u(lo_wants), false, false);
  (void)h;
  return u2f(r[1]);
}
DI void both_halves(float v, float& lo, float& hi) {
  auto r = __builtin_amdgcn_permlane32_swap(f2u(v), f2u(v), false, false);
  lo = u2f(r[0]); hi = u2f(r[1]);
}
#define STAMP(i) do {} while (0)
#define STAMP_FLUSH() do {} while (0)
#define STAMP_ARG
#define STAMP_PASS

#define FSTAMP(i) do {} while (0)
constexpr float LOG2E = 1.4426950408889634f;

template <int I> struct IC { static constexpr int v = I; };
template <int N, class F, int I = 0> DI void sfor(F f) { if constexpr (I < N) { f(IC<I>{}); sfor<N, F, I + 1>(f); } }

enum { OX, OW, OU, OK, OZ, OPEEK, OB, OA, OST, OFLAG, OOM };
struct Op { int kind, idx, i; };
enum { M_KV0, M_EPI, M_ST, M_JOB };
struct Tab { Op v[300]; int mark[48]; int n; };
#define AWV 4
constexpr int AW = AWV;
constexpr int zidx(bool H, int kind, int idx) { return kind == OW ? idx : kind == OK ? (H ? 7 : 14) + idx : (H ? 11 : 7) + idx; }
constexpr int rpos(bool H, int kind, int idx) { return kind == OX ? 2 * idx : kind == OW ? 2 * idx + 1 : kind == OK ? (H ? 14 : 21) + idx : (H ? 17 : 14) + idx; }
constexpr u32 slot_of(bool H, int kind, int idx) { return kind == OK && idx == 3 ? 9 : rpos(H, kind, idx) % 9; }
constexpr Tab make_tab(bool H) {
  Tab t{}; int n = 0;
  auto I4 = [&](int kind, int idx, int lo = 0, int hi = 4) {
    for (int i = lo; i < hi; ++i) t.v[n++] = Op{kind, idx, i};
    if (kind != OX && kind <= OK && hi == (kind == OK && idx == 3 ? 2 : 4)) t.v[n++] = Op{OZ, zidx(H, kind, idx), 0};
  };
  for (int s = 0; s < 7; ++s) { if (s == 6 && H) t.v[n++] = Op{OPEEK, 0, 0}; I4(OX, s); I4(OW, s); }
  if (!H) for (int s = 0; s < 7; ++s) { if (s == 5) t.v[n++] = Op{OPEEK, 0, 0}; I4(OU, s); }
  I4(OK, 0); I4(OK, 1); I4(OK, 2); I4(OK, 3, 0, 2);
  t.mark[M_KV0] = n;
  t.mark[M_EPI] = -1;
  if (H) for (int u = 0; u < 28; ++u) { if (u == KVT) t.mark[M_EPI] = n; I4(OU, u >> 2, u & 3, (u & 3) + 1); }
  else t.v[n++] = Op{OZ, 18, 0};
  if (t.mark[M_EPI] < 0) t.mark[M_EPI] = n;
  if (H) for (int j = 0; j < 14 - NOBEPI; ++j) { if (j == 11) t.v[n++] = Op{OPEEK, 2, 0}; I4(OB, j, 0, 2); }
  t.mark[M_ST] = n; t.v[n++] = Op{OST, 0, 0};
  if (H) {
    for (int j = 14 - NOBEPI; j < 14; ++j) { if (j == 11) t.v[n++] = Op{OPEEK, 2, 0}; I4(OB, j, 0, 2); }
    const int ord[17] = {31, 32, 33, 34, 35, 19, 20, 21, 22, 23, 24, 25, 26, 28, 29, 30, 27};
    int nd = 0, na = AW;
    for (int j = 0; j < AW; ++j) I4(OA, j, 0, 2);
    for (; nd < PRED; ++nd) I4(OB, ord[nd], 0, 2);
    for (int j = 0; j < 28; ++j) {
      t.mark[M_JOB + j] = n;
      if (j == 12) t.v[n++] = Op{OPEEK, 3, 0};
      for (int k = 0; k < 2; ++k) {
        if (na < 14) { I4(OA, na, 0, 2); ++na; }
        else if (nd < 17) { I4(OB, ord[nd], 0, 2); if (++nd == 16) t.v[n++] = Op{OZ, 18, 0}; }
        else if (nd < 22) { I4(OB, nd - 3, 0, 2); ++nd; }
      }
      if (j == 13) for (int k = 14; k < 14 + LW; ++k) I4(OA, k, 0, 2);
      if (j >= 14 && j + LW < 28) I4(OA, j + LW, 0, 2);
    }
    t.mark[M_JOB + 28] = n;
  }
  t.n = n;
  return t;
}
template <bool H> struct TB {
  static constexpr Tab t = make_tab(H);
  static constexpr int find(int kind, int idx, int i) { for (int k = 0; k < t.n; ++k) if (t.v[k].kind == kind && t.v[k].idx == idx && t.v[k].i == i) return k; return 1000; }
  static constexpr int etick(int n) {
    int k = t.mark[M_EPI];
    for (int i = 0; i < n && k < t.mark[M_ST]; ++i) { ++k; while (k < t.mark[M_ST] && (t.v[k].kind == OZ || t.v[k - 1].kind == OPEEK)) ++k; }
    return n >= 29 ? t.mark[M_ST] : k;
  }
  static constexpr int need0(int k) {
    if (k >= t.n || t.v[k].kind > OK || t.v[k].i != 0 || slot_of(H, t.v[k].kind, t.v[k].idx) == 9) return -1;
    const int p = rpos(H, t.v[k].kind, t.v[k].idx);
    return p < 9 || (p > 22 && H) ? -1 : (p - 9) >> 1;
  }
  static constexpr int need(int k) { for (int i = 0; i < k && i < t.n; ++i) if (need0(i) >= need0(k)) return -1; return need0(k); }
};
static_assert(KVT >= 20 && KVT <= 28, "signals of U2 / U6 double as progress marks of the k/v GEMM");
constexpr u32 L_R2 = 0, L_R1 = 9 * 8192;
static_assert(PRED >= 6 && PRED <= 13, "");
constexpr int LAG = 12;
constexpr int cmax(int a, int b) { return a > b ? a : b; }

template <bool HG, bool FIRST, class Hook>
DI void unit_gemm(u32x4 (&xf)[28], f32x16 (&acc)[4], int lane, int w, u32 za, Hook hook STAMP_ARG) {
  const u32 h = lane >> 5, mat = (lane >> 4) & 1, q = (lane & 15) >> 2, pp = lane & 3;
  const u32 vb0 = HG ? mat * 8192 + (8 * h + q) * 128 + ((q & 2) | mat) * 32 + 8 * pp : (8 * h + q) * 256 + mat * 128 + q * 32 + 8 * pp;
  constexpr u32 KS = HG ? 2048 : 4096;
  constexpr int KIND = FIRST ? OW : OU;
  const u32 r = lane & 31;
  const u32 xb = w * 4096 + r * 128 + ((h ^ ((r >> 1) & 7)) * 16);
  u32 vb[2][4], xa[2][4], pre = 0;
#pragma unroll
  for (int c = 0; c < 8; ++c) {
    vb[c >> 2][c & 3] = (vb0 ^ ((c & 3) * 32)) + (c < 4 && !FIRST ? 131072 : (c >> 2) * 65536); OPAQUE(vb[c >> 2][c & 3]);
    if (FIRST) { xa[c >> 2][c & 3] = (xb ^ ((c & 3) * 32)) + (c >> 2) * 81920; OPAQUE(xa[c >> 2][c & 3]); }
  }
  u32x4 fr[4];
  auto rdw = [&](int s, int n) {
    const int slot = slot_of(HG, KIND, s);
    const u32 a = vb[slot >> 2 & 1][n & 3] + (slot & 3) * 16384 + (n >> 2) * KS;
    fr[n & 3] = trfrag(a, a + KS / 4);
  };
  auto rdx = [&](int s, int kk) { const int slot = slot_of(HG, OX, s); xf[4 * s + kk] = lds128(slot < 5 ? xa[0][kk] + slot * 16384 : xa[1][kk] + (slot - 5) * 16384); };
#pragma unroll
  for (int c = 0; c < 4; ++c)
#pragma unroll
    for (int e = 0; e < 16; ++e) acc[c][e] = 0.f;
  z_wait<zidx(HG, KIND, 0)>(za);
  STAMP(FIRST ? 16 : 32);
  if (FIRST) { rdx(0, 0); rdx(0, 1); rdx(0, 2); }
#pragma unroll
  for (int n = 0; n < 4; ++n) rdw(0, n);
  SB();
  sfor<112>([&](auto gc) {
    constexpr int G = decltype(gc)::v, s = G >> 4, n = G & 15;
    acc[n & 3] = mfma(fr[n & 3], xf[4 * s + (n >> 2)], acc[n & 3]);
    SB();
    asm volatile("" : "+v"(acc[n & 3]));
    if constexpr (n == 12 && s < 6) z_wait_pre<zidx(HG, KIND, s + 1)>(za, pre);
    if constexpr (n == 12 && s < 6 && FIRST) rdx(s + 1, 0);
    if constexpr (!FIRST && slot_of(HG, KIND, s) == 8 && n >= 12) { vb[0][n & 3] -= 131072; OPAQUE(vb[0][n & 3]); }
    if constexpr (n < 12) rdw(s, n + 4); else if constexpr (s < 6) rdw(s + 1, n - 12);
    if constexpr (FIRST) {
      if constexpr (n == 0) rdx(s, 3);
      if constexpr (n >= 13 && n < 15 && s < 6) rdx(s + 1, n - 12);
    }
    if constexpr (n == 8 && s < 6) pre = z_peek<zidx(HG, KIND, s + 1)>(za);
    hook(gc);
    SB();
  });
}

DI void load_wphi(u32x4 (&wp)[2], u32 base, int lane) {
  const u32 h = lane >> 5, fb = (lane >> 4) & 1, q = (lane & 15) >> 2, pp = lane & 3;
  u32 col = 16 * fb + 4 * pp; if (col == 28) col = 24;
  const u32 a = base + (4 * h + q) * 56 + col * 2;
  wp[0] = trfrag(a, a + 8 * 56);
  u32x2 lo = tr64(a + 16 * 56), hi = tr64(base + (24 + q) * 56 + col * 2);
  wp[1] = u32x4{lo.x, lo.y, h ? 0u : hi.x, h ? 0u : hi.y};
}
DI u32x4 pack8(const f32x16& v, int s) {
  return u32x4{cvtpk(v[8 * s], v[8 * s + 1]), cvtpk(v[8 * s + 2], v[8 * s + 3]), cvtpk(v[8 * s + 4], v[8 * s + 5]), cvtpk(v[8 * s + 6], v[8 * s + 7])};
}
DI float sigmoid(float t) { return __builtin_amdgcn_rcpf(1.f + __builtin_amdgcn_exp2f(-LOG2E * t)); }
DI float vmax(float a, float b) { float r; asm("v_max_f32 %0, %1, %2" : "=v"(r) : "v"(a), "v"(b)); return r; }
DI float vmin(float a, float b) { float r; asm("v_min_f32 %0, %1, %2" : "=v"(r) : "v"(a), "v"(b)); return r; }
DI float max3(float a, float b, float c) { return __builtin_fmaxf(__builtin_fmaxf(a, b), c); }
DI float min3(float a, float b, float c) { return __builtin_fminf(__builtin_fminf(a, b), c); }
template <class Hook> DI void feature_exp(const u32x4 (&t)[2], const u32x4 (&wp)[2], f32x16& ep, f32x16& en, Hook hook) {
  f32x16 pz;
#pragma unroll
  for (int e = 0; e < 16; ++e) pz[e] = 0.f;
  pz = mfma(wp[0], t[0], pz);
  pz = mfma(wp[1], t[1], pz);
  float mx = max3(max3(max3(pz[0], pz[1], pz[2]), max3(pz[3], pz[4], pz[5]), max3(pz[6], pz[7], pz[8])),
                  max3(max3(pz[9], pz[10], pz[11]), max3(pz[12], pz[13], pz[14]), pz[15]), pz[15]);
  float mn = min3(min3(min3(pz[0], pz[1], pz[2]), min3(pz[3], pz[4], pz[5]), min3(pz[6], pz[7], pz[8])),
                  min3(min3(pz[9], pz[10], pz[11]), min3(pz[12], pz[13], pz[14]), pz[15]), pz[15]);
  float a, b;
  both_halves(mx, a, b); mx = __builtin_fmaxf(a, b) * LOG2E;
  both_halves(mn, a, b); mn = __builtin_fminf(a, b) * LOG2E;
  asm volatile("" : "+v"(mx), "+v"(mn));
  hook();
#pragma unroll
  for (int e = 0; e < 16; ++e) ep[e] = __builtin_amdgcn_exp2f(__builtin_fmaf(pz[e], LOG2E, -mx));
  asm volatile("" : "+v"(ep));
  hook();
#pragma unroll
  for (int e = 0; e < 16; ++e) en[e] = __builtin_amdgcn_exp2f(__builtin_fmaf(pz[e], -LOG2E, mn));
  asm volatile("" : "+v"(en));
  hook();
}
template <class Hook> DI void reduce_kv(int hd, int pc, srd_t ws, int lane, Hook hook STAMP_ARG) {
  const u32 h = lane >> 5, r = lane & 31, l16 = lane * 16;
  u32x4 pv[8];
  for (int it = 0; it < 20000; ++it) {
#pragma unroll
    for (int i = 0; i < 8; ++i) ldg_sc1(pv[i], h * 4096 + r * 16, ws, WS_KVP + (hd * 16 + 2 * i) * 4096 + pc * 512);
    asm volatile("s_waitcnt vmcnt(0)" : "+v"(pv[0]), "+v"(pv[1]), "+v"(pv[2]), "+v"(pv[3]), "+v"(pv[4]), "+v"(pv[5]), "+v"(pv[6]), "+v"(pv[7])::"memory");
    hook();
    if (arrived(pv)) break;
  }
  STAMP(11);
  u32x4 o;
#pragma unroll
  for (int e = 0; e < 4; ++e) {
    float lo[8], hi[8];
#pragma unroll
    for (int i = 0; i < 8; ++i) { lo[i] = u2f(pv[i][e] << 16); hi[i] = u2f(pv[i][e] & 0xffff0000u); }
    float a, c, d, f;
    both_halves(((lo[0] + lo[1]) + (lo[2] + lo[3])) + ((lo[4] + lo[5]) + (lo[6] + lo[7])), a, c);
    both_halves(((hi[0] + hi[1]) + (hi[2] + hi[3])) + ((hi[4] + hi[5]) + (hi[6] + hi[7])), d, f);
    o[e] = cvtpk(a + c, d + f);
    if (r == 29) o[e] = 0x3f803f80u;
    if (e >= 2 && (pc & 3) == 3) o[e] = 0u;
  }
  stg_sc1(not_ones(o), h ? OOB : l16, ws, WS_KVF + hd * 4096 + pc * 512);
}
template <bool H, class Epi, class Mid>
DI void task_o(int head, int rt32, int w, srd_t ws, u64 tag, int lane, const Sync& y, u32 fdone, const f32x16& own, Epi epi, Mid mid STAMP_ARG) {
  const int h = lane >> 5;
  const u32 l16 = lane * 16, fqa = WS_FQ + (rt32 * 4 + (head >> 2)) * 128;
  u32x4 qf[2], wp[2], kf[4], k1[1];
  auto ldq = [&](u32 vo) {
    ldg_sc1o<0>(qf[0], vo, ws, WS_Q + (rt32 * 32 + head * 2) * 1024);
    ldg_sc1o<1024>(qf[1], vo, ws, WS_Q + (rt32 * 32 + head * 2) * 1024);
  };
  auto ldk = [&]() { ldg_sc1(k1[0], l16, ws, WS_KVF + head * 4096 + w * 1024); };
  u64 fq = 0;
  bool early = false;
  epi([&](int c) {
    if (H && c == QPK) flag_peek(fq, ws, fqa);
    if (H && c == QPK + 2) {
      asm volatile("s_waitcnt vmcnt(2)" : "+v"(fq)::"memory");
      early = __builtin_amdgcn_ballot_w64(fq != tag) == 0;
      ldq(early ? l16 : OOB);
    }
  });
  STAMP(5);
  __builtin_amdgcn_s_setprio(3);
  u32 done = 0;
  if (H) {
    asm volatile("s_waitcnt vmcnt(%2)" : "+v"(qf[0]), "+v"(qf[1]) : "n"(1 - QPK) : "memory");
    if (!early) {
      flag_try<true, 0>(done, ws, fdone, tag);
      flag_wait(ws, fqa, tag);
      ldq(l16);
      asm volatile("s_waitcnt vmcnt(0)" : "+v"(qf[0]), "+v"(qf[1])::"memory");
    }
  } else {
    qf[0] = pack8(own, 0); qf[1] = pack8(own, 1);
    __builtin_amdgcn_s_setprio(0);
    if ((rt32 & 28) != 28) mid([&]() { flag_try<true, 0>(done, ws, fdone, tag); }); else flag_try<true, 0>(done, ws, fdone, tag);
  }
  ldk();
  load_wphi(wp, L_WPHI, lane);
  STAMP(6);
  __builtin_amdgcn_s_setprio(H ? PRIOF : 0);
  f32x16 ep, en; feature_exp(qf, wp, ep, en, [&]() { flag_try<false, 1>(done, ws, fdone, tag); });
  u32x4 ef[2][2] = {{pack8(ep, 0), pack8(ep, 1)}, {pack8(en, 0), pack8(en, 1)}};
  asm volatile("" : "+v"(ef[0][0]), "+v"(ef[0][1]), "+v"(ef[1][0]), "+v"(ef[1][1]));
  __builtin_amdgcn_s_setprio(3);
  if (early) { flag_try<true, 1>(done, ws, fdone, tag); VMWAIT(1); } else VMWAIT(0);
  asm volatile("" : "+v"(k1[0])::"memory");
  STAMP(7);
  if (H || (rt32 & 28) == 28) mid([]() {});
  lds_wait<C_SYNC, 1>(y);
  float gate = sigmoid(*(LAS float*)(size_t)(L_GW + 256 + (w * 32 + (lane & 31)) * 4));
  OPAQUE(gate);
  for (int it = 0; it < 20000 && !arrived(k1); ++it) {
    __builtin_amdgcn_s_sleep(PSL);
    ldk();
    asm volatile("s_waitcnt vmcnt(0)" : "+v"(k1[0])::"memory");
  }
  lds_wait<C_PROG, 1>(y);
  sts128(L_KVF + w * 1024 + l16, k1[0]);
  lds_post<C_KVF>(y);
  lds_wait<C_KVF, 1>(y);
#pragma unroll
  for (int i = 0; i < 4; ++i) kf[i] = lds128(L_KVF + i * 1024 + l16);
  STAMP(8);
  f32x16 np, nn;
#pragma unroll
  for (int e = 0; e < 16; ++e) { np[e] = 0.f; nn[e] = 0.f; }
  np = mfma(kf[0], ef[0][0], np); nn = mfma(kf[2], ef[1][0], nn);
  np = mfma(kf[1], ef[0][1], np); nn = mfma(kf[3], ef[1][1], nn);
  const float rp = __builtin_amdgcn_rcpf(np[13]), rn = __builtin_amdgcn_rcpf(nn[13]);
  const float inv = __builtin_amdgcn_rcpf(np[12] * rp + nn[12] * rn + 1e-6f) * gate;
  const float cp = swap_hi(rp * inv, h), cn = swap_hi(rn * inv, h);
  f32x16 o;
#pragma unroll
  for (int e = 0; e < 16; ++e) o[e] = np[e] * cp + nn[e] * cn;
  u32x4 o0 = pack8(o, 0), o1 = pack8(o, 1);
  const float omg = 1.f - gate, oh = u2f(cvtpk(omg, 0.f) << 16);
  o1.z = h ? cvtpk(oh, omg - oh) : o1.z; o1.w = h ? 0u : o1.w;
  stg_sc1(not_ones(o0), l16, ws, WS_O + (rt32 * 32 + head * 2) * 1024);
  stg_sc1(not_ones(o1), l16, ws, WS_O + (rt32 * 32 + head * 2 + 1) * 1024);
}
DI void dump_tile(const f32x16 (&acc)[2][2], u32 base, int lane) {
  const u32 r = lane & 31, a = base + (r >> 3) * 8192 + (r & 7) * 256 + (((lane >> 5) ^ (r & 7)) * 16);
#pragma unroll
  for (int g4 = 0; g4 < 4; ++g4) {
    u32 ag = a ^ (g4 * 32); OPAQUE(ag);
#pragma unroll
    for (int c = 0; c < 2; ++c)
#pragma unroll
      for (int t = 0; t < 2; ++t)
        sts128(ag + t * 32768 + c * 128, __builtin_bit_cast(u32x4, f32x4{acc[c][t][4 * g4], acc[c][t][4 * g4 + 1], acc[c][t][4 * g4 + 2], acc[c][t][4 * g4 + 3]}));
  }
  asm volatile("s_waitcnt lgkmcnt(0)\n\ts_barrier" ::: "memory");
}
template <class Hook> DI void store_q(const f32x16 (&acc)[4], srd_t ws, u32 soff, int lane, Hook hook) {
#pragma unroll
  for (int c = 0; c < 6; ++c) stg_sc1(pack8(acc[c >> 1], c & 1), lane * 16, ws, soff + c * 1024);
}
DI u32 brow_off(int lane) { const u32 row = lane >> 3; return row * 896 + ((lane & 7) ^ (((row >> 1) & 1) * 4)) * 16; }
struct BPath { u32 wa, ra[2]; };
DI BPath make_bpath(u32 base, int lane) {
  const u32 h = lane >> 5, cb = (lane >> 4) & 1, q = (lane & 15) >> 2, pp = lane & 3;
  BPath t;
  t.wa = base + lane * 16; OPAQUE(t.wa);
#pragma unroll
  for (int c = 0; c < 2; ++c) { t.ra[c] = base + (4 * h + q) * 128 + ((c ^ ((q >> 1) & 1)) * 64) + cb * 32 + pp * 8; OPAQUE(t.ra[c]); }
  return t;
}

template <int PE, class Hook> DI void epi_h(const f32x16 (&acc)[4], srd_t ws, u32 soff, int lane, Hook hook) {
  hook(-1);
#pragma unroll
  for (int c = 0; c < 4; ++c) {
    float hv[8];
#pragma unroll
    for (int e = 0; e < 8; ++e) {
      const float x = acc[c][e];
      const float t = __builtin_amdgcn_exp2f(x * __builtin_fmaf(x * x, -0.10294323970f, -2.30220819814f));
      hv[e] = x * __builtin_amdgcn_rcpf(1.f + t) * acc[c][e + 8];
    }
    u32x4 hp = u32x4{cvtpk(hv[0], hv[1]), cvtpk(hv[2], hv[3]), cvtpk(hv[4], hv[5]), cvtpk(hv[6], hv[7])};
    if (PE != 3) { asm volatile("s_setprio 3" : "+v"(hp)); }
    stg_sc1(hp, lane * 16, ws, soff + c * 1024);
    hook(c);
    if (PE != 3) asm volatile("s_setprio %0" ::"n"(PE));
  }
}

template <bool P1, bool isH> DI void body(const Params& p, const int wave) {
  const int tid = threadIdx.x, lane = tid & 63;
  const int w = wave & 3;
  const int b = blockIdx.x, xcd = b & 7, bi = b >> 3;
  const int m = 8 * (xcd >> 2) + (bi & 7), sl = (xcd & 1) * 4 + (bi >> 3), half = (xcd >> 1) & 1;
  const int tl = half * 56 + (xcd & 1) * 32 + bi, rt64 = 16 * (xcd >> 2) + tl / 7, ct2 = tl % 7;
  const int j = 7 * half + sl;
  const int kvhead = 8 * half + sl;
  const int h = lane >> 5;
  const u32 r = lane & 31, l4 = (lane & 3) * 4, l16 = lane * 16;
  const u32 cb = (lane >> 4) & 1, q = (lane & 15) >> 2, pp = lane & 3;
  const srd_t ws = mksrd(p.ws, WS_END), sqkv = mksrd(p.wqkv, D * 3 * D * 2);
  const int rt32 = m * 4 + w;
  STAMP(0);

  if (P1) __builtin_amdgcn_s_setprio(PRIO0);
  const u32 zm = L_Z + wave * 512;
  u32 z1 = L_Z + (lane & 3) * 512 + 256, z2 = z1 + 2048, oob = OOB;
  OPAQUE(z1); OPAQUE(z2); OPAQUE(oob);
  Sync y{lane == 0 ? L_CNT + w * 4 : L_DUM + lane * 4, L_CNT + l4, 1u};
  OPAQUE(y.pa); OPAQUE(y.ra); OPAQUE(y.one);
  u32x2 ev;
  asm volatile("buffer_load_dwordx2 %0, off, %1, 0 sc1" : "=v"(ev) : "s"(ws) : "memory");

  if (P1) {
    u32 fvo, sink = 0, tvo2;
    const srd_t st2 = mksrd(w == 0 ? p.wg : w == 1 ? p.wu : w == 2 ? p.wd : p.wo, w < 3 ? D * DF * 2 : D * D * 2);
    {
      const u32 scr = L_K3 + w * 1024;
      const int km = rt32;
      dma_buf(scr, km < 56 ? (lane >> 2) * 2688 + (lane & 3) * 16 : OOB, sqkv, (km >> 1) * 16 * 2688 + (1 + (km & 1)) * 896 + kvhead * 56);
      u32x4 gwf;
      ldg(gwf, r == 30 ? h * 16 : OOB, mksrd(p.gw, D * 2), (km >> 1) * 32);
      {
        const u32 row = (xcd >> 2) * 224 + bi * 7 + (lane >> 2), c4 = lane & 3;
        const u32 wv = (lane < 28 && !((xcd & 1) && c4 == 3)) ? row * 3584 + (half * 7 + (xcd & 1) * 4 + c4) * 128 : OOB;
        const u32 tvo1 = w < 2 ? ((PFM & 1) ? wv : OOB) : w == 2 ? ((PFM & 4) && lane < 56 ? ((xcd * 32 + bi) * 56 + lane) * 128 : OOB)
                               : ((PFM & 8) && (xcd & 1) && lane < 28 ? row * 2688 + (half * 3 + c4) * 128 : OOB);
        touch(sink, tvo1, w < 2 ? st2 : w == 2 ? mksrd(p.x, S * D * 2) : sqkv);
        tvo2 = w < 2 ? wv + 14 * 128 : w == 2 ? (lane < 49 ? (b * 49 + lane) * 128 : OOB) : (lane < 13 ? (b * 13 + lane) * 128 : OOB);
        if (T2S < 0) touch(sink, tvo2, st2);
      }
      const u32x4 ones = {~0u, ~0u, ~0u, ~0u};
      stg_sc1(ones, l16, ws, WS_KVP + ((kvhead * 16 + m) * 4 + w) * 1024);
      __builtin_amdgcn_s_setprio(3);
      asm volatile("s_waitcnt vmcnt(%2)" : "+v"(ev), "+v"(gwf) : "n"(T2S < 0 ? 3 : 2) : "memory");
      u32x4 kt;
      if (w & 1) {
        const u32 a = scr + (8 * h + q) * 64 + cb * 32 + 8 * pp;
        kt = trfrag(a, a + 256);
        if (r >= 28) kt = gwf;
      } else {
        z_wait<0>(z2);
        u32x4 wp[2]; load_wphi(wp, L_WPHI, lane);
        const u32 a = scr + ((lane & 3) + ((lane >> 1) & 4) + ((lane << 1) & 8)) * 64 + h * 8;
        f32x16 t;
#pragma unroll
        for (int e = 0; e < 16; ++e) t[e] = 0.f;
#pragma unroll
        for (int s = 0; s < 2; ++s) {
          const u32x2 lo = *(LAS u32x2*)(size_t)(a + s * 32), hi = *(LAS u32x2*)(size_t)(a + s * 32 + 16);
          t = mfma(u32x4{lo.x, lo.y, hi.x, hi.y}, wp[s], t);
        }
#pragma unroll
        for (int e = 0; e < 8; ++e) t[e] *= LOG2E;
        kt = pack8(t, 0);
      }
      if (m == 15) kt = ones;
      stg_sc1(kt, m != 14 ? l16 : OOB, ws, m == 15 ? WS_KVF + (kvhead * 4 + w) * 1024 : WS_KT + (kvhead * 56 + km) * 1024);
      fvo = lane == 0 ? WS_FK + (kvhead * 64 + km) * 8 : OOB;
    }
    const u64 tag = (((u64)rfl(ev.y) << 32) | rfl(ev.x)) + 1;
    STAMP(1);
    u32x4 xf[28];
    f32x16 acc[4];
    u32 done = 0;
    set_m0(zm);
    unit_gemm<isH, true>(xf, acc, lane, w, z2, [&](auto gc) {
      constexpr int s = decltype(gc)::v >> 4, n = decltype(gc)::v & 15;
      if constexpr (n == 15) z_post<s>(oob, ws);
      if constexpr (s < 3 && (n == 3 || n == 9)) flag_try<s == 2 && n == 9, s>(done, ws, fvo, tag);
      if constexpr (s == T2S && n == 11) touch(sink, tvo2, st2);
    } STAMP_PASS);
    STAMP(2);
    __builtin_amdgcn_s_setprio(isH ? PRIOE : 3);
    {
      const u32x4 ones = {~0u, ~0u, ~0u, ~0u};
      stg_sc1(ones, l16, ws, WS_O + (rt32 * 32 + kvhead * 2) * 1024);
      stg_sc1(ones, l16, ws, WS_O + (rt32 * 32 + kvhead * 2 + 1) * 1024);
    }
    if (isH) epi_h<PRIOE>(acc, ws, WS_H + (rt32 * 112 + j * 4) * 1024, lane, [](int) {});
    u32x4 qa[8];
    if (!isH) {
#pragma unroll
      for (int c = 0; c < 8; ++c) { qa[c] = pack8(acc[c >> 1], c & 1); OPAQUE(qa[c]); }
    }
    __builtin_amdgcn_s_setprio(isH ? PRIO1 : PRIOQ);
    STAMP(3);
    done = 0;
    fvo = lane != 0 ? OOB : isH ? WS_FH + (rt32 * 16 + j) * 128 : WS_FQ + (rt32 * 4 + half * 2) * 128;
    u64 fk = 0;
    const u32 oh = w * 4 + ((lane >> 3) & 3), ort = rt64 * 2 + h;
    const u32 fka = !isH ? WS_FK + (m * 64 + lane) * 8 : (oh & 7) == 7 ? WS_FQ + (ort * 4 + (oh >> 3) * 2) * 128 : WS_FH + (ort * 16 + oh - (oh >> 3)) * 128;
    if (isH && PAUSE) {
      flag_try<true, 0>(done, ws, fvo, tag);
      for (int it = 0; it < 4000; ++it) {
        if (rfl(*(volatile LAS u32*)(size_t)(L_CNT + C_SYNC + w * 4)) >= PAUSE) break;
        __builtin_amdgcn_s_sleep(1);
      }
    }
    unit_gemm<isH, false>(xf, acc, lane, w, z2, [&](auto gc) {
      constexpr int s = decltype(gc)::v >> 4, n = decltype(gc)::v & 15;
      if constexpr (isH && s < 4 && (n == 3 || n == 9)) flag_try<s == 3 && n == 9, 0>(done, ws, fvo, tag);
      if constexpr (s == 4 && n == 15) lds_post<C_PROG>(y);
      if constexpr (!isH && s == 0 && n == 15) z_post<7>(oob, ws);
      if constexpr (!isH && s == 1 && n == 5) flag_peek(fk, ws, fka);
      if constexpr (!isH && s == 3 && n == 5) flag_check<0>(fk, ws, fka, tag);
      if constexpr (!isH && (s == QAS || s == QAS + 1) && (n & 3) == 1) stg_sc1(qa[(s - QAS) * 4 + (n >> 2)], l16, ws, WS_Q + (rt32 * 32 + half * 16 + (s - QAS) * 4 + (n >> 2)) * 1024);
      if constexpr (!isH && s > QAS + 1 && (n == 3 || n == 9)) flag_try<s == 6 && n == 9, 0>(done, ws, fvo, tag);
    } STAMP_PASS);
    OPAQUE(sink);
    lds_post<C_PROG>(y);
    STAMP(4);
    __builtin_amdgcn_s_setprio(isH ? PRIOF : 3);
    if (isH) flag_peek(fk, ws, fka);
    u32x4 bo[8][2];
    task_o<isH>(kvhead, rt32, w, ws, tag, lane, y, fvo + (isH ? 64 * 16 * 128 : 128), acc[3], [&](auto hook) {
      if (isH) epi_h<PRIOF>(acc, ws, WS_H + (rt32 * 112 + (14 + j) * 4) * 1024, lane, hook);
      else store_q(acc, ws, WS_Q + (rt32 * 32 + half * 16 + 8) * 1024, lane, hook);
    }, [&](auto hook) { if (!isH) reduce_kv(m, half * 4 + w, ws, lane, hook STAMP_PASS); else flag_check<1>(fk, ws, fka, tag); } STAMP_PASS);
    STAMP(10);
    if (!isH) STAMP_FLUSH();
    if (isH) {
      f32x16 oc[2][2];
#pragma unroll
      for (int i = 0; i < 4; ++i)
#pragma unroll
        for (int e = 0; e < 16; ++e) oc[i >> 1][i & 1][e] = 0.f;
      asm volatile("" : "+v"(oc[0][0]), "+v"(oc[0][1]), "+v"(oc[1][0]), "+v"(oc[1][1]));
      u32x4 ao[16];
      const u32 l16o = l16 + 32 * 1024, oso = WS_O + (rt64 * 2 * 32 + w * 8) * 1024;
#pragma unroll
      for (int i = 0; i < 16; ++i) asm volatile("" : "=v"(ao[i]));
      STAMP(14);
      if (ODLY) __builtin_amdgcn_s_sleep(ODLY);
      __builtin_amdgcn_s_setprio(PRIOO);
      u32 miss = 0xff, go = 0xff, next = 0;
      auto fetch = [&]() {
        sfor<8>([&](auto ic) {
          constexpr int i = decltype(ic)::v, hh = i >> 1, t = i & 1;
          ldg2_if<i, (hh & 1) * 2048>(ao[4 * hh + t], ao[4 * hh + 2 + t], go, t ? l16o : l16, ws, oso + (hh >> 1) * 4096);
        });
      };
      auto consume = [&](auto hc) {
        constexpr int hh = decltype(hc)::v;
        if ((go >> (2 * hh)) & 3) {
#pragma unroll
          for (int t = 0; t < 2; ++t) {
            const u32x4 two[2] = {ao[4 * hh + t], ao[4 * hh + 2 + t]};
            if (arrived(two)) miss &= ~(go & (1u << (2 * hh + t)));
          }
        }
        if (next == hh && !((miss >> (2 * hh)) & 3)) {
#pragma unroll
          for (int u = 0; u < 8; ++u) oc[(u >> 1) & 1][u & 1] = mfma(bo[2 * hh + (u >> 2)][(u >> 1) & 1], ao[4 * hh + (u >> 2) * 2 + (u & 1)], oc[(u >> 1) & 1][u & 1]);
          next = hh + 1;
        }
      };
      fetch();
      z_wait<18>(L_Z + 2048 + w * 512 + 256);
      {
        const BPath bp = make_bpath(L_R1 + w * 2048, lane);
#pragma unroll
        for (int i = 0; i < 16; ++i) bo[i >> 1][i & 1] = trfrag(bp.ra[i & 1] + (i >> 1) * 8192, bp.ra[i & 1] + (i >> 1) * 8192 + 1024);
      }
      sfor<4>([&](auto hc) { constexpr int hh = decltype(hc)::v; landed4<12 - 4 * hh>(ao[4 * hh], ao[4 * hh + 1], ao[4 * hh + 2], ao[4 * hh + 3]); consume(hc); });
      STAMP(13);
      go = __builtin_popcount(miss) <= ODIR ? miss : 0;
      const u32 wv = ((lane >> 3) & 1) * 32768 + (lane >> 4) * 2048 + 1024 + (((lane >> 3) + w) & 7) * 128;
      for (int it = 0; it < 20000 && next < 4; ++it) {
        u32 v;
        fetch();
        asm volatile("buffer_load_dword %0, %1, %2, %3 offen sc1" : "=v"(v) : "v"((miss & ~go) ? wv : OOB), "s"(ws), "s"(oso) : "memory");
        asm volatile("s_waitcnt vmcnt(0)" : "+v"(ao[0]), "+v"(ao[1]), "+v"(ao[2]), "+v"(ao[3]), "+v"(ao[4]), "+v"(ao[5]), "+v"(ao[6]), "+v"(ao[7]), "+v"(ao[8]), "+v"(ao[9]),
                     "+v"(ao[10]), "+v"(ao[11]), "+v"(ao[12]), "+v"(ao[13]), "+v"(ao[14]), "+v"(ao[15]), "+v"(v)::"memory");
        const u64 em = __builtin_amdgcn_ballot_w64(v == 0xffffffffu) & 0x0101010101010101ull;
        const u32 empty = (u32)((em * 0x0102040810204080ull) >> 56);
        sfor<4>(consume);
        go = __builtin_popcount(miss) <= ODIR ? miss : miss & ~empty;
      }
#pragma unroll
      for (int t = 0; t < 2; ++t) *(LAS float*)(size_t)(h ? L_GW + (t * 32 + r) * 4 : L_DUM + r * 4) = u2f(ao[2 + t].z << 16) + u2f(ao[2 + t].z & 0xffff0000u);
      STAMP(15);
      dump_tile(oc, L_R1 + w * 2048, lane);
      flag_set(ws, (b | w | lane) ? OOB : WS_E, tag);
    }
  } else {
    using T = TB<isH>;
    const srd_t sx = mksrd(p.x, S * D * 2);
    {
      const srd_t sp = mksrd(p.wphi, 16 * 1568);
      if (w < 2) dma_buf(L_WPHI + w * 1024, l16, sp, kvhead * 1568 + w * 1024);
    }
    constexpr u32 wrs = isH ? 3584 : 2688, wu2 = isH ? 14 * 128 : 4 * 56;
    u32 xvo[4], wvo[4];
#pragma unroll
    for (int i = 0; i < 4; ++i) {
      xvo[i] = (lane >> 3) * 896 + (((lane & 7) ^ ((4 * i + (lane >> 4)) & 7)) * 16) + i * (8 * 896 - 1024);
      if (isH) wvo[i] = (lane >> 3) * 3584 + ((((lane >> 1) & 3) ^ (((lane >> 3) & 2) | (w >> 1))) * 32) + (lane & 1) * 16 + i * (8 * 3584 - 1024);
      else     wvo[i] = (lane >> 4) * 2688 + ((((lane >> 1) & 3) ^ (lane >> 4)) * 56) + ((lane >> 3) & 1) * 32 + (lane & 1) * 16 + i * (4 * 2688 - 1024);
    }
    const srd_t sw = isH ? mksrd(w < 2 ? p.wg : p.wu, D * DF * 2) : sqkv;
    const u32 xso = (m * 128 + w * 32) * 896;
    const u32 wso1 = isH ? (w & 1) * 32 * 3584 + j * 128 : w * 16 * 2688 + 8 * half * 56;
    const u32 mq = w * 4096, mq2 = w * 2048;
    const u32 kso4 = WS_KT + kvhead * 56 * 1024 + mq, kso2 = WS_KT + kvhead * 56 * 1024 + mq2;
    const u32 l16h = l16 + 112 * 1024;
    const u32 hso = WS_H + (rt64 * 2 * 112 + w * 14) * 1024, bso = w * 14 * 16 * 896 + ct2 * 128;
    const srd_t swd = mksrd(p.wd, D * DF * 2);
    const u32 bvo = brow_off(lane);
    auto fadr = [&](int k) -> u32 {
      return k == 0 ? WS_FK + (kvhead * 64 + lane) * 8 : WS_FH + (((k - 2) * 64 + rt64 * 2 + h) * 16 + ((w * 14) >> 2) + ((lane >> 3) & 3)) * 128;
    };
    u32 so = 0, pre = 0, fa = 0;
    u64 fv = 0, tag = 0;
    u32x4 xf[28], af[28][2], bf[19][2], kvs;
    const srd_t swo = mksrd(p.wo, D * D * 2);
    const u32 bvo1 = bvo + 8 * 896 - 1024, bvop = lane < 32 ? bvo1 : OOB;
    const u32 xb = w * 4096 + r * 128 + ((h ^ ((r >> 1) & 7)) * 16);
    constexpr u32 L_PHI = slot_of(isH, OK, 1) * 16384, L_VF = slot_of(isH, OK, 2) * 16384;
    struct { u32 pw[4], pr[2], vw, vr, lr, lw, so; } ea;
    float keep;
    auto op = [&](auto kc) {
      constexpr int K = decltype(kc)::v;
      constexpr Op o = T::t.v[K];
      if constexpr (T::need(K) >= 0) z_wait_pre<T::need(K)>(z1, pre);
      if constexpr (isH && o.kind == OU && o.idx == 6 && o.i == 0) z_wait<zidx(isH, OU, 3)>(z2);
      constexpr int chk = o.i != 0 ? -1 : (o.kind == OK && o.idx == 0) ? 0 : (o.kind == OA && o.idx == 0) ? 2
                        : (o.kind == OA && o.idx == 14) ? 3 : -1;
      if constexpr (chk >= 0) { flag_check<K - 1 - T::find(OPEEK, chk, 0)>(fv, ws, fa, tag); STAMP(11 + chk); }
      if constexpr (o.kind <= OK) {
        constexpr u32 sb = slot_of(isH, o.kind, o.idx) * 16384;
        if constexpr (o.i == 0) {
          if constexpr (o.kind == OX) so = dma_open<sb, o.idx * 128>(mq, xso);
          else if constexpr (o.kind == OK) so = dma_open<sb, o.idx * 16384>(o.idx < 3 ? mq : mq2, o.idx < 3 ? kso4 : kso2);
          else so = dma_open<sb, o.idx * 64 * wrs + (o.kind == OW ? 0u : wu2)>(mq, wso1);
        }
        if constexpr (o.kind == OK) dma_off_sc1<o.i * 1024>(l16, ws, so);
        else dma_off<o.i * 1024>(o.kind == OX ? xvo[o.i] : wvo[o.i], o.kind == OX ? sx : sw, so);
      } else if constexpr (o.kind == OZ) { set_m0(zm); z_post<o.idx>(oob, ws); }
      else if constexpr (o.kind == OPEEK) { fa = fadr(o.idx); flag_peek(fv, ws, fa); }
      else if constexpr (o.kind == OB) {
        if constexpr (o.idx < 19) ldg(bf[o.idx][o.i], bvo, swd, bso + ((o.idx >= 14 ? o.idx + 42 : o.idx) * 16 + o.i * 8) * 896);
        else {
          if constexpr (o.idx == 31 && o.i == 0) lds_wait<C_SYNC, 3>(y);
          if constexpr (o.idx == 35 && o.i == 0) lds_wait<C_PROG, 1>(y);
          if constexpr (o.idx == 28 && o.i == 0) lds_wait<C_PROG, 2>(y);
          if constexpr (o.i == 0) {
            if constexpr (o.idx < 28) so = dma_open<L_R2 + (o.idx - 19) * 8192, (o.idx + 42) * 16 * 896>(mq2, bso);
            else so = dma_open<L_R1 + (o.idx - 28) * 8192, (((o.idx - 28) >> 1) * 28 + (o.idx & 1) * 16) * 896>(mq2, w * 112 * 896 + ct2 * 128);
          }
          dma_off<o.i * 1024>(o.i == 0 ? bvo : (o.idx >= 28 && (o.idx & 1)) ? bvop : bvo1, o.idx < 28 ? swd : swo, so);
        }
      }
      else if constexpr (o.kind == OA) {
        constexpr int ks = o.idx >= 14 ? o.idx + 42 : o.idx;
        ldg_sc1o<(ks & 3) * 1024>(af[o.idx][o.i], o.i ? l16h : l16, ws, hso + (ks >> 2) * 4096);
      } else if constexpr (o.kind == OST) stg_sc1(not_ones(kvs), ea.so, ws, WS_KVP + ((kvhead * 16 + m) * 4 + w) * 1024);
      if constexpr (T::need(K + 3) >= 0) pre = z_peek<T::need(K + 3)>(z1);
      constexpr int J = K - LAG;
      if constexpr (J >= 0) {
        constexpr Op oj = T::t.v[J];
        if constexpr (oj.kind == OX && oj.i == 3) {
          if constexpr (J == 3) {
            asm volatile("s_waitcnt vmcnt(%1)" : "+v"(ev) : "n"(LAG) : "memory");
            tag = (((u64)rfl(ev.y) << 32) | rfl(ev.x)) + 1;
          } else VMWAIT(LAG);
#pragma unroll
          for (int kk = 0; kk < 4; ++kk) xf[4 * oj.idx + kk] = lds128(slot_of(isH, OX, oj.idx) * 16384 + (xb ^ (kk * 32)));
          FSTAMP(16 + oj.idx);
        }
      }
      SB();
    };
    auto ops = [&](auto ac, auto bc) { sfor<decltype(bc)::v - decltype(ac)::v>([&](auto kc) { op(IC<decltype(ac)::v + decltype(kc)::v>{}); }); };
#define OPS(a, b) ops(IC<(a)>{}, IC<(b)>{})
#define MK(m) (T::t.mark[m])
#define TICK(M, n, N) OPS(MK(M) + (MK((M) + 1) - MK(M)) * (n) / (N), MK(M) + (MK((M) + 1) - MK(M)) * ((n) + 1) / (N))
#define ETICK(n) OPS(T::etick(n), T::etick((n) + 1))
    f32x16 ka, va;
#pragma unroll
    for (int e = 0; e < 16; ++e) { ka[e] = 0.f; va[e] = r == 28 ? 1.f : 0.f; }
    __builtin_amdgcn_s_setprio(PRIO2);
    OPS(0, 36);
    {
      const u32 t0 = 4 * cb + q, ra = L_PHI + (w >> 1) * 2048 + (16 * h + t0) * 64;
#pragma unroll
      for (int g4 = 0; g4 < 4; ++g4) { ea.pw[g4] = L_PHI + w * 4096 + r * 64 + (((2 * g4 + h) ^ ((r >> 1) & 7)) * 8); OPAQUE(ea.pw[g4]); }
      ea.pr[0] = ra + (((4 * (w & 1) + pp) ^ (t0 >> 1)) * 8); ea.pr[1] = ra + 512 + (((4 * (w & 1) + pp) ^ ((t0 >> 1) + 4)) * 8);
      ea.vw = L_VF + w * 2048 + l16; ea.vr = L_VF + h * 1024 + ((lane & 15) + 32 * cb) * 16;
      ea.lr = L_VF + w * 2048 + (r >> 4) * 1024 + (30 + 32 * ((r >> 2) & 1)) * 16 + (((r >> 3) & 1) * 4 + (r & 3)) * 2;
      ea.lw = L_GW + 256 + (w * 32 + r) * 4; ea.so = (h * 16 + (lane & 15) + 32 * cb) * 16;
      keep = h ? 0.f : 1.f;
      OPAQUE(ea.pr[0]); OPAQUE(ea.pr[1]); OPAQUE(ea.vw); OPAQUE(ea.vr); OPAQUE(ea.lr); OPAQUE(ea.lw); OPAQUE(ea.so); OPAQUE(keep);
      asm volatile("" : "+v"(va));
    }
    OPS(36, MK(M_KV0));
    STAMP(1);
    {
      u32 ak[4];
#pragma unroll
      for (int t = 0; t < 4; ++t) { ak[t] = slot_of(isH, OK, t) * 16384 + l16; OPAQUE(ak[t]); }
      u32x4 fk[3], fw[3];
      u32 pk = 0;
      auto reads = [&](int ks) { fk[ks % 3] = lds128(ak[ks >> 3] + (ks & 7) * 2048); fw[ks % 3] = lds128(ak[ks >> 3] + (ks & 7) * 2048 + 1024); };
      z_wait<zidx(isH, OK, 0)>(z2);
      reads(0); reads(1); reads(2);
      SB();
      sfor<28>([&](auto kc) {
        constexpr int ks = decltype(kc)::v, t = ks >> 3;
        ka = mfma(fk[ks % 3], xf[ks], ka);
        SB();
        asm volatile("" : "+v"(ka));
        TICK(M_KV0, ks, 28);
        va = mfma(xf[ks], fw[ks % 3], va);
        SB();
        asm volatile("" : "+v"(va));
        if constexpr ((ks & 7) == 3 && t < 3) pk = z_peek<zidx(isH, OK, t + 1)>(z2);
        if constexpr ((ks & 7) == 5) z_wait_pre<zidx(isH, OK, t + 1)>(z2, pk);
        if constexpr (ks + 3 < 28) reads(ks + 3);
        SB();
      });
    }
    STAMP(3);
    {
      ETICK(0);
      float mx = max3(max3(max3(ka[0], ka[1], ka[2]), max3(ka[3], ka[4], ka[5]), max3(ka[6], ka[7], ka[8])),
                      max3(max3(ka[9], ka[10], ka[11]), max3(ka[12], ka[13], ka[14]), ka[15]), ka[15]);
      asm volatile("" : "+v"(mx));
      ETICK(1);
      float mn = min3(min3(min3(ka[0], ka[1], ka[2]), min3(ka[3], ka[4], ka[5]), min3(ka[6], ka[7], ka[8])),
                      min3(min3(ka[9], ka[10], ka[11]), min3(ka[12], ka[13], ka[14]), ka[15]), ka[15]);
      asm volatile("" : "+v"(mn));
      ETICK(2);
      { float a, c; both_halves(mx, a, c); mx = vmax(a, c); both_halves(mn, a, c); mn = vmin(a, c); }
      asm volatile("" : "+v"(mx), "+v"(mn));
      ETICK(3);
      u32x4 vf[2] = {pack8(va, 0), pack8(va, 1)}, vv[4][2];
      asm volatile("" : "+v"(vf[0]), "+v"(vf[1]));
      ETICK(4);
      u32 lg = 0;
      sfor<2>([&](auto sc) {
        constexpr int sg = decltype(sc)::v, E0 = 5 + 10 * sg;
        f32x16 e;
        float ps[4];
        sfor<4>([&](auto gc) {
          constexpr int g4 = decltype(gc)::v;
#pragma unroll
          for (int i = 4 * g4; i < 4 * g4 + 4; ++i) e[i] = __builtin_amdgcn_exp2f(sg == 0 ? ka[i] - mx : mn - ka[i]);
          if constexpr (g4 > 0) { ps[g4 - 1] = (e[4 * g4 - 4] + e[4 * g4 - 3]) + (e[4 * g4 - 2] + e[4 * g4 - 1]); asm volatile("" : "+v"(ps[g4 - 1])); }
          float t = e[4 * g4 + 3]; asm volatile("" : "+v"(t)); e[4 * g4 + 3] = t;
          if constexpr (g4 == 2) pre = sg == 0 ? z_peek<(isH ? zidx(isH, OU, 6) : 18)>(z2) : lds_peek<C_SYNC>(y);
          ETICK(E0 + g4);
        });
        ps[3] = (e[12] + e[13]) + (e[14] + e[15]);
        float a, c; both_halves(__builtin_fmaf(ps[3], keep, ps[2]) + (ps[0] + ps[1]), a, c);
        float rs = __builtin_amdgcn_rcpf(a + c);
        asm volatile("" : "+v"(rs));
        ETICK(E0 + 4);
        if constexpr (sg == 0) {
          z_wait_pre<(isH ? zidx(isH, OU, 6) : 18)>(z2, pre);
          sts128(ea.vw, vf[0]); sts128(ea.vw + 1024, vf[1]);
          lg = *(LAS unsigned short*)(size_t)ea.lr;
        } else {
          lds_wait_pre<C_SYNC, 1>(y.ra, pre);
#pragma unroll
          for (int i = 0; i < 8; ++i) vv[i >> 1][i & 1] = lds128(ea.vr + (i >> 1) * 2048 + (i & 1) * 256);
        }
        SB();
        ETICK(E0 + 5);
        sfor<4>([&](auto gc) {
          constexpr int g4 = decltype(gc)::v;
          sts64(ea.pw[g4] + sg * 2048, u32x2{cvtpk(e[4 * g4] * rs, e[4 * g4 + 1] * rs), cvtpk(e[4 * g4 + 2] * rs, e[4 * g4 + 3] * rs)});
          ETICK(E0 + 6 + g4);
        });
        if constexpr (sg == 0) *(LAS u32*)(size_t)ea.lw = lg << 16;
        lds_post<C_SYNC>(y);
      });
      pre = lds_peek<C_SYNC>(y);
      ETICK(25);
      lds_wait_pre<C_SYNC, 2>(y.ra, pre);
      u32x4 pf[4];
#pragma unroll
      for (int u = 0; u < 4; ++u) pf[u] = trfrag(ea.pr[0] + u * 4096, ea.pr[1] + u * 4096);
      lds_post<C_SYNC>(y);
      SB();
      ETICK(26);
      f32x4 kp[2] = {{0.f, 0.f, 0.f, 0.f}, {0.f, 0.f, 0.f, 0.f}};
#pragma unroll
      for (int i = 0; i < 8; ++i)
        kp[i & 1] = __builtin_amdgcn_mfma_f32_16x16x32_bf16(__builtin_bit_cast(bf16x8, pf[i >> 1]), __builtin_bit_cast(bf16x8, vv[i >> 1][i & 1]), kp[i & 1], 0, 0, 0);
      asm volatile("" : "+v"(kp[0]), "+v"(kp[1]));
      ETICK(27);
      const auto sx0 = __builtin_amdgcn_permlane32_swap(cvtpk(kp[0].x, kp[0].y), cvtpk(kp[1].x, kp[1].y), false, false);
      const auto sx1 = __builtin_amdgcn_permlane32_swap(cvtpk(kp[0].z, kp[0].w), cvtpk(kp[1].z, kp[1].w), false, false);
      kvs = u32x4{sx0[0], sx1[0], sx0[1], sx1[1]};
      ETICK(28);
      OPS(MK(M_ST), MK(M_ST) + 1);
    }
    STAMP(4);
    if constexpr (!isH) {
      STAMP(7);
      STAMP_FLUSH();
    } else {
      f32x16 acc[2][2];
#pragma unroll
      for (int i = 0; i < 4; ++i)
#pragma unroll
        for (int e = 0; e < 16; ++e) acc[i >> 1][i & 1][e] = 0.f;
      BPath bp = make_bpath(L_K3 + w * 2048, lane);
      u32x4 tf[2][2];
      asm volatile("s_waitcnt vmcnt(%2)" : "+v"(bf[0][0]), "+v"(bf[0][1]) : "n"(MK(M_ST) - T::find(OB, 0, 1)) : "memory");
      sts128(bp.wa, bf[0][0]); sts128(bp.wa + 1024, bf[0][1]);
      tf[0][0] = trfrag(bp.ra[0], bp.ra[0] + 1024); tf[0][1] = trfrag(bp.ra[1], bp.ra[1] + 1024);
      OPS(MK(M_ST) + 1, MK(M_JOB));
      __builtin_amdgcn_s_setprio(PRIO3);
      u32x4 lf[14][2];
      sfor<28>([&](auto nc) {
        constexpr int n = decltype(nc)::v, P = MK(M_JOB + n), NL = MK(M_JOB + n + 1) - P, nb = n < 13 ? n + 1 : 13;
        constexpr int need = cmax(T::find(OA, n, 1), T::find(OB, nb, 1));
        static_assert(need < P, "stream order");
        if constexpr (n == 14) {
          static_assert(T::find(OB, 18, 1) < T::find(OPEEK, 3, 0) && T::find(OB, 27, 1) < T::find(OPEEK, 3, 0), "landed behind the flag check");
#pragma unroll
          for (int k = 0; k < 5; ++k) {
            pin2(bf[14 + k][0], bf[14 + k][1]);
            sts128(bp.wa, bf[14 + k][0]); sts128(bp.wa + 1024, bf[14 + k][1]);
            lf[k][0] = trfrag(bp.ra[0], bp.ra[0] + 1024); lf[k][1] = trfrag(bp.ra[1], bp.ra[1] + 1024);
          }
#pragma unroll
          for (int k = 0; k < 9; ++k)
#pragma unroll
            for (int u = 0; u < 2; ++u) {
              if (k == 0) { bp.ra[u] += L_R2 - L_K3; pin1(bp.ra[u]); }
              if (k == 4) { bp.ra[u] += 4 * 8192; pin1(bp.ra[u]); }
              lf[5 + k][u] = trfrag(bp.ra[u] + (k < 4 ? k : k - 4) * 8192, bp.ra[u] + (k < 4 ? k : k - 4) * 8192 + 1024);
            }
          SB();
        }
        if constexpr (n < 13) landed4<P - 1 - need>(af[n][0], af[n][1], bf[nb][0], bf[nb][1]);
        else { VMWAIT(P - 1 - need); pin2(af[n][0], af[n][1]); }
        sfor<4>([&](auto uc) {
          constexpr int u = decltype(uc)::v, c = u >> 1, t = u & 1;
          if constexpr (n < 14) acc[c][t] = mfma(tf[n & 1][c], af[n][t], acc[c][t]); else acc[c][t] = mfma(lf[n - 14][c], af[n][t], acc[c][t]);
          SB();
          asm volatile("" : "+v"(acc[c][t]));
          if constexpr (n < 13) {
            if constexpr (u == 0) {
              sts128(bp.wa, bf[nb][0]); sts128(bp.wa + 1024, bf[nb][1]);
              tf[nb & 1][0] = trfrag(bp.ra[0], bp.ra[0] + 1024); tf[nb & 1][1] = trfrag(bp.ra[1], bp.ra[1] + 1024);
            }
            SB();
          }
          if constexpr (n != 13) OPS(P + NL * u / 4, P + NL * (u + 1) / 4); else if constexpr (u == 3) OPS(P, P + NL);
        });
        if constexpr (n == 12) STAMP(6);
      });
      STAMP(10);
      dump_tile(acc, L_R2 + w * 2048, lane);
    }
  }
  if (isH) {
    const int tok = tid >> 3, pc = tid & 7;
    const u32 a = (tok >> 3) * 8192 + (tok & 7) * 256 + (((pc * 2) ^ (tok & 7)) * 16);
    f32x4 v[8][2];
#pragma unroll
    for (int u = 0; u < 8; ++u)
#pragma unroll
      for (int i = 0; i < 2; ++i) v[u][i] = __builtin_bit_cast(f32x4, lds128((u < 4 ? L_R2 + u * 2048 : L_R1 + (u - 4) * 2048) + (a ^ (i * 16))));
    const float omg = *(LAS float*)(size_t)(L_GW + tok * 4);
    const f32x4 s0 = ((v[0][0] + v[1][0]) + (v[2][0] + v[3][0])) * omg + ((v[4][0] + v[5][0]) + (v[6][0] + v[7][0]));
    const f32x4 s1 = ((v[0][1] + v[1][1]) + (v[2][1] + v[3][1])) * omg + ((v[4][1] + v[5][1]) + (v[6][1] + v[7][1]));
    const u32x4 res = {cvtpk(s0.x, s0.y), cvtpk(s0.z, s0.w), cvtpk(s1.x, s1.y), cvtpk(s1.z, s1.w)};
    if (OUTSC) stg_sc1(res, tok * 896 + pc * 16, mksrd(p.out, S * D * 2), rt64 * 64 * 896 + ct2 * 128);
    else *(u32x4*)(p.out + (rt64 * 64 + tok) * 896 + ct2 * 128 + pc * 16) = res;
    STAMP(9);
    STAMP_FLUSH();
  }
}

__global__ __launch_bounds__(512, 2) void fused(Params p) {
  asm volatile(".subsection 2\n\t.fill 256, 4, 0xBF800000\n\t.subsection 0");
  const int wave = rfl(threadIdx.x >> 6);
  if (wave == 1) {
    u64 pc;
    asm volatile("s_getpc_b64 %0" : "=s"(pc));
    const u32 ln = threadIdx.x & 63;
    asm volatile("s_mov_b32 m0, %2\n\ts_nop 0\n\tglobal_load_lds_dword %0, %1" ::"v"(((ln < 12 ? ln : 0) * 32 + (blockIdx.x >> 3)) * 128), "s"(pc), "s"(L_DUM) : "memory");
  }
  *(LAS u32*)(size_t)(L_Z + (threadIdx.x >> 6) * 512 + 256 + (threadIdx.x & 63) * 4) = 1u;
  *(LAS u32*)(size_t)(L_CNT + (threadIdx.x & 15) * 4) = 0u;
  asm volatile("s_waitcnt lgkmcnt(0)\n\ts_barrier" ::: "memory");
  if (!((blockIdx.x & 1) && blockIdx.x >= 192)) {
    if (wave < 4) body<true, true>(p, wave); else body<false, true>(p, wave);
  } else {
    if (wave < 4) body<true, false>(p, wave); else body<false, false>(p, wave);
  }
}
}
#if defined(__HIP_DEVICE_COMPILE__)
#pragma clang attribute pop
#endif

extern "C" void kernel_launch(void* const* d_in, const int* in_sizes, int n_in, void* d_out, int out_size, void* d_ws, size_t ws_size,
                              hipStream_t stream) {
  hk::Params p;
  p.x = (const char*)d_in[0]; p.wqkv = (const char*)d_in[1]; p.wphi = (const char*)d_in[2]; p.wo = (const char*)d_in[3];
  p.wg = (const char*)d_in[5]; p.wu = (const char*)d_in[6]; p.wd = (const char*)d_in[7]; p.gw = (const char*)d_in[8];
  p.out = (char*)d_out; p.ws = (char*)d_ws;
  (void)hipFuncSetAttribute((const void*)hk::fused, hipFuncAttributeMaxDynamicSharedMemorySize, hk::L_TOTAL);
  hk::fused<<<256, 512, hk::L_TOTAL, stream>>>(p);
}
```

```cpp
#include <hip/hip_runtime.h>
#include <stdint.h>

#if defined(__HIP_DEVICE_COMPILE__)
#pragma clang attribute push(__attribute__((target("no-packed-fp32-ops"))), apply_to = function)
#endif
namespace hk {
typedef unsigned u32;
typedef unsigned long long u64;
typedef __bf16 bf16x8 __attribute__((ext_vector_type(8)));
typedef __bf16 bf16x2 __attribute__((ext_vector_type(2)));
typedef short s16x4 __attribute__((ext_vector_type(4)));
typedef u32 u32x4 __attribute__((ext_vector_type(4)));
typedef u32 u32x2 __attribute__((ext_vector_type(2)));
typedef float f32x16 __attribute__((ext_vector_type(16)));
typedef float f32x4 __attribute__((ext_vector_type(4)));
typedef float f32x2 __attribute__((ext_vector_type(2)));
typedef __amdgpu_buffer_rsrc_t srd_t;
#define LAS __attribute__((address_space(3)))
#define DI __device__ __forceinline__

constexpr int S = 2048, D = 448, DF = 1792;
constexpr u32 WS_E = 0;
constexpr u32 WS_FK = 4096;
constexpr u32 WS_FQ = 131072;
constexpr u32 WS_FH = 393216;
constexpr u32 WS_KVF = 655360;
constexpr u32 WS_KVP = 720896;
constexpr u32 WS_O = WS_KVP + 16 * 16 * 4096;
constexpr u32 WS_Q = WS_O + 64 * 32 * 1024;
constexpr u32 WS_H = WS_Q + 64 * 32 * 1024;
constexpr u32 WS_KT = WS_H + 64 * 112 * 1024;
constexpr u32 WS_END = WS_KT + 16 * 56 * 1024;
constexpr u32 OOB = 0x7ff00000u;
constexpr u32 L_K3 = 9 * 16384;
constexpr u32 L_WPHI = L_K3 + 8192;
constexpr u32 L_GW = L_WPHI + 2048;
constexpr u32 L_Z = L_GW + 1024;
constexpr u32 L_CNT = L_Z + 4096;
constexpr u32 L_DUM = L_CNT + 64;
constexpr u32 L_TOTAL = 163840;
enum { C_PROG = 0, C_SYNC = 16, C_KVF = 32 };
constexpr u32 L_KVF = 143360;

struct Params {
  const char *x, *wqkv, *wphi, *wo, *wg, *wu, *wd, *gw;
  char *out, *ws;
};

DI u32 f2u(float f) { return __builtin_bit_cast(u32, f); }
DI float u2f(u32 u) { return __builtin_bit_cast(float, u); }
DI u32 min(u32 a, u32 b) { return a < b ? a : b; }
DI u32 max(u32 a, u32 b) { return a > b ? a : b; }
DI u32 rfl(u32 v) { return __builtin_amdgcn_readfirstlane(v); }
DI srd_t mksrd(const void* p, u32 bytes) { return __builtin_amdgcn_make_buffer_rsrc((void*)p, 0, bytes, 0x00020000); }
DI u32 cvtpk(float lo, float hi) { f32x2 v = {lo, hi}; return __builtin_bit_cast(u32, __builtin_convertvector(v, bf16x2)); }
DI f32x16 mfma(u32x4 a, u32x4 b, f32x16 c) {
  return __builtin_amdgcn_mfma_f32_32x32x16_bf16(__builtin_bit_cast(bf16x8, a), __builtin_bit_cast(bf16x8, b), c, 0, 0, 0);
}
DI u32x2 tr64(u32 addr) { return __builtin_bit_cast(u32x2, __builtin_amdgcn_ds_read_tr16_b64_v4i16((LAS s16x4*)(size_t)addr)); }
DI u32x4 trfrag(u32 a0, u32 a1) { u32x2 lo = tr64(a0), hi = tr64(a1); return u32x4{lo.x, lo.y, hi.x, hi.y}; }
DI u32x4 lds128(u32 addr) { return *(LAS u32x4*)(size_t)addr; }
DI void sts128(u32 addr, u32x4 v) { *(LAS u32x4*)(size_t)addr = v; }
DI void sts64(u32 addr, u32x2 v) { *(LAS u32x2*)(size_t)addr = v; }
#define VMWAIT(n) asm volatile("s_waitcnt vmcnt(%0)" ::"n"(n) : "memory")
DI void dma_buf(u32 ldsaddr, u32 voff, srd_t srd, u32 soff) {
  asm volatile("s_mov_b32 m0, %0\n\ts_nop 0\n\tbuffer_load_dwordx4 %1, %2, %3 offen lds" ::"s"(ldsaddr), "v"(voff), "s"(srd), "s"(soff) : "memory");
}
template <u32 IMM, u32 SIMM> DI u32 dma_open(u32 base, u32 sbase) {
  u32 so;
  asm volatile("s_add_u32 m0, %1, %2\n\ts_add_u32 %0, %3, %4" : "=s"(so) : "s"(base), "n"(IMM), "s"(sbase), "n"(SIMM) : "scc", "memory");
  return so;
}
template <int OFF> DI void dma_off(u32 voff, srd_t srd, u32 soff) {
  asm volatile("buffer_load_dwordx4 %0, %1, %2 offen offset:%3 lds" ::"v"(voff), "s"(srd), "s"(soff), "n"(OFF) : "memory");
}
template <int OFF> DI void dma_off_sc1(u32 voff, srd_t srd, u32 soff) {
  asm volatile("buffer_load_dwordx4 %0, %1, %2 offen offset:%3 sc1 lds" ::"v"(voff), "s"(srd), "s"(soff), "n"(OFF) : "memory");
}
template <int OFF> DI void ldg_sc1o(u32x4& dst, u32 voff, srd_t srd, u32 soff) {
  asm volatile("buffer_load_dwordx4 %0, %1, %2, %3 offen offset:%4 sc1" : "=v"(dst) : "v"(voff), "s"(srd), "s"(soff), "n"(OFF) : "memory");
}
DI void ldg(u32x4& dst, u32 voff, srd_t srd, u32 soff) {
  asm volatile("buffer_load_dwordx4 %0, %1, %2, %3 offen" : "=v"(dst) : "v"(voff), "s"(srd), "s"(soff) : "memory");
}
template <int BIT, int OFF> DI void ldg2_if(u32x4& a, u32x4& b, u32 mask, u32 voff, srd_t srd, u32 soff) {
  asm volatile("s_bitcmp1_b32 %2, %3\n\ts_cbranch_scc0 .Lsk%=\n\tbuffer_load_dwordx4 %0, %4, %5, %6 offen offset:%7 sc1\n\tbuffer_load_dwordx4 %1, %4, %5, %6 offen offset:%8 sc1\n.Lsk%=:"
               : "+v"(a), "+v"(b) : "s"(mask), "n"(BIT), "v"(voff), "s"(srd), "s"(soff), "n"(OFF), "n"(OFF + 1024) : "scc", "memory");
}
template <int N> DI void landed4(u32x4& a, u32x4& b, u32x4& c, u32x4& d) {
  asm volatile("s_waitcnt vmcnt(%4)" : "+v"(a), "+v"(b), "+v"(c), "+v"(d) : "n"(N) : "memory");
}
template <int N> DI void wait3(u32x4& a, u32x4& b, u32x4& c, u64& f) {
  asm volatile("s_waitcnt vmcnt(%4)" : "+v"(a), "+v"(b), "+v"(c), "+v"(f) : "n"(N) : "memory");
}
DI void pin2(u32x4& a, u32x4& b) { asm volatile("" : "+v"(a), "+v"(b)::"memory"); }
DI void pin1(u32& a) { asm volatile("" : "+v"(a)); }
DI void touch(u32& sink, u32 voff, srd_t srd) {
  asm volatile("buffer_load_dword %0, %1, %2, 0 offen" : "+v"(sink) : "v"(voff), "s"(srd) : "memory");
}
DI void ldg_sc1(u32x4& dst, u32 voff, srd_t srd, u32 soff) {
  asm volatile("buffer_load_dwordx4 %0, %1, %2, %3 offen sc1" : "=v"(dst) : "v"(voff), "s"(srd), "s"(soff) : "memory");
}
DI void stg_sc1(u32x4 v, u32 voff, srd_t srd, u32 soff) {
  asm volatile("buffer_store_dwordx4 %0, %1, %2, %3 offen sc1\n\ts_nop 1" ::"v"(v), "v"(voff), "s"(srd), "s"(soff) : "memory");
}
struct Sync { u32 pa, ra, one; };
template <int C> DI void lds_post(const Sync& y) { asm volatile("ds_add_u32 %0, %1 offset:%2" ::"v"(y.pa), "v"(y.one), "n"(C) : "memory"); }
template <int C> DI u32 lds_peek(const Sync& y) { return *(volatile LAS u32*)(size_t)(y.ra + C); }
template <int C, int T> DI void lds_wait_pre(u32 a, u32 v) {
  u32 cnt;
  asm volatile("v_cmp_gt_u32 vcc, %3, %0\n\ts_cbranch_vccnz .Lsl%=\n.Lse%=:\n\t.subsection 1\n"
               ".Lsl%=:\n\ts_mov_b32 %1, 0x800000\n.Lsp%=:\n\tds_read_b32 %0, %2 offset:%4\n\ts_waitcnt lgkmcnt(0)\n\tv_cmp_gt_u32 vcc, %3, %0\n\ts_cbranch_vccz .Lse%=\n\t"
               "s_sub_u32 %1, %1, 1\n\ts_cbranch_scc0 .Lsp%=\n\ts_branch .Lse%=\n\t.subsection 0"
               : "+v"(v), "=&s"(cnt) : "v"(a), "n"(T), "n"(C) : "vcc", "scc", "memory");
}
template <int C, int T> DI void lds_wait(const Sync& y) { lds_wait_pre<C, T>(y.ra, lds_peek<C>(y)); }
template <int K> DI void z_post(u32 oob, srd_t srd) {
  asm volatile("buffer_load_dword %0, %1, 0 offen offset:%2 lds" ::"v"(oob), "s"(srd), "n"(4 * K + 4) : "memory");
}
DI void set_m0(u32 v) { asm volatile("s_mov_b32 m0, %0\n\ts_nop 0" ::"s"(v) : "memory"); }
template <int K> DI u32 z_peek(u32 za) { return *(volatile LAS u32*)(size_t)(za + 4 * K); }
template <int K> DI void z_wait_pre(u32 za, u32 v) {
  u32 cnt;
  asm volatile("v_cmp_ne_u32 vcc, 0, %0\n\ts_cbranch_vccnz .Lsl%=\n.Lse%=:\n\t.subsection 1\n"
               ".Lsl%=:\n\ts_mov_b32 %1, 0x800000\n.Lsp%=:\n\tds_read_b32 %0, %2 offset:%3\n\ts_waitcnt lgkmcnt(0)\n\tv_cmp_ne_u32 vcc, 0, %0\n\ts_cbranch_vccz .Lse%=\n\t"
               "s_sub_u32 %1, %1, 1\n\ts_cbranch_scc0 .Lsp%=\n\ts_branch .Lse%=\n\t.subsection 0"
               : "+v"(v), "=&s"(cnt) : "v"(za), "n"(4 * K) : "vcc", "scc", "memory");
}
template <int K> DI void z_wait(u32 za) { z_wait_pre<K>(za, z_peek<K>(za)); }
DI void flag_peek(u64& v, srd_t ws, u32 voff) {
  asm volatile("buffer_load_dwordx2 %0, %1, %2, 0 offen sc1" : "=v"(v) : "v"(voff), "s"(ws) : "memory");
}
template <int N> DI void flag_check(u64 v, srd_t ws, u32 voff, u64 tag) {
  u32 cnt;
  asm volatile("s_waitcnt vmcnt(%5)\n\t"
               "v_cmp_ne_u64 vcc, %4, %0\n\ts_cbranch_vccnz .Lfl%=\n.Lfe%=:\n\t.subsection 1\n"
               ".Lfl%=:\n\ts_mov_b32 %1, 0x40000\n"
               ".Lfp%=:\n\tbuffer_load_dwordx2 %0, %2, %3, 0 offen sc1\n\ts_waitcnt vmcnt(0)\n\tv_cmp_ne_u64 vcc, %4, %0\n\ts_cbranch_vccz .Lfe%=\n\t"
               "s_sleep 1\n\ts_sub_u32 %1, %1, 1\n\ts_cbranch_scc0 .Lfp%=\n\ts_branch .Lfe%=\n.Lfz%=:\n\t.subsection 0"
               : "+v"(v), "=&s"(cnt) : "v"(voff), "s"(ws), "s"(tag), "n"(N) : "vcc", "scc", "memory");
}
DI void flag_wait(srd_t ws, u32 voff, u64 tag) { u64 v; flag_peek(v, ws, voff); flag_check<0>(v, ws, voff, tag); }
DI void flag_set(srd_t ws, u32 voff, u64 tag) {
  asm volatile("buffer_store_dwordx2 %0, %1, %2, 0 offen sc1" ::"v"(tag), "v"(voff), "s"(ws) : "memory");
}
DI u32x4 not_ones(u32x4 v) { return u32x4{min(v.x, 0xfffffffeu), min(v.y, 0xfffffffeu), min(v.z, 0xfffffffeu), min(v.w, 0xfffffffeu)}; }
template <int N> DI bool arrived(const u32x4 (&v)[N]) {
  u32 m = 0;
#pragma unroll
  for (int i = 0; i < N; ++i) m = max(max(max(m, v[i].x), v[i].y), max(v[i].z, v[i].w));
  return __builtin_amdgcn_ballot_w64(m == 0xffffffffu) == 0;
}
template <bool FORCE, int NZ> DI void flag_try(u32& done, srd_t ws, u32 voff, u64 tag) {
  u32 t;
#define FT_TAIL "s_cbranch_scc0 .Lfp%=\n.Lfe%=:\n\t.subsection 1\n.Lfp%=:\n\ts_waitcnt vmcnt(%5)\n\tbuffer_store_dwordx2 %2, %3, %4, 0 offen sc1\n\t" \
                "s_mov_b32 %0, 64\n\ts_branch .Lfe%=\n.Lfz%=:\n\t.subsection 0"
  if (FORCE) asm volatile("s_cmp_lg_u32 %0, 0\n\t" FT_TAIL : "+s"(done), "=&s"(t) : "v"(tag), "v"(voff), "s"(ws), "n"(NZ) : "scc", "memory");
  else asm volatile("s_getreg_b32 %1, hwreg(HW_REG_IB_STS, 0, 4)\n\ts_or_b32 %1, %1, %0\n\ts_cmp_gt_u32 %1, %5\n\t" FT_TAIL
                    : "+s"(done), "=&s"(t) : "v"(tag), "v"(voff), "s"(ws), "n"(NZ) : "scc", "memory");
}
#define SB() __builtin_amdgcn_sched_barrier(0)
#define OPAQUE(x) asm volatile("" : "+v"(x))
DI float swap_hi(float lo_wants, int h) {
  auto r = __builtin_amdgcn_permlane32_swap(f2u(lo_wants), f2u(lo_wants), false, false);
  (void)h;
  return u2f(r[1]);
}
DI void both_halves(float v, float& lo, float& hi) {
  auto r = __builtin_amdgcn_permlane32_swap(f2u(v), f2u(v), false, false);
  lo = u2f(r[0]); hi = u2f(r[1]);
}
constexpr int SPINS = 200000;
constexpr float LOG2E = 1.4426950408889634f;

template <int I> struct IC { static constexpr int v = I; };
template <int N, class F, int I = 0> DI void sfor(F f) { if constexpr (I < N) { f(IC<I>{}); sfor<N, F, I + 1>(f); } }

enum { OX, OW, OU, OK, OZ, OPEEK, OB, OA, OST };
struct Op { int kind, idx, i; };
enum { M_KV0, M_EPI, M_ST, M_JOB };
struct Tab { Op v[300]; int mark[48]; int n; };
constexpr int AW = 4;
constexpr int PRED = 8;
constexpr int LW = 6;
constexpr int CV0 = 10;
constexpr int PD = 5;
constexpr int zidx(bool H, int kind, int idx) { return kind == OW ? idx : kind == OK ? (H ? 7 : 14) + idx : (H ? 11 : 7) + idx; }
constexpr int rpos(bool H, int kind, int idx) { return kind == OX ? 2 * idx : kind == OW ? 2 * idx + 1 : kind == OK ? (H ? 14 : 21) + idx : (H ? 17 : 14) + idx; }
constexpr u32 slot_of(bool H, int kind, int idx) { return kind == OK && idx == 3 ? 9 : rpos(H, kind, idx) % 9; }
constexpr Tab make_tab(bool H) {
  Tab t{}; int n = 0;
  auto I4 = [&](int kind, int idx, int lo = 0, int hi = 4) {
    for (int i = lo; i < hi; ++i) t.v[n++] = Op{kind, idx, i};
    if (kind != OX && kind <= OK && hi == (kind == OK && idx == 3 ? 2 : 4)) t.v[n++] = Op{OZ, zidx(H, kind, idx), 0};
  };
  for (int s = 0; s < 7; ++s) { if (s == 6 && H) t.v[n++] = Op{OPEEK, 0, 0}; I4(OX, s); I4(OW, s); }
  if (!H) for (int s = 0; s < 7; ++s) { if (s == 5) t.v[n++] = Op{OPEEK, 0, 0}; I4(OU, s); }
  I4(OK, 0); I4(OK, 1); I4(OK, 2); I4(OK, 3, 0, 2);
  t.mark[M_KV0] = n;
  if (H) for (int s = 0; s < 7; ++s) I4(OU, s);
  else t.v[n++] = Op{OZ, 18, 0};
  t.mark[M_EPI] = n;
  if (H) for (int j = 0; j < 14; ++j) { if (j == 11) t.v[n++] = Op{OPEEK, 2, 0}; I4(OB, j, 0, 2); }
  t.mark[M_ST] = n; t.v[n++] = Op{OST, 0, 0};
  if (H) {
    const int ord[17] = {31, 32, 33, 34, 35, 19, 20, 21, 22, 23, 24, 25, 26, 28, 29, 30, 27};
    int nd = 0, na = AW;
    for (int j = 0; j < AW; ++j) I4(OA, j, 0, 2);
    for (; nd < PRED; ++nd) I4(OB, ord[nd], 0, 2);
    for (int j = 0; j < 28; ++j) {
      t.mark[M_JOB + j] = n;
      if (j == 10) t.v[n++] = Op{OPEEK, 3, 0};
      for (int k = 0; k < 2; ++k) {
        if (na < 14) { I4(OA, na, 0, 2); ++na; }
        else if (nd < 17) { I4(OB, ord[nd], 0, 2); if (++nd == 16) t.v[n++] = Op{OZ, 18, 0}; }
        else if (nd < 22) { I4(OB, nd - 3, 0, 2); ++nd; }
      }
      if (j == 12) for (int k = 14; k < 14 + LW; ++k) I4(OA, k, 0, 2);
      if (j >= 14 && j + LW < 28) I4(OA, j + LW, 0, 2);
    }
    t.mark[M_JOB + 28] = n;
  }
  t.n = n;
  return t;
}
template <bool H> struct TB {
  static constexpr Tab t = make_tab(H);
  static constexpr int find(int kind, int idx, int i) { for (int k = 0; k < t.n; ++k) if (t.v[k].kind == kind && t.v[k].idx == idx && t.v[k].i == i) return k; return 1000; }
  static constexpr int etick(int n) {
    int k = t.mark[M_EPI];
    for (int i = 0; i < n && k < t.mark[M_ST]; ++i) { ++k; while (k < t.mark[M_ST] && (t.v[k].kind == OZ || t.v[k - 1].kind == OPEEK)) ++k; }
    return n >= 29 ? t.mark[M_ST] : k;
  }
  static constexpr int need0(int k) {
    if (k >= t.n || t.v[k].kind > OK || t.v[k].i != 0 || slot_of(H, t.v[k].kind, t.v[k].idx) == 9) return -1;
    const int p = rpos(H, t.v[k].kind, t.v[k].idx);
    return p < 9 || (p > 22 && H) ? -1 : (p - 9) >> 1;
  }
  static constexpr int need(int k) { for (int i = 0; i < k && i < t.n; ++i) if (need0(i) >= need0(k)) return -1; return need0(k); }
};
constexpr u32 L_R2 = 0, L_R1 = 9 * 8192;
constexpr int LAG = 12;
constexpr int cmax(int a, int b) { return a > b ? a : b; }

DI void mfma16i(f32x4& c, u32x4 a, u32x4 b) { asm volatile("v_mfma_f32_16x16x32_bf16 %0, %1, %2, %0" : "+v"(c) : "v"(a), "v"(b)); }
DI void mfma16z(f32x4& c, u32x4 a, u32x4 b) { asm volatile("v_mfma_f32_16x16x32_bf16 %0, %1, %2, 0" : "=&v"(c) : "v"(a), "v"(b)); }
template <bool HG, bool FIRST, class Hook>
DI void unit_gemm(u32x4 (&xf)[28], f32x4 (&acc)[8][2], int lane, int w, u32 za, Hook hook) {
  const u32 kg = lane >> 4, q = (lane & 15) >> 2, pp = lane & 3, t16 = lane & 15;
  const u32 vb0 = (8 * kg + q) * 128 + ((q >> 1) | ((kg & 1) << 1)) * 32 + (((pp & 1) << 1) | (pp >> 1)) * 8;
  constexpr int KIND = FIRST ? OW : OU;
  const u32 xb = w * 4096 + t16 * 128 + ((kg ^ (t16 >> 1)) * 16);
  u32 vb[2][4], xa[2][2], pre = 0;
#pragma unroll
  for (int c = 0; c < 8; ++c) { vb[c >> 2][c & 3] = (vb0 ^ ((c & 3) * 32)) + (c < 4 && !FIRST ? 131072 : (c >> 2) * 65536); OPAQUE(vb[c >> 2][c & 3]); }
  if (FIRST) {
#pragma unroll
    for (int c = 0; c < 4; ++c) { xa[c >> 1][c & 1] = (xb ^ ((c & 1) * 64)) + (c >> 1) * 81920; OPAQUE(xa[c >> 1][c & 1]); }
  }
  u32x2 fl[16], fh[16];
  auto wadr = [&](int s, int n) -> u32 { const int slot = slot_of(HG, KIND, s); return vb[slot >> 2 & 1][n & 3] + (slot & 3) * 16384 + ((n >> 2) & 1) * 8192 + (n >> 3) * 4096; };
  auto rdx = [&](int s, int i) { const int slot = slot_of(HG, OX, s); xf[4 * s + i] = lds128((slot < 5 ? xa[0][i >> 1] + slot * 16384 : xa[1][i >> 1] + (slot - 5) * 16384) + (i & 1) * 2048); };
  z_wait<zidx(HG, KIND, 0)>(za);
  if (FIRST) { rdx(0, 0); rdx(0, 1); rdx(0, 2); }
#pragma unroll
  for (int n = 0; n < PD; ++n) { fl[n] = tr64(wadr(0, n)); fh[n] = tr64(wadr(0, n) + 512); }
  SB();
  sfor<112>([&](auto gc) {
    constexpr int G = decltype(gc)::v, s = G >> 4, n = G & 15, f = n & 7, n4 = (n + PD) & 15, s4 = n < 16 - PD ? s : s + 1;
    constexpr bool all = HG || FIRST;
    constexpr bool live = all || f < 6, next = s4 < 7 && (all || (n4 & 7) < 6);
    if constexpr (all && (n & 1) == 0 && G < 112 - PD) { __builtin_amdgcn_s_waitcnt(0xC07F | ((2 * PD - 4) << 8)); SB(); }
    if constexpr (live) {
      if constexpr (G < 8) mfma16z(acc[f][0], u32x4{fl[n].x, fl[n].y, fh[n].x, fh[n].y}, xf[4 * s + (n >> 3) * 2 + 0]);
      else mfma16i(acc[f][0], u32x4{fl[n].x, fl[n].y, fh[n].x, fh[n].y}, xf[4 * s + (n >> 3) * 2 + 0]);
      SB();
    }
    if constexpr (n == 16 - PD && s < 6) z_wait_pre<zidx(HG, KIND, s + 1)>(za, pre);
    if constexpr (!FIRST && slot_of(HG, KIND, s) == 8 && n >= 16 - PD && n4 < 4) { vb[0][n4] -= 131072; OPAQUE(vb[0][n4]); }
    if constexpr (next) fl[n4] = tr64(wadr(s4, n4));
    SB();
    if constexpr (live) {
      if constexpr (G < 8) mfma16z(acc[f][1], u32x4{fl[n].x, fl[n].y, fh[n].x, fh[n].y}, xf[4 * s + (n >> 3) * 2 + 1]);
      else mfma16i(acc[f][1], u32x4{fl[n].x, fl[n].y, fh[n].x, fh[n].y}, xf[4 * s + (n >> 3) * 2 + 1]);
      SB();
    }
    if constexpr (next) fh[n4] = tr64(wadr(s4, n4) + 512);
    if constexpr (FIRST) {
      if constexpr (n == 0) rdx(s, 3);
      if constexpr (n >= 16 - PD && n4 < 3 && s < 6) rdx(s + 1, n4);
    }
    if constexpr (n == 12 - PD && s < 6) pre = z_peek<zidx(HG, KIND, s + 1)>(za);
    hook(gc);
    SB();
  });
  asm volatile("s_nop 7" : "+v"(acc[0][0]), "+v"(acc[0][1]), "+v"(acc[1][0]), "+v"(acc[1][1]), "+v"(acc[2][0]), "+v"(acc[2][1]), "+v"(acc[3][0]), "+v"(acc[3][1]),
               "+v"(acc[4][0]), "+v"(acc[4][1]), "+v"(acc[5][0]), "+v"(acc[5][1]));
  if (HG || FIRST) asm volatile("" : "+v"(acc[6][0]), "+v"(acc[6][1]), "+v"(acc[7][0]), "+v"(acc[7][1]));
  SB();
}
DI u32x4 frag16(f32x4 a, f32x4 b) {
  const auto x = __builtin_amdgcn_permlane16_swap(cvtpk(a.x, a.y), cvtpk(b.x, b.y), false, false);
  const auto y = __builtin_amdgcn_permlane16_swap(cvtpk(a.z, a.w), cvtpk(b.z, b.w), false, false);
  return u32x4{x[0], y[0], x[1], y[1]};
}

DI void load_wphi(u32x4 (&wp)[2], u32 base, int lane) {
  const u32 h = lane >> 5, fb = (lane >> 4) & 1, q = (lane & 15) >> 2, pp = lane & 3;
  u32 col = 16 * fb + 4 * pp; if (col == 28) col = 24;
  const u32 a = base + (4 * h + q) * 56 + col * 2;
  wp[0] = trfrag(a, a + 8 * 56);
  u32x2 lo = tr64(a + 16 * 56), hi = tr64(base + (24 + q) * 56 + col * 2);
  wp[1] = u32x4{lo.x, lo.y, h ? 0u : hi.x, h ? 0u : hi.y};
}
DI void load_wphi3(u32x4 (&wp)[3], u32 base, int o, int lane) {
  const u32 h = lane >> 5, fb = (lane >> 4) & 1, q = (lane & 15) >> 2, pp = lane & 3;
  u32 col = 16 * fb + 4 * pp; if (col == 28) col = 24;
#pragma unroll
  for (int i = 0; i < 6; ++i) {
    const u32 d0 = 8 * i + 4 * h - o;
    const bool ok = d0 <= 24;
    const u32x2 v = tr64(base + ((ok ? d0 : 0u) + q) * 56 + col * 2);
    wp[i >> 1][(i & 1) * 2] = ok ? v.x : 0u; wp[i >> 1][(i & 1) * 2 + 1] = ok ? v.y : 0u;
  }
}
DI u32x4 pack8(const f32x16& v, int s) {
  return u32x4{cvtpk(v[8 * s], v[8 * s + 1]), cvtpk(v[8 * s + 2], v[8 * s + 3]), cvtpk(v[8 * s + 4], v[8 * s + 5]), cvtpk(v[8 * s + 6], v[8 * s + 7])};
}
DI float sigmoid(float t) { return __builtin_amdgcn_rcpf(1.f + __builtin_amdgcn_exp2f(-LOG2E * t)); }
DI float vmax(float a, float b) { float r; asm("v_max_f32 %0, %1, %2" : "=v"(r) : "v"(a), "v"(b)); return r; }
DI float vmin(float a, float b) { float r; asm("v_min_f32 %0, %1, %2" : "=v"(r) : "v"(a), "v"(b)); return r; }
DI float max3(float a, float b, float c) { return __builtin_fmaxf(__builtin_fmaxf(a, b), c); }
DI float min3(float a, float b, float c) { return __builtin_fminf(__builtin_fminf(a, b), c); }
template <class Hook> DI void feature_exp(const u32x4 (&t)[3], const u32x4 (&wp)[3], f32x16& ep, f32x16& en, Hook hook) {
  f32x16 pz;
#pragma unroll
  for (int e = 0; e < 16; ++e) pz[e] = 0.f;
  pz = mfma(wp[0], t[0], pz);
  pz = mfma(wp[1], t[1], pz);
  pz = mfma(wp[2], t[2], pz);
  float mx = max3(max3(max3(pz[0], pz[1], pz[2]), max3(pz[3], pz[4], pz[5]), max3(pz[6], pz[7], pz[8])),
                  max3(max3(pz[9], pz[10], pz[11]), max3(pz[12], pz[13], pz[14]), pz[15]), pz[15]);
  float mn = min3(min3(min3(pz[0], pz[1], pz[2]), min3(pz[3], pz[4], pz[5]), min3(pz[6], pz[7], pz[8])),
                  min3(min3(pz[9], pz[10], pz[11]), min3(pz[12], pz[13], pz[14]), pz[15]), pz[15]);
  float a, b;
  both_halves(mx, a, b); mx = __builtin_fmaxf(a, b) * LOG2E;
  both_halves(mn, a, b); mn = __builtin_fminf(a, b) * LOG2E;
  asm volatile("" : "+v"(mx), "+v"(mn));
  hook();
#pragma unroll
  for (int e = 0; e < 16; ++e) ep[e] = __builtin_amdgcn_exp2f(__builtin_fmaf(pz[e], LOG2E, -mx));
  asm volatile("" : "+v"(ep));
  hook();
#pragma unroll
  for (int e = 0; e < 16; ++e) en[e] = __builtin_amdgcn_exp2f(__builtin_fmaf(pz[e], -LOG2E, mn));
  asm volatile("" : "+v"(en));
  hook();
}
DI void reduce_kv(int hd, int pc, srd_t ws, int lane) {
  const u32 h = lane >> 5, r = lane & 31, l16 = lane * 16;
  u32x4 pv[8];
  for (int it = 0; it < SPINS; ++it) {
#pragma unroll
    for (int i = 0; i < 8; ++i) ldg_sc1(pv[i], h * 4096 + r * 16, ws, WS_KVP + (hd * 16 + 2 * i) * 4096 + pc * 512);
    asm volatile("s_waitcnt vmcnt(0)" : "+v"(pv[0]), "+v"(pv[1]), "+v"(pv[2]), "+v"(pv[3]), "+v"(pv[4]), "+v"(pv[5]), "+v"(pv[6]), "+v"(pv[7])::"memory");
    if (arrived(pv)) break;
  }
  u32x4 o;
#pragma unroll
  for (int e = 0; e < 4; ++e) {
    float lo[8], hi[8];
#pragma unroll
    for (int i = 0; i < 8; ++i) { lo[i] = u2f(pv[i][e] << 16); hi[i] = u2f(pv[i][e] & 0xffff0000u); }
    float a, c, d, f;
    both_halves(((lo[0] + lo[1]) + (lo[2] + lo[3])) + ((lo[4] + lo[5]) + (lo[6] + lo[7])), a, c);
    both_halves(((hi[0] + hi[1]) + (hi[2] + hi[3])) + ((hi[4] + hi[5]) + (hi[6] + hi[7])), d, f);
    o[e] = cvtpk(a + c, d + f);
    if (r == 29) o[e] = 0x3f803f80u;
    if (e >= 2 && (pc & 3) == 3) o[e] = 0u;
  }
  stg_sc1(not_ones(o), h ? OOB : l16, ws, WS_KVF + hd * 4096 + pc * 512);
}
template <bool H, class Epi, class Mid>
DI void task_o(int head, int rt32, int w, srd_t ws, u64 tag, int lane, const Sync& y, u64 fq, u32 fqa, u32 fdone, u32x4 own0, u32x4 own1, Epi epi, Mid mid) {
  const int h = lane >> 5;
  const u32 l16 = lane * 16;
  const int ro = (head * 28) & 15;
  u32x4 qf[3], wp[3], kf[4], k1[1];
  auto ldq = [&](u32 vo) {
    const u32 so = WS_Q + (rt32 * 28 + ((head * 7) >> 2)) * 1024;
    ldg_sc1o<0>(qf[0], vo, ws, so); ldg_sc1o<1024>(qf[1], vo, ws, so); ldg_sc1o<2048>(qf[2], ro >= 8 ? vo : OOB, ws, so);
  };
  auto ldk = [&]() { ldg_sc1(k1[0], l16, ws, WS_KVF + head * 4096 + w * 1024); };
  bool early = false, second = false;
  if (H) {
    asm volatile("s_waitcnt vmcnt(0)" : "+v"(fq)::"memory");
    early = __builtin_amdgcn_ballot_w64(fq != tag) == 0;
    ldq(early ? l16 : OOB);
    flag_peek(fq, ws, early ? OOB : fqa);
  }
  epi([&](int c) {
    if (H && c == 2) {
      wait3<3>(qf[0], qf[1], qf[2], fq);
      if (!early && __builtin_amdgcn_ballot_w64(fq != tag) == 0) { early = second = true; ldq(l16); }
    }
  });
  __builtin_amdgcn_s_setprio(H ? 3 : 0);
  u32 done = 0;
  if (H) { if (second) VMWAIT(1); wait3<63>(qf[0], qf[1], qf[2], fq); }
  if (!early) {
    flag_try<true, 0>(done, ws, fdone, tag);
    if (H) {
      flag_wait(ws, fqa, tag);
      ldq(l16);
      asm volatile("s_waitcnt vmcnt(0)" : "+v"(qf[0]), "+v"(qf[1]), "+v"(qf[2])::"memory");
    } else {
      qf[0] = own0; qf[1] = own1; qf[2] = u32x4{0u, 0u, 0u, 0u};
      if ((rt32 & 28) != 28) mid();
    }
  }
  ldk();
  load_wphi3(wp, L_WPHI, ro, lane);
  f32x16 ep, en; feature_exp(qf, wp, ep, en, [&]() { flag_try<false, 1>(done, ws, fdone, tag); });
  u32x4 ef[2][2] = {{pack8(ep, 0), pack8(ep, 1)}, {pack8(en, 0), pack8(en, 1)}};
  asm volatile("" : "+v"(ef[0][0]), "+v"(ef[0][1]), "+v"(ef[1][0]), "+v"(ef[1][1]));
  __builtin_amdgcn_s_setprio(3);
  if (early) { flag_try<true, 1>(done, ws, fdone, tag); VMWAIT(1); } else VMWAIT(0);
  asm volatile("" : "+v"(k1[0])::"memory");
  if (!H && (rt32 & 28) == 28) mid();
  lds_wait<C_SYNC, 1>(y);
  float gate = sigmoid(*(LAS float*)(size_t)(L_GW + 256 + (w * 32 + (lane & 31)) * 4));
  OPAQUE(gate);
  for (int it = 0; it < SPINS && !arrived(k1); ++it) {
    __builtin_amdgcn_s_sleep(2);
    ldk();
    asm volatile("s_waitcnt vmcnt(0)" : "+v"(k1[0])::"memory");
  }
  lds_wait<C_PROG, 1>(y);
  sts128(L_KVF + w * 1024 + l16, k1[0]);
  lds_post<C_KVF>(y);
  lds_wait<C_KVF, 1>(y);
#pragma unroll
  for (int i = 0; i < 4; ++i) kf[i] = lds128(L_KVF + i * 1024 + l16);
  f32x16 np, nn;
#pragma unroll
  for (int e = 0; e < 16; ++e) { np[e] = 0.f; nn[e] = 0.f; }
  np = mfma(kf[0], ef[0][0], np); nn = mfma(kf[2], ef[1][0], nn);
  np = mfma(kf[1], ef[0][1], np); nn = mfma(kf[3], ef[1][1], nn);
  const float rp = __builtin_amdgcn_rcpf(np[13]), rn = __builtin_amdgcn_rcpf(nn[13]);
  const float inv = __builtin_amdgcn_rcpf(np[12] * rp + nn[12] * rn + 1e-6f) * gate;
  const float cp = swap_hi(rp * inv, h), cn = swap_hi(rn * inv, h);
  f32x16 o;
#pragma unroll
  for (int e = 0; e < 16; ++e) o[e] = np[e] * cp + nn[e] * cn;
  u32x4 o0 = pack8(o, 0), o1 = pack8(o, 1);
  const float omg = 1.f - gate, oh = u2f(cvtpk(omg, 0.f) << 16);
  o1.z = h ? cvtpk(oh, omg - oh) : o1.z; o1.w = h ? 0u : o1.w;
  stg_sc1(not_ones(o0), l16, ws, WS_O + (rt32 * 32 + head * 2) * 1024);
  stg_sc1(not_ones(o1), l16, ws, WS_O + (rt32 * 32 + head * 2 + 1) * 1024);
}
DI void dump_tile(const f32x16 (&acc)[2][2], u32 base, int lane) {
  const u32 r = lane & 31, a = base + (r >> 3) * 8192 + (r & 7) * 256 + (((lane >> 5) ^ (r & 7)) * 16);
#pragma unroll
  for (int g4 = 0; g4 < 4; ++g4) {
    u32 ag = a ^ (g4 * 32); OPAQUE(ag);
#pragma unroll
    for (int c = 0; c < 2; ++c)
#pragma unroll
      for (int t = 0; t < 2; ++t)
        sts128(ag + t * 32768 + c * 128, __builtin_bit_cast(u32x4, f32x4{acc[c][t][4 * g4], acc[c][t][4 * g4 + 1], acc[c][t][4 * g4 + 2], acc[c][t][4 * g4 + 3]}));
  }
  asm volatile("s_waitcnt lgkmcnt(0)\n\ts_barrier" ::: "memory");
}
DI u32 brow_off(int lane) { const u32 row = lane >> 3; return row * 896 + ((lane & 7) ^ (((row >> 1) & 1) * 4)) * 16; }
struct BPath { u32 wa, ra[2]; };
DI BPath make_bpath(u32 base, int lane) {
  const u32 h = lane >> 5, cb = (lane >> 4) & 1, q = (lane & 15) >> 2, pp = lane & 3;
  BPath t;
  t.wa = base + lane * 16; OPAQUE(t.wa);
#pragma unroll
  for (int c = 0; c < 2; ++c) { t.ra[c] = base + (4 * h + q) * 128 + ((c ^ ((q >> 1) & 1)) * 64) + cb * 32 + pp * 8; OPAQUE(t.ra[c]); }
  return t;
}

template <class Hook> DI void epi_h(const f32x4 (&acc)[8][2], srd_t ws, u32 soff, int lane, Hook hook) {
#pragma unroll
  for (int c = 0; c < 4; ++c) {
    float hv[8];
#pragma unroll
    for (int e = 0; e < 8; ++e) {
      const float x = acc[c][e >> 2][e & 3];
      const float t = __builtin_amdgcn_exp2f(x * __builtin_fmaf(x * x, -0.10294323970f, -2.30220819814f));
      hv[e] = x * __builtin_amdgcn_rcpf(1.f + t) * acc[4 + c][e >> 2][e & 3];
    }
    const u32x4 hp = frag16(f32x4{hv[0], hv[1], hv[2], hv[3]}, f32x4{hv[4], hv[5], hv[6], hv[7]});
    stg_sc1(hp, lane * 16, ws, soff + c * 1024);
    hook(c);
  }
}

template <bool P1, bool isH> DI void body(const Params& p, const int wave) {
  const int tid = threadIdx.x, lane = tid & 63;
  const int w = wave & 3;
  const int b = blockIdx.x, xcd = b & 7, bi = b >> 3;
  const int m = 8 * (xcd >> 2) + (bi & 7), sl = (xcd & 1) * 4 + (bi >> 3), half = (xcd >> 1) & 1;
  const int tl = half * 56 + (xcd & 1) * 32 + bi, rt64 = 16 * (xcd >> 2) + tl / 7, ct2 = tl % 7;
  const int j = 7 * half + sl;
  const int kvhead = 8 * half + sl;
  const int h = lane >> 5;
  const u32 r = lane & 31, l4 = (lane & 3) * 4, l16 = lane * 16;
  const u32 cb = (lane >> 4) & 1, q = (lane & 15) >> 2, pp = lane & 3;
  const srd_t ws = mksrd(p.ws, WS_END), sqkv = mksrd(p.wqkv, D * 3 * D * 2);
  const int rt32 = m * 4 + w;

  if (P1) __builtin_amdgcn_s_setprio(2);
  const u32 zm = L_Z + wave * 512;
  u32 z1 = L_Z + (lane & 3) * 512 + 256, z2 = z1 + 2048, oob = OOB;
  OPAQUE(z1); OPAQUE(z2); OPAQUE(oob);
  Sync y{lane == 0 ? L_CNT + w * 4 : L_DUM + lane * 4, L_CNT + l4, 1u};
  OPAQUE(y.pa); OPAQUE(y.ra); OPAQUE(y.one);
  u32x2 ev;
  asm volatile("buffer_load_dwordx2 %0, off, %1, 0 sc1" : "=v"(ev) : "s"(ws) : "memory");

  if (P1) {
    u32 fvo, sink = 0, tvo2;
    const srd_t st2 = mksrd(w == 0 ? p.wg : w == 1 ? p.wu : w == 2 ? p.wd : p.wo, w < 3 ? D * DF * 2 : D * D * 2);
    {
      const u32 scr = L_K3 + w * 1024;
      const int km = rt32;
      dma_buf(scr, km < 56 ? (lane >> 2) * 2688 + (lane & 3) * 16 : OOB, sqkv, (km >> 1) * 16 * 2688 + (1 + (km & 1)) * 896 + kvhead * 56);
      {
        const srd_t sp = mksrd(p.wphi, 16 * 1568);
        set_m0((w & 1) ? L_K3 + 4096 : L_WPHI);
        dma_off<0>((w & 1) ? OOB : l16, sp, kvhead * 1568);
        dma_off<1024>((w & 1) || lane >= 34 ? OOB : l16, sp, kvhead * 1568);
      }
      u32x4 gwf;
      ldg(gwf, r == 30 ? h * 16 : OOB, mksrd(p.gw, D * 2), (km >> 1) * 32);
      {
        const u32 row = (xcd >> 2) * 224 + bi * 7 + (lane >> 2), c4 = lane & 3;
        const u32 wv = (lane < 28 && !((xcd & 1) && c4 == 3)) ? row * 3584 + (half * 7 + (xcd & 1) * 4 + c4) * 128 : OOB;
        const u32 tvo1 = w < 2 ? wv : w == 2 ? (lane < 56 ? ((xcd * 32 + bi) * 56 + lane) * 128 : OOB)
                               : ((xcd & 1) && lane < 28 ? row * 2688 + (half * 3 + c4) * 128 : OOB);
        touch(sink, tvo1, w < 2 ? st2 : w == 2 ? mksrd(p.x, S * D * 2) : sqkv);
        tvo2 = w < 2 ? wv + 14 * 128 : w == 2 ? (lane < 49 ? (b * 49 + lane) * 128 : OOB) : (lane < 13 ? (b * 13 + lane) * 128 : OOB);
      }
      const u32x4 ones = {~0u, ~0u, ~0u, ~0u};
      stg_sc1(ones, l16, ws, WS_KVP + ((kvhead * 16 + m) * 4 + w) * 1024);
      __builtin_amdgcn_s_setprio(3);
      asm volatile("s_waitcnt vmcnt(2)" : "+v"(ev), "+v"(gwf)::"memory");
      u32x4 kt;
      if (w & 1) {
        const u32 a = scr + (8 * h + q) * 64 + cb * 32 + 8 * pp;
        kt = trfrag(a, a + 256);
        if (r >= 28) kt = gwf;
      } else {
        u32x4 wp[2]; load_wphi(wp, L_WPHI, lane);
        const u32 a = scr + ((lane & 3) + ((lane >> 1) & 4) + ((lane << 1) & 8)) * 64 + h * 8;
        f32x16 t;
#pragma unroll
        for (int e = 0; e < 16; ++e) t[e] = 0.f;
#pragma unroll
        for (int s = 0; s < 2; ++s) {
          const u32x2 lo = *(LAS u32x2*)(size_t)(a + s * 32), hi = *(LAS u32x2*)(size_t)(a + s * 32 + 16);
          t = mfma(u32x4{lo.x, lo.y, hi.x, hi.y}, wp[s], t);
        }
#pragma unroll
        for (int e = 0; e < 8; ++e) t[e] *= LOG2E;
        kt = pack8(t, 0);
      }
      if (m == 15) kt = ones;
      stg_sc1(kt, m != 14 ? l16 : OOB, ws, m == 15 ? WS_KVF + (kvhead * 4 + w) * 1024 : WS_KT + (kvhead * 56 + km) * 1024);
      fvo = lane == 0 ? WS_FK + (kvhead * 64 + km) * 8 : OOB;
    }
    const u64 tag = (((u64)rfl(ev.y) << 32) | rfl(ev.x)) + 1;
    u32x4 xf[28];
    f32x4 acc[8][2];
    u32 done = 0;
    set_m0(zm);
    unit_gemm<isH, true>(xf, acc, lane, w, z2, [&](auto gc) {
      constexpr int s = decltype(gc)::v >> 4, n = decltype(gc)::v & 15;
      if constexpr (n == 15) z_post<s>(oob, ws);
      if constexpr (s <= 2 && (n == 3 || n == 9)) flag_try<s == 2 && n == 9, s>(done, ws, fvo, tag);
      if constexpr (s == 3 && n == 11) touch(sink, tvo2, st2);
    });
    {
      const u32x4 ones = {~0u, ~0u, ~0u, ~0u};
      stg_sc1(ones, l16, ws, WS_O + (rt32 * 32 + kvhead * 2) * 1024);
      stg_sc1(ones, l16, ws, WS_O + (rt32 * 32 + kvhead * 2 + 1) * 1024);
    }
    if (isH) epi_h(acc, ws, WS_H + (rt32 * 112 + j * 4) * 1024, lane, [](int) {});
    u32x4 qa[8];
    if (!isH) {
#pragma unroll
      for (int c = 0; c < 8; ++c) { qa[c] = frag16(acc[c][0], acc[c][1]); OPAQUE(qa[c]); }
    }
    __builtin_amdgcn_s_setprio(isH ? 1 : 3);
    done = 0;
    fvo = lane >= 16 ? OOB : (isH ? WS_FH + (rt32 * 16 + j) * 128 : WS_FQ + (rt32 * 2 + half) * 128) + lane * 8;
    u64 fk = 0;
    const u32 oh = w * 4 + ((lane >> 3) & 3), ort = rt64 * 2 + h;
    const u32 fka = !isH ? WS_FK + (m * 64 + lane) * 8 : (lane & 4) ? WS_FQ + (rt32 * 2 + half) * 128
                  : (oh & 7) == 7 ? WS_FQ + (ort * 2 + (oh >> 3)) * 128 : WS_FH + (ort * 16 + oh - (oh >> 3)) * 128;
    unit_gemm<isH, false>(xf, acc, lane, w, z2, [&](auto gc) {
      constexpr int s = decltype(gc)::v >> 4, n = decltype(gc)::v & 15;
      if constexpr (isH && s <= 3 && (n == 3 || n == 9)) flag_try<s == 3 && n == 9, 0>(done, ws, fvo, tag);
      if constexpr (s == 4 && n == 15) lds_post<C_PROG>(y);
      if constexpr (isH && s == 6 && n == 2) { __builtin_amdgcn_s_setprio(3); flag_peek(fk, ws, fka); __builtin_amdgcn_s_setprio(1); }
      if constexpr (!isH && s == 0 && n == 15) z_post<7>(oob, ws);
      if constexpr (!isH && s == 0 && n == 1) flag_peek(fk, ws, fka);
      if constexpr (!isH && s == 2 && n == 1) flag_check<1>(fk, ws, fka, tag);
    });
    OPAQUE(sink);
    lds_post<C_PROG>(y);
    __builtin_amdgcn_s_setprio(3);
    u32x4 bo[8][2], own[2] = {};
    if (!isH) { const u32x4 b4 = frag16(acc[4][0], acc[4][1]), b5 = frag16(acc[5][0], acc[5][1]); own[0] = half ? qa[6] : b4; own[1] = half ? qa[7] : b5; }
    task_o<isH>(kvhead, rt32, w, ws, tag, lane, y, fk, fka, fvo + (isH ? 64 * 16 * 128 : 0), own[0], own[1], [&](auto hook) {
      if (isH) epi_h(acc, ws, WS_H + (rt32 * 112 + (14 + j) * 4) * 1024, lane, hook);
      else {
#pragma unroll
        for (int c = 0; c < 8; ++c) stg_sc1(qa[c], l16, ws, WS_Q + (rt32 * 28 + half * 20 + c) * 1024);
#pragma unroll
        for (int c = 0; c < 6; ++c) stg_sc1(frag16(acc[c][0], acc[c][1]), l16, ws, WS_Q + (rt32 * 28 + 8 + c + half * (c < 4 ? 8 : 2)) * 1024);
      }
    }, [&]() { if (!isH) reduce_kv(m, half * 4 + w, ws, lane); });
    if (isH) {
      f32x16 oc[2][2];
#pragma unroll
      for (int i = 0; i < 4; ++i)
#pragma unroll
        for (int e = 0; e < 16; ++e) oc[i >> 1][i & 1][e] = 0.f;
      asm volatile("" : "+v"(oc[0][0]), "+v"(oc[0][1]), "+v"(oc[1][0]), "+v"(oc[1][1]));
      u32x4 ao[16];
      const u32 l16o = l16 + 32 * 1024, oso = WS_O + (rt64 * 2 * 32 + w * 8) * 1024;
#pragma unroll
      for (int i = 0; i < 16; ++i) asm volatile("" : "=v"(ao[i]));
      __builtin_amdgcn_s_sleep(28);
      u32 miss = 0xff, go = 0xff, next = 0;
      auto fetch = [&]() {
        sfor<8>([&](auto ic) {
          constexpr int i = decltype(ic)::v, hh = i >> 1, t = i & 1;
          ldg2_if<i, (hh & 1) * 2048>(ao[4 * hh + t], ao[4 * hh + 2 + t], go, t ? l16o : l16, ws, oso + (hh >> 1) * 4096);
        });
      };
      auto consume = [&](auto hc) {
        constexpr int hh = decltype(hc)::v;
        if ((go >> (2 * hh)) & 3) {
#pragma unroll
          for (int t = 0; t < 2; ++t) {
            const u32x4 two[2] = {ao[4 * hh + t], ao[4 * hh + 2 + t]};
            if (arrived(two)) miss &= ~(go & (1u << (2 * hh + t)));
          }
        }
        if (next == hh && !((miss >> (2 * hh)) & 3)) {
#pragma unroll
          for (int u = 0; u < 8; ++u) oc[(u >> 1) & 1][u & 1] = mfma(bo[2 * hh + (u >> 2)][(u >> 1) & 1], ao[4 * hh + (u >> 2) * 2 + (u & 1)], oc[(u >> 1) & 1][u & 1]);
          next = hh + 1;
        }
      };
      fetch();
      z_wait<18>(L_Z + 2048 + w * 512 + 256);
      {
        const BPath bp = make_bpath(L_R1 + w * 2048, lane);
#pragma unroll
        for (int i = 0; i < 16; ++i) bo[i >> 1][i & 1] = trfrag(bp.ra[i & 1] + (i >> 1) * 8192, bp.ra[i & 1] + (i >> 1) * 8192 + 1024);
      }
      sfor<4>([&](auto hc) { constexpr int hh = decltype(hc)::v; landed4<12 - 4 * hh>(ao[4 * hh], ao[4 * hh + 1], ao[4 * hh + 2], ao[4 * hh + 3]); consume(hc); });
      go = __builtin_popcount(miss) <= 4 ? miss : 0;
      const u32 wv = ((lane >> 3) & 1) * 32768 + (lane >> 4) * 2048 + 1024 + (((lane >> 3) + w) & 7) * 128;
      for (int it = 0; it < SPINS && next < 4; ++it) {
        u32 v;
        fetch();
        asm volatile("buffer_load_dword %0, %1, %2, %3 offen sc1" : "=v"(v) : "v"((miss & ~go) ? wv : OOB), "s"(ws), "s"(oso) : "memory");
        asm volatile("s_waitcnt vmcnt(0)" : "+v"(ao[0]), "+v"(ao[1]), "+v"(ao[2]), "+v"(ao[3]), "+v"(ao[4]), "+v"(ao[5]), "+v"(ao[6]), "+v"(ao[7]), "+v"(ao[8]), "+v"(ao[9]),
                     "+v"(ao[10]), "+v"(ao[11]), "+v"(ao[12]), "+v"(ao[13]), "+v"(ao[14]), "+v"(ao[15]), "+v"(v)::"memory");
        const u64 em = __builtin_amdgcn_ballot_w64(v == 0xffffffffu) & 0x0101010101010101ull;
        const u32 empty = (u32)((em * 0x0102040810204080ull) >> 56);
        sfor<4>(consume);
        go = __builtin_popcount(miss) <= 4 ? miss : miss & ~empty;
      }
#pragma unroll
      for (int t = 0; t < 2; ++t) *(LAS float*)(size_t)(h ? L_GW + (t * 32 + r) * 4 : L_DUM + r * 4) = u2f(ao[2 + t].z << 16) + u2f(ao[2 + t].z & 0xffff0000u);
      dump_tile(oc, L_R1 + w * 2048, lane);
      flag_set(ws, (b | w | lane) ? OOB : WS_E, tag);
    }
  } else {
    using T = TB<isH>;
    const srd_t sx = mksrd(p.x, S * D * 2);
    constexpr u32 wrs = isH ? 3584 : 2688;
    u32 xvo[4], wvo[4];
#pragma unroll
    for (int i = 0; i < 4; ++i) {
      xvo[i] = (lane >> 3) * 896 + (((lane & 7) ^ ((4 * i + (lane >> 4)) & 7)) * 16) + i * (8 * 896 - 1024);
      wvo[i] = (lane >> 3) * wrs + ((((lane >> 1) & 3) ^ (((lane >> 4) & 1) | ((i & 1) << 1))) * 32) + (lane & 1) * 16 + i * (8 * wrs - 1024);
    }
    const srd_t sw = isH ? mksrd(w < 2 ? p.wg : p.wu, D * DF * 2) : sqkv;
    const u32 xso = (m * 128 + w * 32) * 896;
    const u32 wso1 = (w & 1) * 32 * wrs + (isH ? j * 128 : half * 640 + (w >> 1) * 128);
    const u32 wso2 = (w & 1) * 32 * wrs + (isH ? (14 + j) * 128 : half ? 512 - (w >> 1) * 64 : 256 + (w >> 1) * 128);
    const u32 mq = w * 4096, mq2 = w * 2048;
    const u32 kso4 = WS_KT + kvhead * 56 * 1024 + mq, kso2 = WS_KT + kvhead * 56 * 1024 + mq2;
    const u32 l16h = l16 + 112 * 1024;
    const u32 hso = WS_H + (rt64 * 2 * 112 + w * 14) * 1024, bso = w * 14 * 16 * 896 + ct2 * 128;
    const srd_t swd = mksrd(p.wd, D * DF * 2);
    const u32 bvo = brow_off(lane);
    auto fadr = [&](int k) -> u32 {
      return k == 0 ? WS_FK + (kvhead * 64 + lane) * 8 : WS_FH + (((k - 2) * 64 + rt64 * 2 + h) * 16 + ((w * 14) >> 2) + ((lane >> 3) & 3)) * 128;
    };
    u32 so = 0, pre = 0, fa = 0;
    u64 fv = 0, tag = 0;
    u32x4 xf[28], af[28][2], bf[19][2], kvs;
    const srd_t swo = mksrd(p.wo, D * D * 2);
    const u32 bvo1 = bvo + 8 * 896 - 1024, bvop = lane < 32 ? bvo1 : OOB;
    const u32 xb = w * 4096 + r * 128 + ((h ^ ((r >> 1) & 7)) * 16);
    constexpr u32 L_PHI = slot_of(isH, OK, 1) * 16384, L_VF = slot_of(isH, OK, 2) * 16384;
    struct { u32 pw[4], pr[2], vw, vr, lr, lw, so; } ea;
    float keep;
    auto op = [&](auto kc) {
      constexpr int K = decltype(kc)::v;
      constexpr Op o = T::t.v[K];
      if constexpr (T::need(K) >= 0) z_wait_pre<T::need(K)>(z1, pre);
      if constexpr (isH && o.kind == OU && o.idx == 6 && o.i == 0) z_wait<zidx(isH, OU, 3)>(z2);
      constexpr int chk = o.i != 0 ? -1 : (o.kind == OK && o.idx == 0) ? 0 : (o.kind == OA && o.idx == 0) ? 2
                        : (o.kind == OA && o.idx == 14) ? 3 : -1;
      if constexpr (chk >= 0) { flag_check<K - 1 - T::find(OPEEK, chk, 0)>(fv, ws, fa, tag); }
      if constexpr (o.kind <= OK) {
        constexpr u32 sb = slot_of(isH, o.kind, o.idx) * 16384;
        if constexpr (o.i == 0) {
          if constexpr (o.kind == OX) so = dma_open<sb, o.idx * 128>(mq, xso);
          else if constexpr (o.kind == OK) so = dma_open<sb, o.idx * 16384>(o.idx < 3 ? mq : mq2, o.idx < 3 ? kso4 : kso2);
          else so = dma_open<sb, o.idx * 64 * wrs>(mq, o.kind == OW ? wso1 : wso2);
          if constexpr (!isH && o.kind == OU && o.idx == 0) {
#pragma unroll
            for (int i = 0; i < 4; ++i) if (w >= 2 && (((lane >> 2) ^ i) & 1)) wvo[i] = OOB;
          }
        }
        if constexpr (o.kind == OK) dma_off_sc1<o.i * 1024>(l16, ws, so);
        else dma_off<o.i * 1024>(o.kind == OX ? xvo[o.i] : wvo[o.i], o.kind == OX ? sx : sw, so);
      } else if constexpr (o.kind == OZ) { set_m0(zm); z_post<o.idx>(oob, ws); }
      else if constexpr (o.kind == OPEEK) { fa = fadr(o.idx); flag_peek(fv, ws, fa); }
      else if constexpr (o.kind == OB) {
        if constexpr (o.idx < 19) ldg(bf[o.idx][o.i], bvo, swd, bso + ((o.idx >= 14 ? o.idx + 42 : o.idx) * 16 + o.i * 8) * 896);
        else {
          if constexpr (o.idx == 31 && o.i == 0) lds_wait<C_SYNC, 3>(y);
          if constexpr (o.idx == 35 && o.i == 0) lds_wait<C_PROG, 1>(y);
          if constexpr (o.idx == 28 && o.i == 0) lds_wait<C_PROG, 2>(y);
          if constexpr (o.i == 0) {
            if constexpr (o.idx < 28) so = dma_open<L_R2 + (o.idx - 19) * 8192, (o.idx + 42) * 16 * 896>(mq2, bso);
            else so = dma_open<L_R1 + (o.idx - 28) * 8192, (((o.idx - 28) >> 1) * 28 + (o.idx & 1) * 16) * 896>(mq2, w * 112 * 896 + ct2 * 128);
          }
          dma_off<o.i * 1024>(o.i == 0 ? bvo : (o.idx >= 28 && (o.idx & 1)) ? bvop : bvo1, o.idx < 28 ? swd : swo, so);
        }
      }
      else if constexpr (o.kind == OA) {
        constexpr int ks = o.idx >= 14 ? o.idx + 42 : o.idx;
        ldg_sc1o<(ks & 3) * 1024>(af[o.idx][o.i], o.i ? l16h : l16, ws, hso + (ks >> 2) * 4096);
      } else if constexpr (o.kind == OST) stg_sc1(not_ones(kvs), ea.so, ws, WS_KVP + ((kvhead * 16 + m) * 4 + w) * 1024);
      if constexpr (T::need(K + 3) >= 0) pre = z_peek<T::need(K + 3)>(z1);
      constexpr int J = K - LAG;
      if constexpr (J >= 0) {
        constexpr Op oj = T::t.v[J];
        if constexpr (oj.kind == OX && oj.i == 3) {
          if constexpr (J == 3) {
            asm volatile("s_waitcnt vmcnt(%1)" : "+v"(ev) : "n"(LAG) : "memory");
            tag = (((u64)rfl(ev.y) << 32) | rfl(ev.x)) + 1;
          } else VMWAIT(LAG);
#pragma unroll
          for (int kk = 0; kk < 4; ++kk) xf[4 * oj.idx + kk] = lds128(slot_of(isH, OX, oj.idx) * 16384 + (xb ^ (kk * 32)));
        }
      }
      SB();
    };
    auto ops = [&](auto ac, auto bc) { sfor<decltype(bc)::v - decltype(ac)::v>([&](auto kc) { op(IC<decltype(ac)::v + decltype(kc)::v>{}); }); };
#define OPS(a, b) ops(IC<(a)>{}, IC<(b)>{})
#define MK(m) (T::t.mark[m])
#define TICK(M, n, N) OPS(MK(M) + (MK((M) + 1) - MK(M)) * (n) / (N), MK(M) + (MK((M) + 1) - MK(M)) * ((n) + 1) / (N))
#define ETICK(n) OPS(T::etick(n), T::etick((n) + 1))
    f32x16 ka, va;
#pragma unroll
    for (int e = 0; e < 16; ++e) { ka[e] = 0.f; va[e] = r == 28 ? 1.f : 0.f; }
    __builtin_amdgcn_s_setprio(2);
    OPS(0, 36);
    {
      const u32 t0 = 4 * cb + q, ra = L_PHI + (w >> 1) * 2048 + (16 * h + t0) * 64;
#pragma unroll
      for (int g4 = 0; g4 < 4; ++g4) { ea.pw[g4] = L_PHI + w * 4096 + r * 64 + (((2 * g4 + h) ^ ((r >> 1) & 7)) * 8); OPAQUE(ea.pw[g4]); }
      ea.pr[0] = ra + (((4 * (w & 1) + pp) ^ (t0 >> 1)) * 8); ea.pr[1] = ra + 512 + (((4 * (w & 1) + pp) ^ ((t0 >> 1) + 4)) * 8);
      ea.vw = L_VF + w * 2048 + l16; ea.vr = L_VF + h * 1024 + ((lane & 15) + 32 * cb) * 16;
      ea.lr = L_VF + w * 2048 + (r >> 4) * 1024 + (30 + 32 * ((r >> 2) & 1)) * 16 + (((r >> 3) & 1) * 4 + (r & 3)) * 2;
      ea.lw = L_GW + 256 + (w * 32 + r) * 4; ea.so = (h * 16 + (lane & 15) + 32 * cb) * 16;
      keep = h ? 0.f : 1.f;
      OPAQUE(ea.pr[0]); OPAQUE(ea.pr[1]); OPAQUE(ea.vw); OPAQUE(ea.vr); OPAQUE(ea.lr); OPAQUE(ea.lw); OPAQUE(ea.so); OPAQUE(keep);
      asm volatile("" : "+v"(va));
    }
    OPS(36, MK(M_KV0));
    {
      u32 ak[4];
#pragma unroll
      for (int t = 0; t < 4; ++t) { ak[t] = slot_of(isH, OK, t) * 16384 + l16; OPAQUE(ak[t]); }
      u32x4 fk[3], fw[3];
      u32 pk = 0;
      auto reads = [&](int ks) { fk[ks % 3] = lds128(ak[ks >> 3] + (ks & 7) * 2048); fw[ks % 3] = lds128(ak[ks >> 3] + (ks & 7) * 2048 + 1024); };
      z_wait<zidx(isH, OK, 0)>(z2);
      reads(0); reads(1); reads(2);
      SB();
      sfor<28>([&](auto kc) {
        constexpr int ks = decltype(kc)::v, t = ks >> 3;
        ka = mfma(fk[ks % 3], xf[ks], ka);
        SB();
        asm volatile("" : "+v"(ka));
        TICK(M_KV0, ks, 28);
        va = mfma(xf[ks], fw[ks % 3], va);
        SB();
        asm volatile("" : "+v"(va));
        if constexpr ((ks & 7) == 3 && t < 3) pk = z_peek<zidx(isH, OK, t + 1)>(z2);
        if constexpr ((ks & 7) == 5) z_wait_pre<zidx(isH, OK, t + 1)>(z2, pk);
        if constexpr (ks + 3 < 28) reads(ks + 3);
        SB();
      });
    }
    {
      ETICK(0);
      float mx = max3(max3(max3(ka[0], ka[1], ka[2]), max3(ka[3], ka[4], ka[5]), max3(ka[6], ka[7], ka[8])),
                      max3(max3(ka[9], ka[10], ka[11]), max3(ka[12], ka[13], ka[14]), ka[15]), ka[15]);
      asm volatile("" : "+v"(mx));
      ETICK(1);
      float mn = min3(min3(min3(ka[0], ka[1], ka[2]), min3(ka[3], ka[4], ka[5]), min3(ka[6], ka[7], ka[8])),
                      min3(min3(ka[9], ka[10], ka[11]), min3(ka[12], ka[13], ka[14]), ka[15]), ka[15]);
      asm volatile("" : "+v"(mn));
      ETICK(2);
      { float a, c; both_halves(mx, a, c); mx = vmax(a, c); both_halves(mn, a, c); mn = vmin(a, c); }
      asm volatile("" : "+v"(mx), "+v"(mn));
      ETICK(3);
      u32x4 vf[2] = {pack8(va, 0), pack8(va, 1)}, vv[4][2];
      asm volatile("" : "+v"(vf[0]), "+v"(vf[1]));
      ETICK(4);
      u32 lg = 0;
      sfor<2>([&](auto sc) {
        constexpr int sg = decltype(sc)::v, E0 = 5 + 10 * sg;
        f32x16 e;
        float ps[4];
        sfor<4>([&](auto gc) {
          constexpr int g4 = decltype(gc)::v;
#pragma unroll
          for (int i = 4 * g4; i < 4 * g4 + 4; ++i) e[i] = __builtin_amdgcn_exp2f(sg == 0 ? ka[i] - mx : mn - ka[i]);
          if constexpr (g4 > 0) { ps[g4 - 1] = (e[4 * g4 - 4] + e[4 * g4 - 3]) + (e[4 * g4 - 2] + e[4 * g4 - 1]); asm volatile("" : "+v"(ps[g4 - 1])); }
          float t = e[4 * g4 + 3]; asm volatile("" : "+v"(t)); e[4 * g4 + 3] = t;
          if constexpr (g4 == 2) pre = sg == 0 ? z_peek<(isH ? zidx(isH, OU, 6) : 18)>(z2) : lds_peek<C_SYNC>(y);
          ETICK(E0 + g4);
        });
        ps[3] = (e[12] + e[13]) + (e[14] + e[15]);
        float a, c; both_halves(__builtin_fmaf(ps[3], keep, ps[2]) + (ps[0] + ps[1]), a, c);
        float rs = __builtin_amdgcn_rcpf(a + c);
        asm volatile("" : "+v"(rs));
        ETICK(E0 + 4);
        if constexpr (sg == 0) {
          z_wait_pre<(isH ? zidx(isH, OU, 6) : 18)>(z2, pre);
          sts128(ea.vw, vf[0]); sts128(ea.vw + 1024, vf[1]);
          lg = *(LAS unsigned short*)(size_t)ea.lr;
        } else {
          lds_wait_pre<C_SYNC, 1>(y.ra, pre);
#pragma unroll
          for (int i = 0; i < 8; ++i) vv[i >> 1][i & 1] = lds128(ea.vr + (i >> 1) * 2048 + (i & 1) * 256);
        }
        SB();
        ETICK(E0 + 5);
        sfor<4>([&](auto gc) {
          constexpr int g4 = decltype(gc)::v;
          sts64(ea.pw[g4] + sg * 2048, u32x2{cvtpk(e[4 * g4] * rs, e[4 * g4 + 1] * rs), cvtpk(e[4 * g4 + 2] * rs, e[4 * g4 + 3] * rs)});
          ETICK(E0 + 6 + g4);
        });
        if constexpr (sg == 0) *(LAS u32*)(size_t)ea.lw = lg << 16;
        lds_post<C_SYNC>(y);
      });
      pre = lds_peek<C_SYNC>(y);
      ETICK(25);
      lds_wait_pre<C_SYNC, 2>(y.ra, pre);
      u32x4 pf[4];
#pragma unroll
      for (int u = 0; u < 4; ++u) pf[u] = trfrag(ea.pr[0] + u * 4096, ea.pr[1] + u * 4096);
      lds_post<C_SYNC>(y);
      SB();
      ETICK(26);
      f32x4 kp[2] = {{0.f, 0.f, 0.f, 0.f}, {0.f, 0.f, 0.f, 0.f}};
#pragma unroll
      for (int i = 0; i < 8; ++i)
        kp[i & 1] = __builtin_amdgcn_mfma_f32_16x16x32_bf16(__builtin_bit_cast(bf16x8, pf[i >> 1]), __builtin_bit_cast(bf16x8, vv[i >> 1][i & 1]), kp[i & 1], 0, 0, 0);
      asm volatile("" : "+v"(kp[0]), "+v"(kp[1]));
      ETICK(27);
      const auto sx0 = __builtin_amdgcn_permlane32_swap(cvtpk(kp[0].x, kp[0].y), cvtpk(kp[1].x, kp[1].y), false, false);
      const auto sx1 = __builtin_amdgcn_permlane32_swap(cvtpk(kp[0].z, kp[0].w), cvtpk(kp[1].z, kp[1].w), false, false);
      kvs = u32x4{sx0[0], sx1[0], sx0[1], sx1[1]};
      ETICK(28);
      OPS(MK(M_ST), MK(M_ST) + 1);
    }
    if constexpr (!isH) {
    } else {
      f32x16 acc[2][2];
#pragma unroll
      for (int i = 0; i < 4; ++i)
#pragma unroll
        for (int e = 0; e < 16; ++e) acc[i >> 1][i & 1][e] = 0.f;
      BPath bp = make_bpath(L_K3 + w * 2048, lane);
      u32x4 tf[2][2];
      asm volatile("s_waitcnt vmcnt(%2)" : "+v"(bf[0][0]), "+v"(bf[0][1]) : "n"(MK(M_ST) - T::find(OB, 0, 1)) : "memory");
      sts128(bp.wa, bf[0][0]); sts128(bp.wa + 1024, bf[0][1]);
      tf[0][0] = trfrag(bp.ra[0], bp.ra[0] + 1024); tf[0][1] = trfrag(bp.ra[1], bp.ra[1] + 1024);
      OPS(MK(M_ST) + 1, MK(M_JOB));
      __builtin_amdgcn_s_setprio(1);
      u32x4 lf[14][2];
      u32 lr[2][2];
      sfor<28>([&](auto nc) {
        constexpr int n = decltype(nc)::v, P = MK(M_JOB + n), NL = MK(M_JOB + n + 1) - P, nb = n < 13 ? n + 1 : 13;
        constexpr int k0 = n < CV0 ? 0 : n > 13 ? 9 : (n - CV0) * 9 / (14 - CV0), k1 = n < CV0 ? 0 : n > 13 ? 9 : (n + 1 - CV0) * 9 / (14 - CV0);
        constexpr int need = cmax(cmax(T::find(OA, n, 1), T::find(OB, nb, 1)), k1 > k0 ? T::find(OB, 18 + k1, 1) : 0);
        static_assert(need < P, "stream order");
        if constexpr (n == CV0) {
#pragma unroll
          for (int i = 0; i < 4; ++i) { lr[i >> 1][i & 1] = bp.ra[i & 1] + (L_R2 - L_K3) + (i >> 1) * 4 * 8192; pin1(lr[i >> 1][i & 1]); }
        }
        if constexpr (n == 14) {
          static_assert(T::find(OB, 18, 1) < T::find(OA, 14, 0) && T::find(OB, 27, 1) < T::find(OB, 18, 1), "stream order");
          VMWAIT(P - 1 - T::find(OB, 18, 1));
#pragma unroll
          for (int k = 0; k < 5; ++k) {
            pin2(bf[14 + k][0], bf[14 + k][1]);
            sts128(bp.wa, bf[14 + k][0]); sts128(bp.wa + 1024, bf[14 + k][1]);
            lf[k][0] = trfrag(bp.ra[0], bp.ra[0] + 1024); lf[k][1] = trfrag(bp.ra[1], bp.ra[1] + 1024);
          }
          SB();
        }
        if constexpr (n < 13) landed4<P - 1 - need>(af[n][0], af[n][1], bf[nb][0], bf[nb][1]);
        else { VMWAIT(P - 1 - need); pin2(af[n][0], af[n][1]); }
        sfor<4>([&](auto uc) {
          constexpr int u = decltype(uc)::v, c = u >> 1, t = u & 1;
          if constexpr (n < 14) acc[c][t] = mfma(tf[n & 1][c], af[n][t], acc[c][t]); else acc[c][t] = mfma(lf[n - 14][c], af[n][t], acc[c][t]);
          SB();
          asm volatile("" : "+v"(acc[c][t]));
          if constexpr (n < 13) {
            if constexpr (u == 0) {
              sts128(bp.wa, bf[nb][0]); sts128(bp.wa + 1024, bf[nb][1]);
              tf[nb & 1][0] = trfrag(bp.ra[0], bp.ra[0] + 1024); tf[nb & 1][1] = trfrag(bp.ra[1], bp.ra[1] + 1024);
            }
            SB();
          }
          if constexpr (u >= 1 && k0 + u - 1 < k1) {
            constexpr int k = k0 + u - 1;
#pragma unroll
            for (int i = 0; i < 2; ++i) lf[5 + k][i] = trfrag(lr[k >> 2 ? 1 : 0][i] + (k < 4 ? k : k - 4) * 8192, lr[k >> 2 ? 1 : 0][i] + (k < 4 ? k : k - 4) * 8192 + 1024);
            SB();
          }
          OPS(P + NL * u / 4, P + NL * (u + 1) / 4);
        });
      });
      dump_tile(acc, L_R2 + w * 2048, lane);
    }
  }
  if (isH) {
    const int tok = tid >> 3, pc = tid & 7;
    const u32 a = (tok >> 3) * 8192 + (tok & 7) * 256 + (((pc * 2) ^ (tok & 7)) * 16);
    f32x4 v[8][2];
#pragma unroll
    for (int u = 0; u < 8; ++u)
#pragma unroll
      for (int i = 0; i < 2; ++i) v[u][i] = __builtin_bit_cast(f32x4, lds128((u < 4 ? L_R2 + u * 2048 : L_R1 + (u - 4) * 2048) + (a ^ (i * 16))));
    const float omg = *(LAS float*)(size_t)(L_GW + tok * 4);
    const f32x4 s0 = ((v[0][0] + v[1][0]) + (v[2][0] + v[3][0])) * omg + ((v[4][0] + v[5][0]) + (v[6][0] + v[7][0]));
    const f32x4 s1 = ((v[0][1] + v[1][1]) + (v[2][1] + v[3][1])) * omg + ((v[4][1] + v[5][1]) + (v[6][1] + v[7][1]));
    const u32x4 res = {cvtpk(s0.x, s0.y), cvtpk(s0.z, s0.w), cvtpk(s1.x, s1.y), cvtpk(s1.z, s1.w)};
    *(u32x4*)(p.out + (rt64 * 64 + tok) * 896 + ct2 * 128 + pc * 16) = res;
  }
}

__global__ __launch_bounds__(512, 2) void fused(Params p) {
  asm volatile(".subsection 2\n.Lhk_text_end:\n\t.fill 256, 4, 0xBF800000\n\t.subsection 0");
  const int wave = rfl(threadIdx.x >> 6);
  if (wave == 1) {
    u64 pc; u32 len;
    asm volatile("s_getpc_b64 %0\n.Lpc%=:\n\ts_mov_b32 %1, .Lhk_text_end-.Lpc%=" : "=s"(pc), "=s"(len));
    const u32 off = ((threadIdx.x & 63) * 32 + (blockIdx.x >> 3)) * 128;
    asm volatile("s_mov_b32 m0, %2\n\ts_nop 0\n\tglobal_load_lds_dword %0, %1" ::"v"(off < len ? off : 0u), "s"(pc), "s"(L_DUM) : "memory");
  }
  *(LAS u32*)(size_t)(L_Z + (threadIdx.x >> 6) * 512 + 256 + (threadIdx.x & 63) * 4) = 1u;
  *(LAS u32*)(size_t)(L_CNT + (threadIdx.x & 15) * 4) = 0u;
  asm volatile("s_waitcnt lgkmcnt(0)\n\ts_barrier" ::: "memory");
  if (!((blockIdx.x & 1) && blockIdx.x >= 192)) {
    if (wave < 4) body<true, true>(p, wave); else body<false, true>(p, wave);
  } else {
    if (wave < 4) body<true, false>(p, wave); else body<false, false>(p, wave);
  }
}
}
#if defined(__HIP_DEVICE_COMPILE__)
#pragma clang attribute pop
#endif

extern "C" void kernel_launch(void* const* d_in, const int* in_sizes, int n_in, void* d_out, int out_size, void* d_ws, size_t ws_size,
                              hipStream_t stream) {
  hk::Params p;
  p.x = (const char*)d_in[0]; p.wqkv = (const char*)d_in[1]; p.wphi = (const char*)d_in[2]; p.wo = (const char*)d_in[3];
  p.wg = (const char*)d_in[5]; p.wu = (const char*)d_in[6]; p.wd = (const char*)d_in[7]; p.gw = (const char*)d_in[8];
  p.out = (char*)d_out; p.ws = (char*)d_ws;
  (void)hipFuncSetAttribute((const void*)hk::fused, hipFuncAttributeMaxDynamicSharedMemorySize, hk::L_TOTAL);
  hk::fused<<<256, 512, hk::L_TOTAL, stream>>>(p);
}
```

```cpp
#include <hip/hip_runtime.h>
#include <stdint.h>

#if defined(__HIP_DEVICE_COMPILE__)
#pragma clang attribute push(__attribute__((target("no-packed-fp32-ops"))), apply_to = function)
#endif
namespace hk {
typedef unsigned u32;
typedef unsigned long long u64;
typedef __bf16 bf16x8 __attribute__((ext_vector_type(8)));
typedef __bf16 bf16x2 __attribute__((ext_vector_type(2)));
typedef short s16x4 __attribute__((ext_vector_type(4)));
typedef u32 u32x4 __attribute__((ext_vector_type(4)));
typedef u32 u32x2 __attribute__((ext_vector_type(2)));
typedef float f32x16 __attribute__((ext_vector_type(16)));
typedef float f32x4 __attribute__((ext_vector_type(4)));
typedef float f32x2 __attribute__((ext_vector_type(2)));
typedef __amdgpu_buffer_rsrc_t srd_t;
#define LAS __attribute__((address_space(3)))
#define DI __device__ __forceinline__

constexpr int S = 2048, D = 448, DF = 1792;
constexpr u32 WS_E = 0;
constexpr u32 WS_FK = 4096;
constexpr u32 WS_FQ = 131072;
constexpr u32 WS_FH = 393216;
constexpr u32 WS_KVF = 655360;
constexpr u32 WS_KVP = 720896;
constexpr u32 WS_O = WS_KVP + 16 * 16 * 4096;
constexpr u32 WS_Q = WS_O + 64 * 32 * 1024;
constexpr u32 WS_H = WS_Q + 64 * 32 * 1024;
constexpr u32 WS_KT = WS_H + 64 * 112 * 1024;
constexpr u32 WS_END = WS_KT + 16 * 56 * 1024;
constexpr u32 OOB = 0x7ff00000u;
constexpr u32 L_K3 = 9 * 16384;
constexpr u32 L_WPHI = L_K3 + 8192;
constexpr u32 L_GW = L_WPHI + 2048;
constexpr u32 L_Z = L_GW + 1024;
constexpr u32 L_CNT = L_Z + 4096;
constexpr u32 L_DUM = L_CNT + 64;
constexpr u32 L_TOTAL = 163840;
enum { C_PROG = 0, C_SYNC = 16, C_KVF = 32 };
constexpr u32 L_KVF = 143360;

struct Params {
  const char *x, *wqkv, *wphi, *wo, *wg, *wu, *wd, *gw;
  char *out, *ws;
};

DI u32 f2u(float f) { return __builtin_bit_cast(u32, f); }
DI float u2f(u32 u) { return __builtin_bit_cast(float, u); }
DI u32 min(u32 a, u32 b) { return a < b ? a : b; }
DI u32 max(u32 a, u32 b) { return a > b ? a : b; }
DI u32 rfl(u32 v) { return __builtin_amdgcn_readfirstlane(v); }
DI srd_t mksrd(const void* p, u32 bytes) { return __builtin_amdgcn_make_buffer_rsrc((void*)p, 0, bytes, 0x00020000); }
DI u32 cvtpk(float lo, float hi) { f32x2 v = {lo, hi}; return __builtin_bit_cast(u32, __builtin_convertvector(v, bf16x2)); }
DI f32x16 mfma(u32x4 a, u32x4 b, f32x16 c) {
  return __builtin_amdgcn_mfma_f32_32x32x16_bf16(__builtin_bit_cast(bf16x8, a), __builtin_bit_cast(bf16x8, b), c, 0, 0, 0);
}
DI u32x2 tr64(u32 addr) { return __builtin_bit_cast(u32x2, __builtin_amdgcn_ds_read_tr16_b64_v4i16((LAS s16x4*)(size_t)addr)); }
DI u32x4 trfrag(u32 a0, u32 a1) { u32x2 lo = tr64(a0), hi = tr64(a1); return u32x4{lo.x, lo.y, hi.x, hi.y}; }
DI u32x4 lds128(u32 addr) { return *(LAS u32x4*)(size_t)addr; }
DI void sts128(u32 addr, u32x4 v) { *(LAS u32x4*)(size_t)addr = v; }
DI void sts64(u32 addr, u32x2 v) { *(LAS u32x2*)(size_t)addr = v; }
#define VMWAIT(n) asm volatile("s_waitcnt vmcnt(%0)" ::"n"(n) : "memory")
DI void dma_buf(u32 ldsaddr, u32 voff, srd_t srd, u32 soff) {
  asm volatile("s_mov_b32 m0, %0\n\ts_nop 0\n\tbuffer_load_dwordx4 %1, %2, %3 offen lds" ::"s"(ldsaddr), "v"(voff), "s"(srd), "s"(soff) : "memory");
}
template <u32 IMM, u32 SIMM> DI u32 dma_open(u32 base, u32 sbase) {
  u32 so;
  asm volatile("s_add_u32 m0, %1, %2\n\ts_add_u32 %0, %3, %4" : "=s"(so) : "s"(base), "n"(IMM), "s"(sbase), "n"(SIMM) : "scc", "memory");
  return so;
}
template <int OFF> DI void dma_off(u32 voff, srd_t srd, u32 soff) {
  asm volatile("buffer_load_dwordx4 %0, %1, %2 offen offset:%3 lds" ::"v"(voff), "s"(srd), "s"(soff), "n"(OFF) : "memory");
}
template <int OFF> DI void dma_off_sc1(u32 voff, srd_t srd, u32 soff) {
  asm volatile("buffer_load_dwordx4 %0, %1, %2 offen offset:%3 sc1 lds" ::"v"(voff), "s"(srd), "s"(soff), "n"(OFF) : "memory");
}
template <int OFF> DI void ldg_sc1o(u32x4& dst, u32 voff, srd_t srd, u32 soff) {
  asm volatile("buffer_load_dwordx4 %0, %1, %2, %3 offen offset:%4 sc1" : "=v"(dst) : "v"(voff), "s"(srd), "s"(soff), "n"(OFF) : "memory");
}
DI void ldg(u32x4& dst, u32 voff, srd_t srd, u32 soff) {
  asm volatile("buffer_load_dwordx4 %0, %1, %2, %3 offen" : "=v"(dst) : "v"(voff), "s"(srd), "s"(soff) : "memory");
}
template <int BIT, int OFF> DI void ldg2_if(u32x4& a, u32x4& b, u32 mask, u32 voff, srd_t srd, u32 soff) {
  asm volatile("s_bitcmp1_b32 %2, %3\n\ts_cbranch_scc0 .Lsk%=\n\tbuffer_load_dwordx4 %0, %4, %5, %6 offen offset:%7 sc1\n\tbuffer_load_dwordx4 %1, %4, %5, %6 offen offset:%8 sc1\n.Lsk%=:"
               : "+v"(a), "+v"(b) : "s"(mask), "n"(BIT), "v"(voff), "s"(srd), "s"(soff), "n"(OFF), "n"(OFF + 1024) : "scc", "memory");
}
template <int N> DI void landed4(u32x4& a, u32x4& b, u32x4& c, u32x4& d) {
  asm volatile("s_waitcnt vmcnt(%4)" : "+v"(a), "+v"(b), "+v"(c), "+v"(d) : "n"(N) : "memory");
}
template <int N> DI void wait3(u32x4& a, u32x4& b, u32x4& c, u64& f) {
  asm volatile("s_waitcnt vmcnt(%4)" : "+v"(a), "+v"(b), "+v"(c), "+v"(f) : "n"(N) : "memory");
}
DI void pin2(u32x4& a, u32x4& b) { asm volatile("" : "+v"(a), "+v"(b)::"memory"); }
DI void pin1(u32& a) { asm volatile("" : "+v"(a)); }
DI void touch(u32& sink, u32 voff, srd_t srd) {
  asm volatile("buffer_load_dword %0, %1, %2, 0 offen" : "+v"(sink) : "v"(voff), "s"(srd) : "memory");
}
DI void ldg_sc1(u32x4& dst, u32 voff, srd_t srd, u32 soff) {
  asm volatile("buffer_load_dwordx4 %0, %1, %2, %3 offen sc1" : "=v"(dst) : "v"(voff), "s"(srd), "s"(soff) : "memory");
}
DI void stg_sc1(u32x4 v, u32 voff, srd_t srd, u32 soff) {
  asm volatile("buffer_store_dwordx4 %0, %1, %2, %3 offen sc1\n\ts_nop 1" ::"v"(v), "v"(voff), "s"(srd), "s"(soff) : "memory");
}
struct Sync { u32 pa, ra, one; };
template <int C> DI void lds_post(const Sync& y) { asm volatile("ds_add_u32 %0, %1 offset:%2" ::"v"(y.pa), "v"(y.one), "n"(C) : "memory"); }
template <int C> DI u32 lds_peek(const Sync& y) { return *(volatile LAS u32*)(size_t)(y.ra + C); }
template <int C, int T> DI void lds_wait_pre(u32 a, u32 v) {
  u32 cnt;
  asm volatile("v_cmp_gt_u32 vcc, %3, %0\n\ts_cbranch_vccnz .Lsl%=\n.Lse%=:\n\t.subsection 1\n"
               ".Lsl%=:\n\ts_mov_b32 %1, 0x800000\n.Lsp%=:\n\tds_read_b32 %0, %2 offset:%4\n\ts_waitcnt lgkmcnt(0)\n\tv_cmp_gt_u32 vcc, %3, %0\n\ts_cbranch_vccz .Lse%=\n\t"
               "s_sub_u32 %1, %1, 1\n\ts_cbranch_scc0 .Lsp%=\n\ts_branch .Lse%=\n\t.subsection 0"
               : "+v"(v), "=&s"(cnt) : "v"(a), "n"(T), "n"(C) : "vcc", "scc", "memory");
}
template <int C, int T> DI void lds_wait(const Sync& y) { lds_wait_pre<C, T>(y.ra, lds_peek<C>(y)); }
template <int K> DI void z_post(u32 oob, srd_t srd) {
  asm volatile("buffer_load_dword %0, %1, 0 offen offset:%2 lds" ::"v"(oob), "s"(srd), "n"(4 * K + 4) : "memory");
}
DI void set_m0(u32 v) { asm volatile("s_mov_b32 m0, %0\n\ts_nop 0" ::"s"(v) : "memory"); }
template <int K> DI u32 z_peek(u32 za) { return *(volatile LAS u32*)(size_t)(za + 4 * K); }
template <int K> DI void z_wait_pre(u32 za, u32 v) {
  u32 cnt;
  asm volatile("v_cmp_ne_u32 vcc, 0, %0\n\ts_cbranch_vccnz .Lsl%=\n.Lse%=:\n\t.subsection 1\n"
               ".Lsl%=:\n\ts_mov_b32 %1, 0x800000\n.Lsp%=:\n\tds_read_b32 %0, %2 offset:%3\n\ts_waitcnt lgkmcnt(0)\n\tv_cmp_ne_u32 vcc, 0, %0\n\ts_cbranch_vccz .Lse%=\n\t"
               "s_sub_u32 %1, %1, 1\n\ts_cbranch_scc0 .Lsp%=\n\ts_branch .Lse%=\n\t.subsection 0"
               : "+v"(v), "=&s"(cnt) : "v"(za), "n"(4 * K) : "vcc", "scc", "memory");
}
template <int K> DI void z_wait(u32 za) { z_wait_pre<K>(za, z_peek<K>(za)); }
DI void flag_peek(u64& v, srd_t ws, u32 voff) {
  asm volatile("buffer_load_dwordx2 %0, %1, %2, 0 offen sc1" : "=v"(v) : "v"(voff), "s"(ws) : "memory");
}
template <int N> DI void flag_check(u64 v, srd_t ws, u32 voff, u64 tag) {
  u32 cnt;
  asm volatile("s_waitcnt vmcnt(%5)\n\t"
               "v_cmp_ne_u64 vcc, %4, %0\n\ts_cbranch_vccnz .Lfl%=\n.Lfe%=:\n\t.subsection 1\n"
               ".Lfl%=:\n\ts_mov_b32 %1, 0x40000\n"
               ".Lfp%=:\n\tbuffer_load_dwordx2 %0, %2, %3, 0 offen sc1\n\ts_waitcnt vmcnt(0)\n\tv_cmp_ne_u64 vcc, %4, %0\n\ts_cbranch_vccz .Lfe%=\n\t"
               "s_sleep 1\n\ts_sub_u32 %1, %1, 1\n\ts_cbranch_scc0 .Lfp%=\n\ts_branch .Lfe%=\n.Lfz%=:\n\t.subsection 0"
               : "+v"(v), "=&s"(cnt) : "v"(voff), "s"(ws), "s"(tag), "n"(N) : "vcc", "scc", "memory");
}
DI void flag_wait(srd_t ws, u32 voff, u64 tag) { u64 v; flag_peek(v, ws, voff); flag_check<0>(v, ws, voff, tag); }
DI void flag_set(srd_t ws, u32 voff, u64 tag) {
  asm volatile("buffer_store_dwordx2 %0, %1, %2, 0 offen sc1" ::"v"(tag), "v"(voff), "s"(ws) : "memory");
}
DI u32x4 not_ones(u32x4 v) { return u32x4{min(v.x, 0xfffffffeu), min(v.y, 0xfffffffeu), min(v.z, 0xfffffffeu), min(v.w, 0xfffffffeu)}; }
template <int N> DI bool arrived(const u32x4 (&v)[N]) {
  u32 m = 0;
#pragma unroll
  for (int i = 0; i < N; ++i) m = max(max(max(m, v[i].x), v[i].y), max(v[i].z, v[i].w));
  return __builtin_amdgcn_ballot_w64(m == 0xffffffffu) == 0;
}
template <bool FORCE, int NZ> DI void flag_try(u32& done, srd_t ws, u32 voff, u64 tag) {
  u32 t;
#define FT_TAIL "s_cbranch_scc0 .Lfp%=\n.Lfe%=:\n\t.subsection 1\n.Lfp%=:\n\ts_waitcnt vmcnt(%5)\n\tbuffer_store_dwordx2 %2, %3, %4, 0 offen sc1\n\t" \
                "s_mov_b32 %0, 64\n\ts_branch .Lfe%=\n.Lfz%=:\n\t.subsection 0"
  if (FORCE) asm volatile("s_cmp_lg_u32 %0, 0\n\t" FT_TAIL : "+s"(done), "=&s"(t) : "v"(tag), "v"(voff), "s"(ws), "n"(NZ) : "scc", "memory");
  else asm volatile("s_getreg_b32 %1, hwreg(HW_REG_IB_STS, 0, 4)\n\ts_or_b32 %1, %1, %0\n\ts_cmp_gt_u32 %1, %5\n\t" FT_TAIL
                    : "+s"(done), "=&s"(t) : "v"(tag), "v"(voff), "s"(ws), "n"(NZ) : "scc", "memory");
}
#define SB() __builtin_amdgcn_sched_barrier(0)
#define OPAQUE(x) asm volatile("" : "+v"(x))
DI float swap_hi(float lo_wants, int h) {
  auto r = __builtin_amdgcn_permlane32_swap(f2u(lo_wants), f2u(lo_wants), false, false);
  (void)h;
  return u2f(r[1]);
}
DI void both_halves(float v, float& lo, float& hi) {
  auto r = __builtin_amdgcn_permlane32_swap(f2u(v), f2u(v), false, false);
  lo = u2f(r[0]); hi = u2f(r[1]);
}
constexpr int SPINS = 200000;
constexpr float LOG2E = 1.4426950408889634f;

template <int I> struct IC { static constexpr int v = I; };
template <int N, class F, int I = 0> DI void sfor(F f) { if constexpr (I < N) { f(IC<I>{}); sfor<N, F, I + 1>(f); } }

enum { OX, OW, OU, OK, OZ, OPEEK, OB, OA, OST };
struct Op { int kind, idx, i; };
enum { M_KV0, M_EPI, M_ST, M_JOB };
struct Tab { Op v[300]; int mark[48]; int n; };
constexpr int AW = 4;
constexpr int PRED = 8;
constexpr int LW = 6;
constexpr int CV0 = 10;
constexpr int PD = 5;
constexpr int zidx(bool H, int kind, int idx) { return kind == OW ? idx : kind == OK ? (H ? 7 : 14) + idx : (H ? 11 : 7) + idx; }
constexpr int rpos(bool H, int kind, int idx) { return kind == OX ? 2 * idx : kind == OW ? 2 * idx + 1 : kind == OK ? (H ? 14 : 21) + idx : (H ? 17 : 14) + idx; }
constexpr u32 slot_of(bool H, int kind, int idx) { return kind == OK && idx == 3 ? 9 : rpos(H, kind, idx) % 9; }
constexpr Tab make_tab(bool H) {
  Tab t{}; int n = 0;
  auto I4 = [&](int kind, int idx, int lo = 0, int hi = 4) {
    for (int i = lo; i < hi; ++i) t.v[n++] = Op{kind, idx, i};
    if (kind != OX && kind <= OK && hi == (kind == OK && idx == 3 ? 2 : 4)) t.v[n++] = Op{OZ, zidx(H, kind, idx), 0};
  };
  for (int s = 0; s < 7; ++s) { if (s == 6 && H) t.v[n++] = Op{OPEEK, 0, 0}; I4(OX, s); I4(OW, s); }
  if (!H) for (int s = 0; s < 7; ++s) { if (s == 5) t.v[n++] = Op{OPEEK, 0, 0}; I4(OU, s); }
  I4(OK, 0); I4(OK, 1); I4(OK, 2); I4(OK, 3, 0, 2);
  t.mark[M_KV0] = n;
  if (H) for (int s = 0; s < 7; ++s) I4(OU, s);
  else t.v[n++] = Op{OZ, 18, 0};
  t.mark[M_EPI] = n;
  if (H) for (int j = 0; j < 14; ++j) { if (j == 11) t.v[n++] = Op{OPEEK, 2, 0}; I4(OB, j, 0, 2); }
  t.mark[M_ST] = n; t.v[n++] = Op{OST, 0, 0};
  if (H) {
    const int ord[17] = {31, 32, 33, 34, 35, 19, 20, 21, 22, 23, 24, 25, 26, 28, 29, 30, 27};
    int nd = 0, na = AW;
    for (int j = 0; j < AW; ++j) I4(OA, j, 0, 2);
    for (; nd < PRED; ++nd) I4(OB, ord[nd], 0, 2);
    for (int j = 0; j < 28; ++j) {
      t.mark[M_JOB + j] = n;
      if (j == 11) t.v[n++] = Op{OPEEK, 3, 0};
      for (int k = 0; k < 2; ++k) {
        if (na < 14) { I4(OA, na, 0, 2); ++na; }
        else if (nd < 17) { I4(OB, ord[nd], 0, 2); if (++nd == 16) t.v[n++] = Op{OZ, 18, 0}; }
        else if (nd < 22) { I4(OB, nd - 3, 0, 2); ++nd; }
      }
      if (j == 12) for (int k = 14; k < 14 + LW; ++k) I4(OA, k, 0, 2);
      if (j >= 14 && j + LW < 28) I4(OA, j + LW, 0, 2);
    }
    t.mark[M_JOB + 28] = n;
  }
  t.n = n;
  return t;
}
template <bool H> struct TB {
  static constexpr Tab t = make_tab(H);
  static constexpr int find(int kind, int idx, int i) { for (int k = 0; k < t.n; ++k) if (t.v[k].kind == kind && t.v[k].idx == idx && t.v[k].i == i) return k; return 1000; }
  static constexpr int etick(int n) {
    int k = t.mark[M_EPI];
    for (int i = 0; i < n && k < t.mark[M_ST]; ++i) { ++k; while (k < t.mark[M_ST] && (t.v[k].kind == OZ || t.v[k - 1].kind == OPEEK)) ++k; }
    return n >= 29 ? t.mark[M_ST] : k;
  }
  static constexpr int need0(int k) {
    if (k >= t.n || t.v[k].kind > OK || t.v[k].i != 0 || slot_of(H, t.v[k].kind, t.v[k].idx) == 9) return -1;
    const int p = rpos(H, t.v[k].kind, t.v[k].idx);
    return p < 9 || (p > 22 && H) ? -1 : (p - 9) >> 1;
  }
  static constexpr int need(int k) { for (int i = 0; i < k && i < t.n; ++i) if (need0(i) >= need0(k)) return -1; return need0(k); }
};
constexpr u32 L_R2 = 0, L_R1 = 9 * 8192;
constexpr int LAG = 12;
constexpr int cmax(int a, int b) { return a > b ? a : b; }

DI void mfma16i(f32x4& c, u32x4 a, u32x4 b) { asm volatile("v_mfma_f32_16x16x32_bf16 %0, %1, %2, %0" : "+v"(c) : "v"(a), "v"(b)); }
DI void mfma16z(f32x4& c, u32x4 a, u32x4 b) { asm volatile("v_mfma_f32_16x16x32_bf16 %0, %1, %2, 0" : "=&v"(c) : "v"(a), "v"(b)); }
template <bool HG, bool FIRST, class Hook>
DI void unit_gemm(u32x4 (&xf)[28], f32x4 (&acc)[8][2], int lane, int w, u32 za, Hook hook) {
  const u32 kg = lane >> 4, q = (lane & 15) >> 2, pp = lane & 3, t16 = lane & 15;
  const u32 vb0 = (8 * kg + q) * 128 + ((q >> 1) | ((kg & 1) << 1)) * 32 + (((pp & 1) << 1) | (pp >> 1)) * 8;
  constexpr int KIND = FIRST ? OW : OU;
  const u32 xb = w * 4096 + t16 * 128 + ((kg ^ (t16 >> 1)) * 16);
  u32 vb[2][4], xa[2][2], pre = 0;
#pragma unroll
  for (int c = 0; c < 8; ++c) { vb[c >> 2][c & 3] = (vb0 ^ ((c & 3) * 32)) + (c < 4 && !FIRST ? 131072 : (c >> 2) * 65536); OPAQUE(vb[c >> 2][c & 3]); }
  if (FIRST) {
#pragma unroll
    for (int c = 0; c < 4; ++c) { xa[c >> 1][c & 1] = (xb ^ ((c & 1) * 64)) + (c >> 1) * 81920; OPAQUE(xa[c >> 1][c & 1]); }
  }
  u32x2 fl[16], fh[16];
  auto wadr = [&](int s, int n) -> u32 { const int slot = slot_of(HG, KIND, s); return vb[slot >> 2 & 1][n & 3] + (slot & 3) * 16384 + ((n >> 2) & 1) * 8192 + (n >> 3) * 4096; };
  auto rdx = [&](int s, int i) { const int slot = slot_of(HG, OX, s); xf[4 * s + i] = lds128((slot < 5 ? xa[0][i >> 1] + slot * 16384 : xa[1][i >> 1] + (slot - 5) * 16384) + (i & 1) * 2048); };
  z_wait<zidx(HG, KIND, 0)>(za);
  if (FIRST) { rdx(0, 0); rdx(0, 1); rdx(0, 2); }
#pragma unroll
  for (int n = 0; n < PD; ++n) { fl[n] = tr64(wadr(0, n)); fh[n] = tr64(wadr(0, n) + 512); }
  SB();
  sfor<112>([&](auto gc) {
    constexpr int G = decltype(gc)::v, s = G >> 4, n = G & 15, f = n & 7, n4 = (n + PD) & 15, s4 = n < 16 - PD ? s : s + 1;
    constexpr bool all = HG || FIRST;
    constexpr bool live = all || f < 6, next = s4 < 7 && (all || (n4 & 7) < 6);
    if constexpr (all && (n & 1) == 0 && G < 112 - PD) { __builtin_amdgcn_s_waitcnt(0xC07F | ((2 * PD - 4) << 8)); SB(); }
    if constexpr (live) {
      if constexpr (G < 8) mfma16z(acc[f][0], u32x4{fl[n].x, fl[n].y, fh[n].x, fh[n].y}, xf[4 * s + (n >> 3) * 2 + 0]);
      else mfma16i(acc[f][0], u32x4{fl[n].x, fl[n].y, fh[n].x, fh[n].y}, xf[4 * s + (n >> 3) * 2 + 0]);
      SB();
    }
    if constexpr (n == 16 - PD && s < 6) z_wait_pre<zidx(HG, KIND, s + 1)>(za, pre);
    if constexpr (!FIRST && slot_of(HG, KIND, s) == 8 && n >= 16 - PD && n4 < 4) { vb[0][n4] -= 131072; OPAQUE(vb[0][n4]); }
    if constexpr (next) fl[n4] = tr64(wadr(s4, n4));
    SB();
    if constexpr (live) {
      if constexpr (G < 8) mfma16z(acc[f][1], u32x4{fl[n].x, fl[n].y, fh[n].x, fh[n].y}, xf[4 * s + (n >> 3) * 2 + 1]);
      else mfma16i(acc[f][1], u32x4{fl[n].x, fl[n].y, fh[n].x, fh[n].y}, xf[4 * s + (n >> 3) * 2 + 1]);
      SB();
    }
    if constexpr (next) fh[n4] = tr64(wadr(s4, n4) + 512);
    if constexpr (FIRST) {
      if constexpr (n == 0) rdx(s, 3);
      if constexpr (n >= 16 - PD && n4 < 3 && s < 6) rdx(s + 1, n4);
    }
    if constexpr (n == 12 - PD && s < 6) pre = z_peek<zidx(HG, KIND, s + 1)>(za);
    hook(gc);
    SB();
  });
  asm volatile("s_nop 7" : "+v"(acc[0][0]), "+v"(acc[0][1]), "+v"(acc[1][0]), "+v"(acc[1][1]), "+v"(acc[2][0]), "+v"(acc[2][1]), "+v"(acc[3][0]), "+v"(acc[3][1]),
               "+v"(acc[4][0]), "+v"(acc[4][1]), "+v"(acc[5][0]), "+v"(acc[5][1]));
  if (HG || FIRST) asm volatile("" : "+v"(acc[6][0]), "+v"(acc[6][1]), "+v"(acc[7][0]), "+v"(acc[7][1]));
  SB();
}
DI u32x4 frag16(f32x4 a, f32x4 b) {
  const auto x = __builtin_amdgcn_permlane16_swap(cvtpk(a.x, a.y), cvtpk(b.x, b.y), false, false);
  const auto y = __builtin_amdgcn_permlane16_swap(cvtpk(a.z, a.w), cvtpk(b.z, b.w), false, false);
  return u32x4{x[0], y[0], x[1], y[1]};
}

DI void load_wphi(u32x4 (&wp)[2], u32 base, int lane) {
  const u32 h = lane >> 5, fb = (lane >> 4) & 1, q = (lane & 15) >> 2, pp = lane & 3;
  u32 col = 16 * fb + 4 * pp; if (col == 28) col = 24;
  const u32 a = base + (4 * h + q) * 56 + col * 2;
  wp[0] = trfrag(a, a + 8 * 56);
  u32x2 lo = tr64(a + 16 * 56), hi = tr64(base + (24 + q) * 56 + col * 2);
  wp[1] = u32x4{lo.x, lo.y, h ? 0u : hi.x, h ? 0u : hi.y};
}
DI void load_wphi3(u32x4 (&wp)[3], u32 base, int o, int lane) {
  const u32 h = lane >> 5, fb = (lane >> 4) & 1, q = (lane & 15) >> 2, pp = lane & 3;
  u32 col = 16 * fb + 4 * pp; if (col == 28) col = 24;
#pragma unroll
  for (int i = 0; i < 6; ++i) {
    const u32 d0 = 8 * i + 4 * h - o;
    const bool ok = d0 <= 24;
    const u32x2 v = tr64(base + ((ok ? d0 : 0u) + q) * 56 + col * 2);
    wp[i >> 1][(i & 1) * 2] = ok ? v.x : 0u; wp[i >> 1][(i & 1) * 2 + 1] = ok ? v.y : 0u;
  }
}
DI u32x4 pack8(const f32x16& v, int s) {
  return u32x4{cvtpk(v[8 * s], v[8 * s + 1]), cvtpk(v[8 * s + 2], v[8 * s + 3]), cvtpk(v[8 * s + 4], v[8 * s + 5]), cvtpk(v[8 * s + 6], v[8 * s + 7])};
}
DI float sigmoid(float t) { return __builtin_amdgcn_rcpf(1.f + __builtin_amdgcn_exp2f(-LOG2E * t)); }
DI float vmax(float a, float b) { float r; asm("v_max_f32 %0, %1, %2" : "=v"(r) : "v"(a), "v"(b)); return r; }
DI float vmin(float a, float b) { float r; asm("v_min_f32 %0, %1, %2" : "=v"(r) : "v"(a), "v"(b)); return r; }
DI float max3(float a, float b, float c) { return __builtin_fmaxf(__builtin_fmaxf(a, b), c); }
DI float min3(float a, float b, float c) { return __builtin_fminf(__builtin_fminf(a, b), c); }
template <class Hook> DI void feature_exp(const u32x4 (&t)[3], const u32x4 (&wp)[3], f32x16& ep, f32x16& en, Hook hook) {
  f32x16 pz;
#pragma unroll
  for (int e = 0; e < 16; ++e) pz[e] = 0.f;
  pz = mfma(wp[0], t[0], pz);
  pz = mfma(wp[1], t[1], pz);
  pz = mfma(wp[2], t[2], pz);
  float mx = max3(max3(max3(pz[0], pz[1], pz[2]), max3(pz[3], pz[4], pz[5]), max3(pz[6], pz[7], pz[8])),
                  max3(max3(pz[9], pz[10], pz[11]), max3(pz[12], pz[13], pz[14]), pz[15]), pz[15]);
  float mn = min3(min3(min3(pz[0], pz[1], pz[2]), min3(pz[3], pz[4], pz[5]), min3(pz[6], pz[7], pz[8])),
                  min3(min3(pz[9], pz[10], pz[11]), min3(pz[12], pz[13], pz[14]), pz[15]), pz[15]);
  float a, b;
  both_halves(mx, a, b); mx = __builtin_fmaxf(a, b) * LOG2E;
  both_halves(mn, a, b); mn = __builtin_fminf(a, b) * LOG2E;
  asm volatile("" : "+v"(mx), "+v"(mn));
  hook();
#pragma unroll
  for (int e = 0; e < 16; ++e) ep[e] = __builtin_amdgcn_exp2f(__builtin_fmaf(pz[e], LOG2E, -mx));
  asm volatile("" : "+v"(ep));
  hook();
#pragma unroll
  for (int e = 0; e < 16; ++e) en[e] = __builtin_amdgcn_exp2f(__builtin_fmaf(pz[e], -LOG2E, mn));
  asm volatile("" : "+v"(en));
  hook();
}
DI void reduce_kv(int hd, int pc, srd_t ws, int lane) {
  const u32 h = lane >> 5, r = lane & 31, l16 = lane * 16;
  u32x4 pv[8];
  __builtin_amdgcn_s_sleep(12);
  for (int it = 0; it < SPINS; ++it) {
#pragma unroll
    for (int i = 0; i < 8; ++i) ldg_sc1(pv[i], h * 4096 + r * 16, ws, WS_KVP + (hd * 16 + 2 * i) * 4096 + pc * 512);
    asm volatile("s_waitcnt vmcnt(0)" : "+v"(pv[0]), "+v"(pv[1]), "+v"(pv[2]), "+v"(pv[3]), "+v"(pv[4]), "+v"(pv[5]), "+v"(pv[6]), "+v"(pv[7])::"memory");
    if (arrived(pv)) break;
  }
  u32x4 o;
#pragma unroll
  for (int e = 0; e < 4; ++e) {
    float lo[8], hi[8];
#pragma unroll
    for (int i = 0; i < 8; ++i) { lo[i] = u2f(pv[i][e] << 16); hi[i] = u2f(pv[i][e] & 0xffff0000u); }
    float a, c, d, f;
    both_halves(((lo[0] + lo[1]) + (lo[2] + lo[3])) + ((lo[4] + lo[5]) + (lo[6] + lo[7])), a, c);
    both_halves(((hi[0] + hi[1]) + (hi[2] + hi[3])) + ((hi[4] + hi[5]) + (hi[6] + hi[7])), d, f);
    o[e] = cvtpk(a + c, d + f);
    if (r == 29) o[e] = 0x3f803f80u;
    if (e >= 2 && (pc & 3) == 3) o[e] = 0u;
  }
  stg_sc1(not_ones(o), h ? OOB : l16, ws, WS_KVF + hd * 4096 + pc * 512);
}
template <bool H, class Epi, class Mid>
DI void task_o(int head, int rt32, int w, srd_t ws, u64 tag, int lane, const Sync& y, u64 fq, u32 fqa, u32 fdone, u32x4 own0, u32x4 own1, Epi epi, Mid mid) {
  const int h = lane >> 5;
  const u32 l16 = lane * 16;
  const int ro = (head * 28) & 15;
  u32x4 qf[3], wp[3], kf[4], k1[1];
  auto ldq = [&](u32 vo) {
    const u32 so = WS_Q + (rt32 * 28 + ((head * 7) >> 2)) * 1024;
    ldg_sc1o<0>(qf[0], vo, ws, so); ldg_sc1o<1024>(qf[1], vo, ws, so); ldg_sc1o<2048>(qf[2], ro >= 8 ? vo : OOB, ws, so);
  };
  auto ldk = [&]() { ldg_sc1(k1[0], l16, ws, WS_KVF + head * 4096 + w * 1024); };
  bool early = false, second = false;
  if (H) {
    asm volatile("s_waitcnt vmcnt(0)" : "+v"(fq)::"memory");
    early = __builtin_amdgcn_ballot_w64(fq != tag) == 0;
    ldq(early ? l16 : OOB);
    flag_peek(fq, ws, early ? OOB : fqa);
  }
  epi([&](int c) {
    if (H && c == 2) {
      wait3<3>(qf[0], qf[1], qf[2], fq);
      if (!early && __builtin_amdgcn_ballot_w64(fq != tag) == 0) { early = second = true; ldq(l16); }
    }
  });
  __builtin_amdgcn_s_setprio(H ? 3 : 0);
  u32 done = 0;
  if (H) { if (second) VMWAIT(1); wait3<63>(qf[0], qf[1], qf[2], fq); }
  if (!early) {
    flag_try<true, 0>(done, ws, fdone, tag);
    if (H) {
      flag_wait(ws, fqa, tag);
      ldq(l16);
      asm volatile("s_waitcnt vmcnt(0)" : "+v"(qf[0]), "+v"(qf[1]), "+v"(qf[2])::"memory");
    } else {
      qf[0] = own0; qf[1] = own1; qf[2] = u32x4{0u, 0u, 0u, 0u};
      if ((rt32 & 28) != 28) mid();
    }
  }
  float gate = 0.f;
  if (H) {
    const u32 p1 = lds_peek<C_SYNC>(y), p2 = lds_peek<C_PROG>(y);
    lds_wait_pre<C_SYNC, 1>(y.ra, p1); lds_wait_pre<C_PROG, 1>(y.ra, p2);
    gate = *(LAS float*)(size_t)(L_GW + 256 + (w * 32 + (lane & 31)) * 4);
  }
  load_wphi3(wp, L_WPHI, ro, lane);
  if (H) { gate = sigmoid(gate); OPAQUE(gate); }
  int hk = 0;
  f32x16 ep, en; feature_exp(qf, wp, ep, en, [&]() { if (hk++ == 0) ldk(); flag_try<false, 1>(done, ws, fdone, tag); });
  __builtin_amdgcn_s_setprio(3);
  if (early) { flag_try<true, 1>(done, ws, fdone, tag); VMWAIT(1); } else VMWAIT(0);
  asm volatile("" : "+v"(k1[0])::"memory");
  if (!H && (rt32 & 28) == 28) mid();
  if (!H) {
    lds_wait<C_SYNC, 1>(y); lds_wait<C_PROG, 1>(y);
    gate = sigmoid(*(LAS float*)(size_t)(L_GW + 256 + (w * 32 + (lane & 31)) * 4));
    OPAQUE(gate);
  }
  for (int it = 0; it < SPINS && !arrived(k1); ++it) {
    __builtin_amdgcn_s_sleep(12);
    ldk();
    asm volatile("s_waitcnt vmcnt(0)" : "+v"(k1[0])::"memory");
  }
  sts128(L_KVF + w * 1024 + l16, k1[0]);
  lds_post<C_KVF>(y);
  asm volatile("" : "+v"(ep), "+v"(en));
  u32x4 ef[2][2] = {{pack8(ep, 0), pack8(ep, 1)}, {pack8(en, 0), pack8(en, 1)}};
  asm volatile("" : "+v"(ef[0][0]), "+v"(ef[0][1]), "+v"(ef[1][0]), "+v"(ef[1][1]));
  lds_wait<C_KVF, 1>(y);
#pragma unroll
  for (int i = 0; i < 4; ++i) kf[i] = lds128(L_KVF + i * 1024 + l16);
  f32x16 np, nn;
#pragma unroll
  for (int e = 0; e < 16; ++e) { np[e] = 0.f; nn[e] = 0.f; }
  np = mfma(kf[0], ef[0][0], np); nn = mfma(kf[2], ef[1][0], nn);
  np = mfma(kf[1], ef[0][1], np); nn = mfma(kf[3], ef[1][1], nn);
  const float rp = __builtin_amdgcn_rcpf(np[13]), rn = __builtin_amdgcn_rcpf(nn[13]);
  const float inv = __builtin_amdgcn_rcpf(np[12] * rp + nn[12] * rn + 1e-6f) * gate;
  const float cp = swap_hi(rp * inv, h), cn = swap_hi(rn * inv, h);
  f32x16 o;
#pragma unroll
  for (int e = 0; e < 16; ++e) o[e] = np[e] * cp + nn[e] * cn;
  u32x4 o0 = pack8(o, 0), o1 = pack8(o, 1);
  const float omg = 1.f - gate, oh = u2f(cvtpk(omg, 0.f) << 16);
  o1.z = h ? cvtpk(oh, omg - oh) : o1.z; o1.w = h ? 0u : o1.w;
  stg_sc1(not_ones(o0), l16, ws, WS_O + (rt32 * 32 + head * 2) * 1024);
  stg_sc1(not_ones(o1), l16, ws, WS_O + (rt32 * 32 + head * 2 + 1) * 1024);
}
DI void dump_tile(const f32x16 (&acc)[2][2], u32 base, int lane) {
  const u32 r = lane & 31, a = base + (r >> 3) * 8192 + (r & 7) * 256 + (((lane >> 5) ^ (r & 7)) * 16);
#pragma unroll
  for (int g4 = 0; g4 < 4; ++g4) {
    u32 ag = a ^ (g4 * 32); OPAQUE(ag);
#pragma unroll
    for (int c = 0; c < 2; ++c)
#pragma unroll
      for (int t = 0; t < 2; ++t)
        sts128(ag + t * 32768 + c * 128, __builtin_bit_cast(u32x4, f32x4{acc[c][t][4 * g4], acc[c][t][4 * g4 + 1], acc[c][t][4 * g4 + 2], acc[c][t][4 * g4 + 3]}));
  }
  asm volatile("s_waitcnt lgkmcnt(0)\n\ts_barrier" ::: "memory");
}
DI u32 brow_off(int lane) { const u32 row = lane >> 3; return row * 896 + ((lane & 7) ^ (((row >> 1) & 1) * 4)) * 16; }
struct BPath { u32 wa, ra[2]; };
DI BPath make_bpath(u32 base, int lane) {
  const u32 h = lane >> 5, cb = (lane >> 4) & 1, q = (lane & 15) >> 2, pp = lane & 3;
  BPath t;
  t.wa = base + lane * 16; OPAQUE(t.wa);
#pragma unroll
  for (int c = 0; c < 2; ++c) { t.ra[c] = base + (4 * h + q) * 128 + ((c ^ ((q >> 1) & 1)) * 64) + cb * 32 + pp * 8; OPAQUE(t.ra[c]); }
  return t;
}

template <class Hook> DI void epi_h(const f32x4 (&acc)[8][2], srd_t ws, u32 soff, int lane, Hook hook) {
#pragma unroll
  for (int c = 0; c < 4; ++c) {
    float hv[8];
#pragma unroll
    for (int e = 0; e < 8; ++e) {
      const float x = acc[c][e >> 2][e & 3];
      const float t = __builtin_amdgcn_exp2f(x * __builtin_fmaf(x * x, -0.10294323970f, -2.30220819814f));
      hv[e] = x * __builtin_amdgcn_rcpf(1.f + t) * acc[4 + c][e >> 2][e & 3];
    }
    const u32x4 hp = frag16(f32x4{hv[0], hv[1], hv[2], hv[3]}, f32x4{hv[4], hv[5], hv[6], hv[7]});
    stg_sc1(hp, lane * 16, ws, soff + c * 1024);
    hook(c);
  }
}

template <bool P1, bool isH> DI void body(const Params& p, const int wave) {
  const int tid = threadIdx.x, lane = tid & 63;
  const int w = wave & 3;
  const int b = blockIdx.x, xcd = b & 7, bi = b >> 3;
  const int m = 8 * (xcd >> 2) + (bi & 7), sl = (xcd & 1) * 4 + (bi >> 3), half = (xcd >> 1) & 1;
  const int tl = half * 56 + (xcd & 1) * 32 + bi, rt64 = 16 * (xcd >> 2) + tl / 7, ct2 = tl % 7;
  const int j = 7 * half + sl;
  const int kvhead = 8 * half + sl;
  const int h = lane >> 5;
  const u32 r = lane & 31, l4 = (lane & 3) * 4, l16 = lane * 16;
  const u32 cb = (lane >> 4) & 1, q = (lane & 15) >> 2, pp = lane & 3;
  const srd_t ws = mksrd(p.ws, WS_END), sqkv = mksrd(p.wqkv, D * 3 * D * 2);
  const int rt32 = m * 4 + w;

  if (P1) __builtin_amdgcn_s_setprio(2);
  const u32 zm = L_Z + wave * 512;
  u32 z1 = L_Z + (lane & 3) * 512 + 256, z2 = z1 + 2048, oob = OOB;
  OPAQUE(z1); OPAQUE(z2); OPAQUE(oob);
  Sync y{lane == 0 ? L_CNT + w * 4 : L_DUM + lane * 4, L_CNT + l4, 1u};
  OPAQUE(y.pa); OPAQUE(y.ra); OPAQUE(y.one);
  u32x2 ev;
  asm volatile("buffer_load_dwordx2 %0, off, %1, 0 sc1" : "=v"(ev) : "s"(ws) : "memory");

  if (P1) {
    u32 fvo, sink = 0, tvo2;
    const srd_t st2 = mksrd(w == 0 ? p.wg : w == 1 ? p.wu : w == 2 ? p.wd : p.wo, w < 3 ? D * DF * 2 : D * D * 2);
    {
      const u32 scr = L_K3 + w * 1024;
      const int km = rt32;
      dma_buf(scr, km < 56 ? (lane >> 2) * 2688 + (lane & 3) * 16 : OOB, sqkv, (km >> 1) * 16 * 2688 + (1 + (km & 1)) * 896 + kvhead * 56);
      {
        const srd_t sp = mksrd(p.wphi, 16 * 1568);
        set_m0((w & 1) ? L_K3 + 4096 : L_WPHI);
        dma_off<0>((w & 1) ? OOB : l16, sp, kvhead * 1568);
        dma_off<1024>((w & 1) || lane >= 34 ? OOB : l16, sp, kvhead * 1568);
      }
      u32x4 gwf;
      ldg(gwf, r == 30 ? h * 16 : OOB, mksrd(p.gw, D * 2), (km >> 1) * 32);
      {
        const u32 row = (xcd >> 2) * 224 + bi * 7 + (lane >> 2), c4 = lane & 3;
        const u32 wv = (lane < 28 && !((xcd & 1) && c4 == 3)) ? row * 3584 + (half * 7 + (xcd & 1) * 4 + c4) * 128 : OOB;
        const u32 tvo1 = w < 2 ? wv : w == 2 ? (lane < 56 ? ((xcd * 32 + bi) * 56 + lane) * 128 : OOB)
                               : ((xcd & 1) && lane < 28 ? row * 2688 + (half * 3 + c4) * 128 : OOB);
        touch(sink, tvo1, w < 2 ? st2 : w == 2 ? mksrd(p.x, S * D * 2) : sqkv);
        tvo2 = w < 2 ? wv + 14 * 128 : w == 2 ? (lane < 49 ? (b * 49 + lane) * 128 : OOB) : (lane < 13 ? (b * 13 + lane) * 128 : OOB);
      }
      touch(sink, lane * 2097152 + 64 * 1024 - 128, w == 0 ? ws : w == 1 ? mksrd(p.out, S * D * 2) : st2);
      const u32x4 ones = {~0u, ~0u, ~0u, ~0u};
      stg_sc1(ones, l16, ws, WS_KVP + ((kvhead * 16 + m) * 4 + w) * 1024);
      __builtin_amdgcn_s_setprio(3);
      asm volatile("s_waitcnt vmcnt(3)" : "+v"(ev), "+v"(gwf)::"memory");
      u32x4 kt;
      if (w & 1) {
        const u32 a = scr + (8 * h + q) * 64 + cb * 32 + 8 * pp;
        kt = trfrag(a, a + 256);
        if (r >= 28) kt = gwf;
      } else {
        u32x4 wp[2]; load_wphi(wp, L_WPHI, lane);
        const u32 a = scr + ((lane & 3) + ((lane >> 1) & 4) + ((lane << 1) & 8)) * 64 + h * 8;
        f32x16 t;
#pragma unroll
        for (int e = 0; e < 16; ++e) t[e] = 0.f;
#pragma unroll
        for (int s = 0; s < 2; ++s) {
          const u32x2 lo = *(LAS u32x2*)(size_t)(a + s * 32), hi = *(LAS u32x2*)(size_t)(a + s * 32 + 16);
          t = mfma(u32x4{lo.x, lo.y, hi.x, hi.y}, wp[s], t);
        }
#pragma unroll
        for (int e = 0; e < 8; ++e) t[e] *= LOG2E;
        kt = pack8(t, 0);
      }
      if (m == 15) kt = ones;
      stg_sc1(kt, m != 14 ? l16 : OOB, ws, m == 15 ? WS_KVF + (kvhead * 4 + w) * 1024 : WS_KT + (kvhead * 56 + km) * 1024);
      fvo = lane == 0 ? WS_FK + (kvhead * 64 + km) * 8 : OOB;
    }
    const u64 tag = (((u64)rfl(ev.y) << 32) | rfl(ev.x)) + 1;
    u32x4 xf[28];
    f32x4 acc[8][2];
    u32 done = 0;
    set_m0(zm);
    unit_gemm<isH, true>(xf, acc, lane, w, z2, [&](auto gc) {
      constexpr int s = decltype(gc)::v >> 4, n = decltype(gc)::v & 15;
      if constexpr (n == 15) z_post<s>(oob, ws);
      if constexpr (s <= 2 && (n == 3 || n == 9)) flag_try<s == 2 && n == 9, s>(done, ws, fvo, tag);
      if constexpr (s == 3 && n == 11) touch(sink, tvo2, st2);
    });
    {
      const u32x4 ones = {~0u, ~0u, ~0u, ~0u};
      stg_sc1(ones, l16, ws, WS_O + (rt32 * 32 + kvhead * 2) * 1024);
      stg_sc1(ones, l16, ws, WS_O + (rt32 * 32 + kvhead * 2 + 1) * 1024);
    }
    if (isH) epi_h(acc, ws, WS_H + (rt32 * 112 + j * 4) * 1024, lane, [](int) {});
    u32x4 qa[8];
    if (!isH) {
#pragma unroll
      for (int c = 0; c < 8; ++c) { qa[c] = frag16(acc[c][0], acc[c][1]); OPAQUE(qa[c]); }
    }
    __builtin_amdgcn_s_setprio(isH ? 1 : 3);
    done = 0;
    fvo = lane >= 16 ? OOB : (isH ? WS_FH + (rt32 * 16 + j) * 128 : WS_FQ + (rt32 * 2 + half) * 128) + lane * 8;
    u64 fk = 0;
    const u32 oh = w * 4 + ((lane >> 3) & 3), ort = rt64 * 2 + h;
    const u32 fka = !isH ? WS_FK + (m * 64 + lane) * 8 : (lane & 4) ? WS_FQ + (rt32 * 2 + half) * 128
                  : (oh & 7) == 7 ? WS_FQ + (ort * 2 + (oh >> 3)) * 128 : WS_FH + (ort * 16 + oh - (oh >> 3)) * 128;
    unit_gemm<isH, false>(xf, acc, lane, w, z2, [&](auto gc) {
      constexpr int s = decltype(gc)::v >> 4, n = decltype(gc)::v & 15;
      if constexpr (isH && s <= 3 && (n == 3 || n == 9)) flag_try<s == 3 && n == 9, 0>(done, ws, fvo, tag);
      if constexpr (s == 4 && n == 15) lds_post<C_PROG>(y);
      if constexpr (isH && s == 6 && n == 2) { __builtin_amdgcn_s_setprio(3); flag_peek(fk, ws, fka); __builtin_amdgcn_s_setprio(1); }
      if constexpr (!isH && s == 0 && n == 15) z_post<7>(oob, ws);
      if constexpr (!isH && s == 0 && n == 1) flag_peek(fk, ws, fka);
      if constexpr (!isH && s == 2 && n == 1) flag_check<1>(fk, ws, fka, tag);
    });
    OPAQUE(sink);
    lds_post<C_PROG>(y);
    __builtin_amdgcn_s_setprio(3);
    u32x4 bo[8][2], own[2] = {};
    if (!isH) { const u32x4 b4 = frag16(acc[4][0], acc[4][1]), b5 = frag16(acc[5][0], acc[5][1]); own[0] = half ? qa[6] : b4; own[1] = half ? qa[7] : b5; }
    task_o<isH>(kvhead, rt32, w, ws, tag, lane, y, fk, fka, fvo + (isH ? 64 * 16 * 128 : 0), own[0], own[1], [&](auto hook) {
      if (isH) epi_h(acc, ws, WS_H + (rt32 * 112 + (14 + j) * 4) * 1024, lane, hook);
      else {
#pragma unroll
        for (int c = 0; c < 8; ++c) stg_sc1(qa[c], l16, ws, WS_Q + (rt32 * 28 + half * 20 + c) * 1024);
#pragma unroll
        for (int c = 0; c < 6; ++c) stg_sc1(frag16(acc[c][0], acc[c][1]), l16, ws, WS_Q + (rt32 * 28 + 8 + c + half * (c < 4 ? 8 : 2)) * 1024);
      }
    }, [&]() { if (!isH) reduce_kv(m, half * 4 + w, ws, lane); });
    if (isH) {
      f32x16 oc[2][2];
#pragma unroll
      for (int i = 0; i < 4; ++i)
#pragma unroll
        for (int e = 0; e < 16; ++e) oc[i >> 1][i & 1][e] = 0.f;
      asm volatile("" : "+v"(oc[0][0]), "+v"(oc[0][1]), "+v"(oc[1][0]), "+v"(oc[1][1]));
      u32x4 ao[16];
      const u32 l16o = l16 + 32 * 1024, oso = WS_O + (rt64 * 2 * 32 + w * 8) * 1024;
#pragma unroll
      for (int i = 0; i < 16; ++i) asm volatile("" : "=v"(ao[i]));
      __builtin_amdgcn_s_sleep(26);
      u32 miss = 0xff, go = 0xff, next = 0;
      auto fetch = [&]() {
        sfor<8>([&](auto ic) {
          constexpr int i = decltype(ic)::v, hh = i >> 1, t = i & 1;
          ldg2_if<i, (hh & 1) * 2048>(ao[4 * hh + t], ao[4 * hh + 2 + t], go, t ? l16o : l16, ws, oso + (hh >> 1) * 4096);
        });
      };
      auto consume = [&](auto hc) {
        constexpr int hh = decltype(hc)::v;
        if ((go >> (2 * hh)) & 3) {
#pragma unroll
          for (int t = 0; t < 2; ++t) {
            const u32x4 two[2] = {ao[4 * hh + t], ao[4 * hh + 2 + t]};
            if (arrived(two)) miss &= ~(go & (1u << (2 * hh + t)));
          }
        }
        if (next == hh && !((miss >> (2 * hh)) & 3)) {
#pragma unroll
          for (int u = 0; u < 8; ++u) oc[(u >> 1) & 1][u & 1] = mfma(bo[2 * hh + (u >> 2)][(u >> 1) & 1], ao[4 * hh + (u >> 2) * 2 + (u & 1)], oc[(u >> 1) & 1][u & 1]);
          next = hh + 1;
        }
      };
      fetch();
      z_wait<18>(L_Z + 2048 + w * 512 + 256);
      {
        const BPath bp = make_bpath(L_R1 + w * 2048, lane);
#pragma unroll
        for (int i = 0; i < 16; ++i) bo[i >> 1][i & 1] = trfrag(bp.ra[i & 1] + (i >> 1) * 8192, bp.ra[i & 1] + (i >> 1) * 8192 + 1024);
      }
      sfor<4>([&](auto hc) { constexpr int hh = decltype(hc)::v; landed4<12 - 4 * hh>(ao[4 * hh], ao[4 * hh + 1], ao[4 * hh + 2], ao[4 * hh + 3]); consume(hc); });
      go = __builtin_popcount(miss) <= 4 ? miss : 0;
      const u32 wv = ((lane >> 3) & 1) * 32768 + (lane >> 4) * 2048 + 1024 + (((lane >> 3) + w) & 7) * 128;
      for (int it = 0; it < SPINS && next < 4; ++it) {
        u32 v;
        fetch();
        asm volatile("buffer_load_dword %0, %1, %2, %3 offen sc1" : "=v"(v) : "v"((miss & ~go) ? wv : OOB), "s"(ws), "s"(oso) : "memory");
        asm volatile("s_waitcnt vmcnt(0)" : "+v"(ao[0]), "+v"(ao[1]), "+v"(ao[2]), "+v"(ao[3]), "+v"(ao[4]), "+v"(ao[5]), "+v"(ao[6]), "+v"(ao[7]), "+v"(ao[8]), "+v"(ao[9]),
                     "+v"(ao[10]), "+v"(ao[11]), "+v"(ao[12]), "+v"(ao[13]), "+v"(ao[14]), "+v"(ao[15]), "+v"(v)::"memory");
        const u64 em = __builtin_amdgcn_ballot_w64(v == 0xffffffffu) & 0x0101010101010101ull;
        const u32 empty = (u32)((em * 0x0102040810204080ull) >> 56);
        sfor<4>(consume);
        go = __builtin_popcount(miss) <= 4 ? miss : miss & ~empty;
      }
#pragma unroll
      for (int t = 0; t < 2; ++t) *(LAS float*)(size_t)(h ? L_GW + (t * 32 + r) * 4 : L_DUM + r * 4) = u2f(ao[2 + t].z << 16) + u2f(ao[2 + t].z & 0xffff0000u);
      dump_tile(oc, L_R1 + w * 2048, lane);
      flag_set(ws, (b | w | lane) ? OOB : WS_E, tag);
    }
  } else {
    using T = TB<isH>;
    const srd_t sx = mksrd(p.x, S * D * 2);
    constexpr u32 wrs = isH ? 3584 : 2688;
    u32 xvo[4], wvo[4];
#pragma unroll
    for (int i = 0; i < 4; ++i) {
      xvo[i] = (lane >> 3) * 896 + (((lane & 7) ^ ((4 * i + (lane >> 4)) & 7)) * 16) + i * (8 * 896 - 1024);
      wvo[i] = (lane >> 3) * wrs + ((((lane >> 1) & 3) ^ (((lane >> 4) & 1) | ((i & 1) << 1))) * 32) + (lane & 1) * 16 + i * (8 * wrs - 1024);
    }
    const srd_t sw = isH ? mksrd(w < 2 ? p.wg : p.wu, D * DF * 2) : sqkv;
    const u32 xso = (m * 128 + w * 32) * 896;
    const u32 wso1 = (w & 1) * 32 * wrs + (isH ? j * 128 : half * 640 + (w >> 1) * 128);
    const u32 wso2 = (w & 1) * 32 * wrs + (isH ? (14 + j) * 128 : half ? 512 - (w >> 1) * 64 : 256 + (w >> 1) * 128);
    const u32 mq = w * 4096, mq2 = w * 2048;
    const u32 kso4 = WS_KT + kvhead * 56 * 1024 + mq, kso2 = WS_KT + kvhead * 56 * 1024 + mq2;
    const u32 l16h = l16 + 112 * 1024;
    const u32 hso = WS_H + (rt64 * 2 * 112 + w * 14) * 1024, bso = w * 14 * 16 * 896 + ct2 * 128;
    const srd_t swd = mksrd(p.wd, D * DF * 2);
    const u32 bvo = brow_off(lane);
    auto fadr = [&](int k) -> u32 {
      return k == 0 ? WS_FK + (kvhead * 64 + lane) * 8 : WS_FH + (((k - 2) * 64 + rt64 * 2 + h) * 16 + ((w * 14) >> 2) + ((lane >> 3) & 3)) * 128;
    };
    u32 so = 0, pre = 0, fa = 0;
    u64 fv = 0, tag = 0;
    u32x4 xf[28], af[28][2], bf[19][2], kvs;
    const srd_t swo = mksrd(p.wo, D * D * 2);
    const u32 bvo1 = bvo + 8 * 896 - 1024, bvop = lane < 32 ? bvo1 : OOB;
    const u32 xb = w * 4096 + r * 128 + ((h ^ ((r >> 1) & 7)) * 16);
    constexpr u32 L_PHI = slot_of(isH, OK, 1) * 16384, L_VF = slot_of(isH, OK, 2) * 16384;
    struct { u32 pw[4], pr[2], vw, vr, lr, lw, so; } ea;
    float keep;
    auto op = [&](auto kc) {
      constexpr int K = decltype(kc)::v;
      constexpr Op o = T::t.v[K];
      if constexpr (T::need(K) >= 0) z_wait_pre<T::need(K)>(z1, pre);
      if constexpr (isH && o.kind == OU && o.idx == 6 && o.i == 0) z_wait<zidx(isH, OU, 3)>(z2);
      constexpr int chk = o.i != 0 ? -1 : (o.kind == OK && o.idx == 0) ? 0 : (o.kind == OA && o.idx == 0) ? 2
                        : (o.kind == OA && o.idx == 14) ? 3 : -1;
      if constexpr (chk >= 0) { flag_check<K - 1 - T::find(OPEEK, chk, 0)>(fv, ws, fa, tag); }
      if constexpr (o.kind <= OK) {
        constexpr u32 sb = slot_of(isH, o.kind, o.idx) * 16384;
        if constexpr (o.i == 0) {
          if constexpr (o.kind == OX) so = dma_open<sb, o.idx * 128>(mq, xso);
          else if constexpr (o.kind == OK) so = dma_open<sb, o.idx * 16384>(o.idx < 3 ? mq : mq2, o.idx < 3 ? kso4 : kso2);
          else so = dma_open<sb, o.idx * 64 * wrs>(mq, o.kind == OW ? wso1 : wso2);
          if constexpr (!isH && o.kind == OU && o.idx == 0) {
#pragma unroll
            for (int i = 0; i < 4; ++i) if (w >= 2 && (((lane >> 2) ^ i) & 1)) wvo[i] = OOB;
          }
        }
        if constexpr (o.kind == OK) dma_off_sc1<o.i * 1024>(l16, ws, so);
        else dma_off<o.i * 1024>(o.kind == OX ? xvo[o.i] : wvo[o.i], o.kind == OX ? sx : sw, so);
      } else if constexpr (o.kind == OZ) { set_m0(zm); z_post<o.idx>(oob, ws); }
      else if constexpr (o.kind == OPEEK) { fa = fadr(o.idx); flag_peek(fv, ws, fa); }
      else if constexpr (o.kind == OB) {
        if constexpr (o.idx < 19) ldg(bf[o.idx][o.i], bvo, swd, bso + ((o.idx >= 14 ? o.idx + 42 : o.idx) * 16 + o.i * 8) * 896);
        else {
          if constexpr (o.idx == 31 && o.i == 0) lds_wait<C_SYNC, 3>(y);
          if constexpr (o.idx == 35 && o.i == 0) lds_wait<C_PROG, 1>(y);
          if constexpr (o.idx == 28 && o.i == 0) lds_wait<C_PROG, 2>(y);
          if constexpr (o.i == 0) {
            if constexpr (o.idx < 28) so = dma_open<L_R2 + (o.idx - 19) * 8192, (o.idx + 42) * 16 * 896>(mq2, bso);
            else so = dma_open<L_R1 + (o.idx - 28) * 8192, (((o.idx - 28) >> 1) * 28 + (o.idx & 1) * 16) * 896>(mq2, w * 112 * 896 + ct2 * 128);
          }
          dma_off<o.i * 1024>(o.i == 0 ? bvo : (o.idx >= 28 && (o.idx & 1)) ? bvop : bvo1, o.idx < 28 ? swd : swo, so);
        }
      }
      else if constexpr (o.kind == OA) {
        constexpr int ks = o.idx >= 14 ? o.idx + 42 : o.idx;
        ldg_sc1o<(ks & 3) * 1024>(af[o.idx][o.i], o.i ? l16h : l16, ws, hso + (ks >> 2) * 4096);
      } else if constexpr (o.kind == OST) stg_sc1(not_ones(kvs), ea.so, ws, WS_KVP + ((kvhead * 16 + m) * 4 + w) * 1024);
      if constexpr (T::need(K + 3) >= 0) pre = z_peek<T::need(K + 3)>(z1);
      constexpr int J = K - LAG;
      if constexpr (J >= 0) {
        constexpr Op oj = T::t.v[J];
        if constexpr (oj.kind == OX && oj.i == 3) {
          if constexpr (J == 3) {
            asm volatile("s_waitcnt vmcnt(%1)" : "+v"(ev) : "n"(LAG) : "memory");
            tag = (((u64)rfl(ev.y) << 32) | rfl(ev.x)) + 1;
          } else VMWAIT(LAG);
#pragma unroll
          for (int kk = 0; kk < 4; ++kk) xf[4 * oj.idx + kk] = lds128(slot_of(isH, OX, oj.idx) * 16384 + (xb ^ (kk * 32)));
        }
      }
      SB();
    };
    auto ops = [&](auto ac, auto bc) { sfor<decltype(bc)::v - decltype(ac)::v>([&](auto kc) { op(IC<decltype(ac)::v + decltype(kc)::v>{}); }); };
#define OPS(a, b) ops(IC<(a)>{}, IC<(b)>{})
#define MK(m) (T::t.mark[m])
#define TICK(M, n, N) OPS(MK(M) + (MK((M) + 1) - MK(M)) * (n) / (N), MK(M) + (MK((M) + 1) - MK(M)) * ((n) + 1) / (N))
#define ETICK(n) OPS(T::etick(n), T::etick((n) + 1))
    f32x16 ka, va;
#pragma unroll
    for (int e = 0; e < 16; ++e) { ka[e] = 0.f; va[e] = r == 28 ? 1.f : 0.f; }
    __builtin_amdgcn_s_setprio(2);
    OPS(0, 36);
    {
      const u32 t0 = 4 * cb + q, ra = L_PHI + (w >> 1) * 2048 + (16 * h + t0) * 64;
#pragma unroll
      for (int g4 = 0; g4 < 4; ++g4) { ea.pw[g4] = L_PHI + w * 4096 + r * 64 + (((2 * g4 + h) ^ ((r >> 1) & 7)) * 8); OPAQUE(ea.pw[g4]); }
      ea.pr[0] = ra + (((4 * (w & 1) + pp) ^ (t0 >> 1)) * 8); ea.pr[1] = ra + 512 + (((4 * (w & 1) + pp) ^ ((t0 >> 1) + 4)) * 8);
      ea.vw = L_VF + w * 2048 + l16; ea.vr = L_VF + h * 1024 + ((lane & 15) + 32 * cb) * 16;
      ea.lr = L_VF + w * 2048 + (r >> 4) * 1024 + (30 + 32 * ((r >> 2) & 1)) * 16 + (((r >> 3) & 1) * 4 + (r & 3)) * 2;
      ea.lw = L_GW + 256 + (w * 32 + r) * 4; ea.so = (h * 16 + (lane & 15) + 32 * cb) * 16;
      keep = h ? 0.f : 1.f;
      OPAQUE(ea.pr[0]); OPAQUE(ea.pr[1]); OPAQUE(ea.vw); OPAQUE(ea.vr); OPAQUE(ea.lr); OPAQUE(ea.lw); OPAQUE(ea.so); OPAQUE(keep);
      asm volatile("" : "+v"(va));
    }
    OPS(36, MK(M_KV0));
    {
      u32 ak[4];
#pragma unroll
      for (int t = 0; t < 4; ++t) { ak[t] = slot_of(isH, OK, t) * 16384 + l16; OPAQUE(ak[t]); }
      u32x4 fk[3], fw[3];
      u32 pk = 0;
      auto reads = [&](int ks) { fk[ks % 3] = lds128(ak[ks >> 3] + (ks & 7) * 2048); fw[ks % 3] = lds128(ak[ks >> 3] + (ks & 7) * 2048 + 1024); };
      z_wait<zidx(isH, OK, 0)>(z2);
      reads(0); reads(1); reads(2);
      SB();
      sfor<28>([&](auto kc) {
        constexpr int ks = decltype(kc)::v, t = ks >> 3;
        ka = mfma(fk[ks % 3], xf[ks], ka);
        SB();
        asm volatile("" : "+v"(ka));
        TICK(M_KV0, ks, 28);
        va = mfma(xf[ks], fw[ks % 3], va);
        SB();
        asm volatile("" : "+v"(va));
        if constexpr ((ks & 7) == 3 && t < 3) pk = z_peek<zidx(isH, OK, t + 1)>(z2);
        if constexpr ((ks & 7) == 5) z_wait_pre<zidx(isH, OK, t + 1)>(z2, pk);
        if constexpr (ks + 3 < 28) reads(ks + 3);
        SB();
      });
    }
    {
      ETICK(0);
      float mx = max3(max3(max3(ka[0], ka[1], ka[2]), max3(ka[3], ka[4], ka[5]), max3(ka[6], ka[7], ka[8])),
                      max3(max3(ka[9], ka[10], ka[11]), max3(ka[12], ka[13], ka[14]), ka[15]), ka[15]);
      asm volatile("" : "+v"(mx));
      ETICK(1);
      float mn = min3(min3(min3(ka[0], ka[1], ka[2]), min3(ka[3], ka[4], ka[5]), min3(ka[6], ka[7], ka[8])),
                      min3(min3(ka[9], ka[10], ka[11]), min3(ka[12], ka[13], ka[14]), ka[15]), ka[15]);
      asm volatile("" : "+v"(mn));
      ETICK(2);
      { float a, c; both_halves(mx, a, c); mx = vmax(a, c); both_halves(mn, a, c); mn = vmin(a, c); }
      asm volatile("" : "+v"(mx), "+v"(mn));
      ETICK(3);
      u32x4 vf[2] = {pack8(va, 0), pack8(va, 1)}, vv[4][2];
      asm volatile("" : "+v"(vf[0]), "+v"(vf[1]));
      ETICK(4);
      u32 lg = 0;
      sfor<2>([&](auto sc) {
        constexpr int sg = decltype(sc)::v, E0 = 5 + 10 * sg;
        f32x16 e;
        float ps[4];
        sfor<4>([&](auto gc) {
          constexpr int g4 = decltype(gc)::v;
#pragma unroll
          for (int i = 4 * g4; i < 4 * g4 + 4; ++i) e[i] = __builtin_amdgcn_exp2f(sg == 0 ? ka[i] - mx : mn - ka[i]);
          if constexpr (g4 > 0) { ps[g4 - 1] = (e[4 * g4 - 4] + e[4 * g4 - 3]) + (e[4 * g4 - 2] + e[4 * g4 - 1]); asm volatile("" : "+v"(ps[g4 - 1])); }
          float t = e[4 * g4 + 3]; asm volatile("" : "+v"(t)); e[4 * g4 + 3] = t;
          if constexpr (g4 == 2) pre = sg == 0 ? z_peek<(isH ? zidx(isH, OU, 6) : 18)>(z2) : lds_peek<C_SYNC>(y);
          ETICK(E0 + g4);
        });
        ps[3] = (e[12] + e[13]) + (e[14] + e[15]);
        float a, c; both_halves(__builtin_fmaf(ps[3], keep, ps[2]) + (ps[0] + ps[1]), a, c);
        float rs = __builtin_amdgcn_rcpf(a + c);
        asm volatile("" : "+v"(rs));
        ETICK(E0 + 4);
        if constexpr (sg == 0) {
          z_wait_pre<(isH ? zidx(isH, OU, 6) : 18)>(z2, pre);
          sts128(ea.vw, vf[0]); sts128(ea.vw + 1024, vf[1]);
          lg = *(LAS unsigned short*)(size_t)ea.lr;
        } else {
          lds_wait_pre<C_SYNC, 1>(y.ra, pre);
#pragma unroll
          for (int i = 0; i < 8; ++i) vv[i >> 1][i & 1] = lds128(ea.vr + (i >> 1) * 2048 + (i & 1) * 256);
        }
        SB();
        ETICK(E0 + 5);
        sfor<4>([&](auto gc) {
          constexpr int g4 = decltype(gc)::v;
          sts64(ea.pw[g4] + sg * 2048, u32x2{cvtpk(e[4 * g4] * rs, e[4 * g4 + 1] * rs), cvtpk(e[4 * g4 + 2] * rs, e[4 * g4 + 3] * rs)});
          ETICK(E0 + 6 + g4);
        });
        if constexpr (sg == 0) *(LAS u32*)(size_t)ea.lw = lg << 16;
        lds_post<C_SYNC>(y);
      });
      pre = lds_peek<C_SYNC>(y);
      ETICK(25);
      lds_wait_pre<C_SYNC, 2>(y.ra, pre);
      u32x4 pf[4];
#pragma unroll
      for (int u = 0; u < 4; ++u) pf[u] = trfrag(ea.pr[0] + u * 4096, ea.pr[1] + u * 4096);
      lds_post<C_SYNC>(y);
      SB();
      ETICK(26);
      f32x4 kp[2] = {{0.f, 0.f, 0.f, 0.f}, {0.f, 0.f, 0.f, 0.f}};
#pragma unroll
      for (int i = 0; i < 8; ++i)
        kp[i & 1] = __builtin_amdgcn_mfma_f32_16x16x32_bf16(__builtin_bit_cast(bf16x8, pf[i >> 1]), __builtin_bit_cast(bf16x8, vv[i >> 1][i & 1]), kp[i & 1], 0, 0, 0);
      asm volatile("" : "+v"(kp[0]), "+v"(kp[1]));
      ETICK(27);
      const auto sx0 = __builtin_amdgcn_permlane32_swap(cvtpk(kp[0].x, kp[0].y), cvtpk(kp[1].x, kp[1].y), false, false);
      const auto sx1 = __builtin_amdgcn_permlane32_swap(cvtpk(kp[0].z, kp[0].w), cvtpk(kp[1].z, kp[1].w), false, false);
      kvs = u32x4{sx0[0], sx1[0], sx0[1], sx1[1]};
      ETICK(28);
      OPS(MK(M_ST), MK(M_ST) + 1);
    }
    if constexpr (!isH) {
    } else {
      f32x16 acc[2][2];
#pragma unroll
      for (int i = 0; i < 4; ++i)
#pragma unroll
        for (int e = 0; e < 16; ++e) acc[i >> 1][i & 1][e] = 0.f;
      BPath bp = make_bpath(L_K3 + w * 2048, lane);
      u32x4 tf[2][2];
      asm volatile("s_waitcnt vmcnt(%2)" : "+v"(bf[0][0]), "+v"(bf[0][1]) : "n"(MK(M_ST) - T::find(OB, 0, 1)) : "memory");
      sts128(bp.wa, bf[0][0]); sts128(bp.wa + 1024, bf[0][1]);
      tf[0][0] = trfrag(bp.ra[0], bp.ra[0] + 1024); tf[0][1] = trfrag(bp.ra[1], bp.ra[1] + 1024);
      OPS(MK(M_ST) + 1, MK(M_JOB));
      __builtin_amdgcn_s_setprio(1);
      u32x4 lf[14][2];
      u32 lr[2][2];
      sfor<28>([&](auto nc) {
        constexpr int n = decltype(nc)::v, P = MK(M_JOB + n), NL = MK(M_JOB + n + 1) - P, nb = n < 13 ? n + 1 : 13;
        constexpr int k0 = n < CV0 ? 0 : n > 13 ? 9 : (n - CV0) * 9 / (14 - CV0), k1 = n < CV0 ? 0 : n > 13 ? 9 : (n + 1 - CV0) * 9 / (14 - CV0);
        constexpr int need = cmax(cmax(T::find(OA, n, 1), T::find(OB, nb, 1)), k1 > k0 ? T::find(OB, 18 + k1, 1) : 0);
        static_assert(need < P, "stream order");
        if constexpr (n == CV0) {
#pragma unroll
          for (int i = 0; i < 4; ++i) { lr[i >> 1][i & 1] = bp.ra[i & 1] + (L_R2 - L_K3) + (i >> 1) * 4 * 8192; pin1(lr[i >> 1][i & 1]); }
        }
        if constexpr (n == 14) {
          static_assert(T::find(OB, 18, 1) < T::find(OA, 14, 0) && T::find(OB, 27, 1) < T::find(OB, 18, 1), "stream order");
          VMWAIT(P - 1 - T::find(OB, 18, 1));
#pragma unroll
          for (int k = 0; k < 5; ++k) {
            pin2(bf[14 + k][0], bf[14 + k][1]);
            sts128(bp.wa, bf[14 + k][0]); sts128(bp.wa + 1024, bf[14 + k][1]);
            lf[k][0] = trfrag(bp.ra[0], bp.ra[0] + 1024); lf[k][1] = trfrag(bp.ra[1], bp.ra[1] + 1024);
          }
          SB();
        }
        if constexpr (n < 13) landed4<P - 1 - need>(af[n][0], af[n][1], bf[nb][0], bf[nb][1]);
        else { VMWAIT(P - 1 - need); pin2(af[n][0], af[n][1]); }
        sfor<4>([&](auto uc) {
          constexpr int u = decltype(uc)::v, c = u >> 1, t = u & 1;
          if constexpr (n < 14) acc[c][t] = mfma(tf[n & 1][c], af[n][t], acc[c][t]); else acc[c][t] = mfma(lf[n - 14][c], af[n][t], acc[c][t]);
          SB();
          asm volatile("" : "+v"(acc[c][t]));
          if constexpr (n < 13) {
            if constexpr (u == 0) {
              sts128(bp.wa, bf[nb][0]); sts128(bp.wa + 1024, bf[nb][1]);
              tf[nb & 1][0] = trfrag(bp.ra[0], bp.ra[0] + 1024); tf[nb & 1][1] = trfrag(bp.ra[1], bp.ra[1] + 1024);
            }
            SB();
          }
          if constexpr (u >= 1 && k0 + u - 1 < k1) {
            constexpr int k = k0 + u - 1;
#pragma unroll
            for (int i = 0; i < 2; ++i) lf[5 + k][i] = trfrag(lr[k >> 2 ? 1 : 0][i] + (k < 4 ? k : k - 4) * 8192, lr[k >> 2 ? 1 : 0][i] + (k < 4 ? k : k - 4) * 8192 + 1024);
            SB();
          }
          OPS(P + NL * u / 4, P + NL * (u + 1) / 4);
        });
      });
      dump_tile(acc, L_R2 + w * 2048, lane);
    }
  }
  if (isH) {
    const int tok = tid >> 3, pc = tid & 7;
    const u32 a = (tok >> 3) * 8192 + (tok & 7) * 256 + (((pc * 2) ^ (tok & 7)) * 16);
    f32x4 v[8][2];
#pragma unroll
    for (int u = 0; u < 8; ++u)
#pragma unroll
      for (int i = 0; i < 2; ++i) v[u][i] = __builtin_bit_cast(f32x4, lds128((u < 4 ? L_R2 + u * 2048 : L_R1 + (u - 4) * 2048) + (a ^ (i * 16))));
    const float omg = *(LAS float*)(size_t)(L_GW + tok * 4);
    const f32x4 s0 = ((v[0][0] + v[1][0]) + (v[2][0] + v[3][0])) * omg + ((v[4][0] + v[5][0]) + (v[6][0] + v[7][0]));
    const f32x4 s1 = ((v[0][1] + v[1][1]) + (v[2][1] + v[3][1])) * omg + ((v[4][1] + v[5][1]) + (v[6][1] + v[7][1]));
    const u32x4 res = {cvtpk(s0.x, s0.y), cvtpk(s0.z, s0.w), cvtpk(s1.x, s1.y), cvtpk(s1.z, s1.w)};
    *(u32x4*)(p.out + (rt64 * 64 + tok) * 896 + ct2 * 128 + pc * 16) = res;
  }
}

__global__ __launch_bounds__(512, 2) void fused(Params p) {
  asm volatile(".subsection 2\n.Lhk_text_end:\n\t.fill 256, 4, 0xBF800000\n\t.subsection 0");
  const int wave = rfl(threadIdx.x >> 6);
  if (wave == 1) {
    u64 pc; u32 len;
    asm volatile("s_getpc_b64 %0\n.Lpc%=:\n\ts_mov_b32 %1, .Lhk_text_end-.Lpc%=" : "=s"(pc), "=s"(len));
    const u32 off = ((threadIdx.x & 63) * 32 + (blockIdx.x >> 3)) * 128;
    asm volatile("s_mov_b32 m0, %2\n\ts_nop 0\n\tglobal_load_lds_dword %0, %1" ::"v"(off < len ? off : 0u), "s"(pc), "s"(L_DUM) : "memory");
  }
  *(LAS u32*)(size_t)(L_Z + (threadIdx.x >> 6) * 512 + 256 + (threadIdx.x & 63) * 4) = 1u;
  *(LAS u32*)(size_t)(L_CNT + (threadIdx.x & 15) * 4) = 0u;
  asm volatile("s_waitcnt lgkmcnt(0)\n\ts_barrier" ::: "memory");
  if (!((blockIdx.x & 1) && blockIdx.x >= 192)) {
    if (wave < 4) body<true, true>(p, wave); else body<false, true>(p, wave);
  } else {
    if (wave < 4) body<true, false>(p, wave); else body<false, false>(p, wave);
  }
}
}
#if defined(__HIP_DEVICE_COMPILE__)
#pragma clang attribute pop
#endif

extern "C" void kernel_launch(void* const* d_in, const int* in_sizes, int n_in, void* d_out, int out_size, void* d_ws, size_t ws_size,
                              hipStream_t stream) {
  hk::Params p;
  p.x = (const char*)d_in[0]; p.wqkv = (const char*)d_in[1]; p.wphi = (const char*)d_in[2]; p.wo = (const char*)d_in[3];
  p.wg = (const char*)d_in[5]; p.wu = (const char*)d_in[6]; p.wd = (const char*)d_in[7]; p.gw = (const char*)d_in[8];
  p.out = (char*)d_out; p.ws = (char*)d_ws;
  (void)hipFuncSetAttribute((const void*)hk::fused, hipFuncAttributeMaxDynamicSharedMemorySize, hk::L_TOTAL);
  hk::fused<<<256, 512, hk::L_TOTAL, stream>>>(p);
}
```

```cpp
#include <hip/hip_runtime.h>
#include <stdint.h>

#if defined(__HIP_DEVICE_COMPILE__)
#pragma clang attribute push(__attribute__((target("no-packed-fp32-ops"))), apply_to = function)
#endif
namespace hk {
typedef unsigned u32;
typedef unsigned long long u64;
typedef __bf16 bf16x8 __attribute__((ext_vector_type(8)));
typedef __bf16 bf16x2 __attribute__((ext_vector_type(2)));
typedef short s16x4 __attribute__((ext_vector_type(4)));
typedef u32 u32x4 __attribute__((ext_vector_type(4)));
typedef u32 u32x2 __attribute__((ext_vector_type(2)));
typedef float f32x16 __attribute__((ext_vector_type(16)));
typedef float f32x4 __attribute__((ext_vector_type(4)));
typedef float f32x2 __attribute__((ext_vector_type(2)));
typedef __amdgpu_buffer_rsrc_t srd_t;
#define LAS __attribute__((address_space(3)))
#define DI __device__ __forceinline__

constexpr int S = 2048, D = 448, DF = 1792;
constexpr u32 WS_E = 0;
constexpr u32 WS_FK = 4096;
constexpr u32 WS_FQ = 131072;
constexpr u32 WS_FH = 393216;
constexpr u32 WS_KVF = 655360;
constexpr u32 WS_KVP = 720896;
constexpr u32 WS_O = WS_KVP + 16 * 16 * 4096;
constexpr u32 WS_Q = WS_O + 64 * 32 * 1024;
constexpr u32 WS_H = WS_Q + 64 * 32 * 1024;
constexpr u32 WS_KT = WS_H + 64 * 112 * 1024;
constexpr u32 WS_END = WS_KT + 16 * 56 * 1024;
constexpr u32 OOB = 0x7ff00000u;
constexpr u32 L_K3 = 9 * 16384;
constexpr u32 L_WPHI = L_K3 + 8192;
constexpr u32 L_GW = L_WPHI + 2048;
constexpr u32 L_Z = L_GW + 1024;
constexpr u32 L_CNT = L_Z + 4096;
constexpr u32 L_DUM = L_CNT + 64;
constexpr u32 L_TOTAL = 163840;
enum { C_PROG = 0, C_SYNC = 16, C_KVF = 32 };
constexpr u32 L_KVF = 143360;

struct Params {
  const char *x, *wqkv, *wphi, *wo, *wg, *wu, *wd, *gw;
  char *out, *ws;
};

DI u32 f2u(float f) { return __builtin_bit_cast(u32, f); }
DI float u2f(u32 u) { return __builtin_bit_cast(float, u); }
DI u32 min(u32 a, u32 b) { return a < b ? a : b; }
DI u32 max(u32 a, u32 b) { return a > b ? a : b; }
DI u32 rfl(u32 v) { return __builtin_amdgcn_readfirstlane(v); }
DI srd_t mksrd(const void* p, u32 bytes) { return __builtin_amdgcn_make_buffer_rsrc((void*)p, 0, bytes, 0x00020000); }
DI u32 cvtpk(float lo, float hi) { f32x2 v = {lo, hi}; return __builtin_bit_cast(u32, __builtin_convertvector(v, bf16x2)); }
DI f32x16 mfma(u32x4 a, u32x4 b, f32x16 c) {
  return __builtin_amdgcn_mfma_f32_32x32x16_bf16(__builtin_bit_cast(bf16x8, a), __builtin_bit_cast(bf16x8, b), c, 0, 0, 0);
}
DI u32x2 tr64(u32 addr) { return __builtin_bit_cast(u32x2, __builtin_amdgcn_ds_read_tr16_b64_v4i16((LAS s16x4*)(size_t)addr)); }
DI u32x4 trfrag(u32 a0, u32 a1) { u32x2 lo = tr64(a0), hi = tr64(a1); return u32x4{lo.x, lo.y, hi.x, hi.y}; }
DI u32x4 lds128(u32 addr) { return *(LAS u32x4*)(size_t)addr; }
DI void sts128(u32 addr, u32x4 v) { *(LAS u32x4*)(size_t)addr = v; }
DI void sts64(u32 addr, u32x2 v) { *(LAS u32x2*)(size_t)addr = v; }
#define VMWAIT(n) asm volatile("s_waitcnt vmcnt(%0)" ::"n"(n) : "memory")
DI void dma_buf(u32 ldsaddr, u32 voff, srd_t srd, u32 soff) {
  asm volatile("s_mov_b32 m0, %0\n\ts_nop 0\n\tbuffer_load_dwordx4 %1, %2, %3 offen lds" ::"s"(ldsaddr), "v"(voff), "s"(srd), "s"(soff) : "memory");
}
template <u32 IMM, u32 SIMM> DI u32 dma_open(u32 base, u32 sbase) {
  u32 so;
  asm volatile("s_add_u32 m0, %1, %2\n\ts_add_u32 %0, %3, %4" : "=s"(so) : "s"(base), "n"(IMM), "s"(sbase), "n"(SIMM) : "scc", "memory");
  return so;
}
template <int OFF> DI void dma_off(u32 voff, srd_t srd, u32 soff) {
  asm volatile("buffer_load_dwordx4 %0, %1, %2 offen offset:%3 lds" ::"v"(voff), "s"(srd), "s"(soff), "n"(OFF) : "memory");
}
template <int OFF> DI void dma_off_sc1(u32 voff, srd_t srd, u32 soff) {
  asm volatile("buffer_load_dwordx4 %0, %1, %2 offen offset:%3 sc1 lds" ::"v"(voff), "s"(srd), "s"(soff), "n"(OFF) : "memory");
}
template <int OFF> DI void ldg_sc1o(u32x4& dst, u32 voff, srd_t srd, u32 soff) {
  asm volatile("buffer_load_dwordx4 %0, %1, %2, %3 offen offset:%4 sc1" : "=v"(dst) : "v"(voff), "s"(srd), "s"(soff), "n"(OFF) : "memory");
}
DI void ldg(u32x4& dst, u32 voff, srd_t srd, u32 soff) {
  asm volatile("buffer_load_dwordx4 %0, %1, %2, %3 offen" : "=v"(dst) : "v"(voff), "s"(srd), "s"(soff) : "memory");
}
template <int BIT, int OFF> DI void ldg2_if(u32x4& a, u32x4& b, u32 mask, u32 voff, srd_t srd, u32 soff) {
  asm volatile("s_bitcmp1_b32 %2, %3\n\ts_cbranch_scc0 .Lsk%=\n\tbuffer_load_dwordx4 %0, %4, %5, %6 offen offset:%7 sc1\n\tbuffer_load_dwordx4 %1, %4, %5, %6 offen offset:%8 sc1\n.Lsk%=:"
               : "+v"(a), "+v"(b) : "s"(mask), "n"(BIT), "v"(voff), "s"(srd), "s"(soff), "n"(OFF), "n"(OFF + 1024) : "scc", "memory");
}
template <int N> DI void landed4(u32x4& a, u32x4& b, u32x4& c, u32x4& d) {
  asm volatile("s_waitcnt vmcnt(%4)" : "+v"(a), "+v"(b), "+v"(c), "+v"(d) : "n"(N) : "memory");
}
template <int N> DI void wait3(u32x4& a, u32x4& b, u32x4& c, u64& f) {
  asm volatile("s_waitcnt vmcnt(%4)" : "+v"(a), "+v"(b), "+v"(c), "+v"(f) : "n"(N) : "memory");
}
DI void pin2(u32x4& a, u32x4& b) { asm volatile("" : "+v"(a), "+v"(b)::"memory"); }
DI void pin1(u32& a) { asm volatile("" : "+v"(a)); }
DI void touch(u32& sink, u32 voff, srd_t srd) {
  asm volatile("buffer_load_dword %0, %1, %2, 0 offen" : "+v"(sink) : "v"(voff), "s"(srd) : "memory");
}
DI void ldg_sc1(u32x4& dst, u32 voff, srd_t srd, u32 soff) {
  asm volatile("buffer_load_dwordx4 %0, %1, %2, %3 offen sc1" : "=v"(dst) : "v"(voff), "s"(srd), "s"(soff) : "memory");
}
DI void stg_sc1(u32x4 v, u32 voff, srd_t srd, u32 soff) {
  asm volatile("buffer_store_dwordx4 %0, %1, %2, %3 offen sc1\n\ts_nop 1" ::"v"(v), "v"(voff), "s"(srd), "s"(soff) : "memory");
}
struct Sync { u32 pa, ra, one; };
template <int C> DI void lds_post(const Sync& y) { asm volatile("ds_add_u32 %0, %1 offset:%2" ::"v"(y.pa), "v"(y.one), "n"(C) : "memory"); }
template <int C> DI u32 lds_peek(const Sync& y) { return *(volatile LAS u32*)(size_t)(y.ra + C); }
template <int C, int T> DI void lds_wait_pre(u32 a, u32 v) {
  u32 cnt;
  asm volatile("v_cmp_gt_u32 vcc, %3, %0\n\ts_cbranch_vccnz .Lsl%=\n.Lse%=:\n\t.subsection 1\n"
               ".Lsl%=:\n\ts_mov_b32 %1, 0x800000\n.Lsp%=:\n\tds_read_b32 %0, %2 offset:%4\n\ts_waitcnt lgkmcnt(0)\n\tv_cmp_gt_u32 vcc, %3, %0\n\ts_cbranch_vccz .Lse%=\n\t"
               "s_sub_u32 %1, %1, 1\n\ts_cbranch_scc0 .Lsp%=\n\ts_branch .Lse%=\n\t.subsection 0"
               : "+v"(v), "=&s"(cnt) : "v"(a), "n"(T), "n"(C) : "vcc", "scc", "memory");
}
template <int C, int T> DI void lds_wait(const Sync& y) { lds_wait_pre<C, T>(y.ra, lds_peek<C>(y)); }
template <int K> DI void z_post(u32 oob, srd_t srd) {
  asm volatile("buffer_load_dword %0, %1, 0 offen offset:%2 lds" ::"v"(oob), "s"(srd), "n"(4 * K + 4) : "memory");
}
DI void set_m0(u32 v) { asm volatile("s_mov_b32 m0, %0\n\ts_nop 0" ::"s"(v) : "memory"); }
template <int K> DI u32 z_peek(u32 za) { return *(volatile LAS u32*)(size_t)(za + 4 * K); }
template <int K> DI void z_wait_pre(u32 za, u32 v) {
  u32 cnt;
  asm volatile("v_cmp_ne_u32 vcc, 0, %0\n\ts_cbranch_vccnz .Lsl%=\n.Lse%=:\n\t.subsection 1\n"
               ".Lsl%=:\n\ts_mov_b32 %1, 0x800000\n.Lsp%=:\n\tds_read_b32 %0, %2 offset:%3\n\ts_waitcnt lgkmcnt(0)\n\tv_cmp_ne_u32 vcc, 0, %0\n\ts_cbranch_vccz .Lse%=\n\t"
               "s_sub_u32 %1, %1, 1\n\ts_cbranch_scc0 .Lsp%=\n\ts_branch .Lse%=\n\t.subsection 0"
               : "+v"(v), "=&s"(cnt) : "v"(za), "n"(4 * K) : "vcc", "scc", "memory");
}
template <int K> DI void z_wait(u32 za) { z_wait_pre<K>(za, z_peek<K>(za)); }
DI void flag_peek(u64& v, srd_t ws, u32 voff) {
  asm volatile("buffer_load_dwordx2 %0, %1, %2, 0 offen sc1" : "=v"(v) : "v"(voff), "s"(ws) : "memory");
}
template <int N> DI void flag_check(u64 v, srd_t ws, u32 voff, u64 tag) {
  u32 cnt;
  asm volatile("s_waitcnt vmcnt(%5)\n\t"
               "v_cmp_ne_u64 vcc, %4, %0\n\ts_cbranch_vccnz .Lfl%=\n.Lfe%=:\n\t.subsection 1\n"
               ".Lfl%=:\n\ts_mov_b32 %1, 0x40000\n"
               ".Lfp%=:\n\tbuffer_load_dwordx2 %0, %2, %3, 0 offen sc1\n\ts_waitcnt vmcnt(0)\n\tv_cmp_ne_u64 vcc, %4, %0\n\ts_cbranch_vccz .Lfe%=\n\t"
               "s_sleep 1\n\ts_sub_u32 %1, %1, 1\n\ts_cbranch_scc0 .Lfp%=\n\ts_branch .Lfe%=\n.Lfz%=:\n\t.subsection 0"
               : "+v"(v), "=&s"(cnt) : "v"(voff), "s"(ws), "s"(tag), "n"(N) : "vcc", "scc", "memory");
}
DI void flag_wait(srd_t ws, u32 voff, u64 tag) { u64 v; flag_peek(v, ws, voff); flag_check<0>(v, ws, voff, tag); }
DI void flag_set(srd_t ws, u32 voff, u64 tag) {
  asm volatile("buffer_store_dwordx2 %0, %1, %2, 0 offen sc1" ::"v"(tag), "v"(voff), "s"(ws) : "memory");
}
DI u32x4 not_ones(u32x4 v) { return u32x4{min(v.x, 0xfffffffeu), min(v.y, 0xfffffffeu), min(v.z, 0xfffffffeu), min(v.w, 0xfffffffeu)}; }
template <int N> DI bool arrived(const u32x4 (&v)[N]) {
  u32 m = 0;
#pragma unroll
  for (int i = 0; i < N; ++i) m = max(max(max(m, v[i].x), v[i].y), max(v[i].z, v[i].w));
  return __builtin_amdgcn_ballot_w64(m == 0xffffffffu) == 0;
}
template <bool FORCE, int NZ> DI void flag_try(u32& done, srd_t ws, u32 voff, u64 tag) {
  u32 t;
#define FT_TAIL "s_cbranch_scc0 .Lfp%=\n.Lfe%=:\n\t.subsection 1\n.Lfp%=:\n\ts_waitcnt vmcnt(%5)\n\tbuffer_store_dwordx2 %2, %3, %4, 0 offen sc1\n\t" \
                "s_mov_b32 %0, 64\n\ts_branch .Lfe%=\n.Lfz%=:\n\t.subsection 0"
  if (FORCE) asm volatile("s_cmp_lg_u32 %0, 0\n\t" FT_TAIL : "+s"(done), "=&s"(t) : "v"(tag), "v"(voff), "s"(ws), "n"(NZ) : "scc", "memory");
  else asm volatile("s_getreg_b32 %1, hwreg(HW_REG_IB_STS, 0, 4)\n\ts_or_b32 %1, %1, %0\n\ts_cmp_gt_u32 %1, %5\n\t" FT_TAIL
                    : "+s"(done), "=&s"(t) : "v"(tag), "v"(voff), "s"(ws), "n"(NZ) : "scc", "memory");
}
#define SB() __builtin_amdgcn_sched_barrier(0)
#define OPAQUE(x) asm volatile("" : "+v"(x))
DI float swap_hi(float lo_wants, int h) {
  auto r = __builtin_amdgcn_permlane32_swap(f2u(lo_wants), f2u(lo_wants), false, false);
  (void)h;
  return u2f(r[1]);
}
DI void both_halves(float v, float& lo, float& hi) {
  auto r = __builtin_amdgcn_permlane32_swap(f2u(v), f2u(v), false, false);
  lo = u2f(r[0]); hi = u2f(r[1]);
}
constexpr int SPINS = 200000;
constexpr float LOG2E = 1.4426950408889634f;

template <int I> struct IC { static constexpr int v = I; };
template <int N, class F, int I = 0> DI void sfor(F f) { if constexpr (I < N) { f(IC<I>{}); sfor<N, F, I + 1>(f); } }

enum { OX, OW, OU, OK, OZ, OPEEK, OB, OA, OST };
struct Op { int kind, idx, i; };
enum { M_KV0, M_EPI, M_ST, M_JOB };
struct Tab { Op v[300]; int mark[48]; int n; };
constexpr int AW = 4;
constexpr int PRED = 8;
constexpr int LW = 6;
constexpr int CV0 = 10;
constexpr int PD = 5;
constexpr int zidx(bool H, int kind, int idx) { return kind == OW ? idx : kind == OK ? (H ? 7 : 14) + idx : (H ? 11 : 7) + idx; }
constexpr int rpos(bool H, int kind, int idx) { return kind == OX ? 2 * idx : kind == OW ? 2 * idx + 1 : kind == OK ? (H ? 14 : 21) + idx : (H ? 17 : 14) + idx; }
constexpr u32 slot_of(bool H, int kind, int idx) { return kind == OK && idx == 3 ? 9 : rpos(H, kind, idx) % 9; }
constexpr Tab make_tab(bool H) {
  Tab t{}; int n = 0;
  auto I4 = [&](int kind, int idx, int lo = 0, int hi = 4) {
    for (int i = lo; i < hi; ++i) t.v[n++] = Op{kind, idx, i};
    if (kind != OX && kind <= OK && hi == (kind == OK && idx == 3 ? 2 : 4)) t.v[n++] = Op{OZ, zidx(H, kind, idx), 0};
  };
  for (int s = 0; s < 7; ++s) { if (s == 6 && H) t.v[n++] = Op{OPEEK, 0, 0}; I4(OX, s); I4(OW, s); }
  if (!H) for (int s = 0; s < 7; ++s) { if (s == 5) t.v[n++] = Op{OPEEK, 0, 0}; I4(OU, s); }
  I4(OK, 0); I4(OK, 1); I4(OK, 2); I4(OK, 3, 0, 2);
  t.mark[M_KV0] = n;
  if (H) for (int s = 0; s < 7; ++s) I4(OU, s);
  else t.v[n++] = Op{OZ, 18, 0};
  t.mark[M_EPI] = n;
  if (H) for (int j = 0; j < 14; ++j) { if (j == 11) t.v[n++] = Op{OPEEK, 2, 0}; I4(OB, j, 0, 2); }
  t.mark[M_ST] = n; t.v[n++] = Op{OST, 0, 0};
  if (H) {
    const int ord[17] = {31, 32, 33, 34, 35, 19, 20, 21, 22, 23, 24, 25, 26, 28, 29, 30, 27};
    int nd = 0, na = AW;
    for (int j = 0; j < AW; ++j) I4(OA, j, 0, 2);
    for (; nd < PRED; ++nd) I4(OB, ord[nd], 0, 2);
    for (int j = 0; j < 28; ++j) {
      t.mark[M_JOB + j] = n;
      if (j == 11) t.v[n++] = Op{OPEEK, 3, 0};
      for (int k = 0; k < 2; ++k) {
        if (na < 14) { I4(OA, na, 0, 2); ++na; }
        else if (nd < 17) { I4(OB, ord[nd], 0, 2); if (++nd == 16) t.v[n++] = Op{OZ, 18, 0}; }
        else if (nd < 22) { I4(OB, nd - 3, 0, 2); ++nd; }
      }
      if (j == 12) for (int k = 14; k < 14 + LW; ++k) I4(OA, k, 0, 2);
      if (j >= 14 && j + LW < 28) I4(OA, j + LW, 0, 2);
    }
    t.mark[M_JOB + 28] = n;
  }
  t.n = n;
  return t;
}
template <bool H> struct TB {
  static constexpr Tab t = make_tab(H);
  static constexpr int find(int kind, int idx, int i) { for (int k = 0; k < t.n; ++k) if (t.v[k].kind == kind && t.v[k].idx == idx && t.v[k].i == i) return k; return 1000; }
  static constexpr int etick(int n) {
    int k = t.mark[M_EPI];
    for (int i = 0; i < n && k < t.mark[M_ST]; ++i) { ++k; while (k < t.mark[M_ST] && (t.v[k].kind == OZ || t.v[k - 1].kind == OPEEK)) ++k; }
    return n >= 29 ? t.mark[M_ST] : k;
  }
  static constexpr int need0(int k) {
    if (k >= t.n || t.v[k].kind > OK || t.v[k].i != 0 || slot_of(H, t.v[k].kind, t.v[k].idx) == 9) return -1;
    const int p = rpos(H, t.v[k].kind, t.v[k].idx);
    return p < 9 || (p > 22 && H) ? -1 : (p - 9) >> 1;
  }
  static constexpr int need(int k) { for (int i = 0; i < k && i < t.n; ++i) if (need0(i) >= need0(k)) return -1; return need0(k); }
};
constexpr u32 L_R2 = 0, L_R1 = 9 * 8192;
constexpr int LAG = 12;
constexpr int cmax(int a, int b) { return a > b ? a : b; }

DI void mfma16i(f32x4& c, u32x4 a, u32x4 b) { asm volatile("v_mfma_f32_16x16x32_bf16 %0, %1, %2, %0" : "+v"(c) : "v"(a), "v"(b)); }
DI void mfma16z(f32x4& c, u32x4 a, u32x4 b) { asm volatile("v_mfma_f32_16x16x32_bf16 %0, %1, %2, 0" : "=&v"(c) : "v"(a), "v"(b)); }
template <bool HG, bool FIRST, class Hook>
DI void unit_gemm(u32x4 (&xf)[28], f32x4 (&acc)[8][2], int lane, int w, u32 za, Hook hook) {
  const u32 kg = lane >> 4, q = (lane & 15) >> 2, pp = lane & 3, t16 = lane & 15;
  const u32 vb0 = (8 * kg + q) * 128 + ((q >> 1) | ((kg & 1) << 1)) * 32 + (((pp & 1) << 1) | (pp >> 1)) * 8;
  constexpr int KIND = FIRST ? OW : OU;
  const u32 xb = w * 4096 + t16 * 128 + ((kg ^ (t16 >> 1)) * 16);
  u32 vb[2][4], xa[2][2], pre = 0;
#pragma unroll
  for (int c = 0; c < 8; ++c) { vb[c >> 2][c & 3] = (vb0 ^ ((c & 3) * 32)) + (c < 4 && !FIRST ? 131072 : (c >> 2) * 65536); OPAQUE(vb[c >> 2][c & 3]); }
  if (FIRST) {
#pragma unroll
    for (int c = 0; c < 4; ++c) { xa[c >> 1][c & 1] = (xb ^ ((c & 1) * 64)) + (c >> 1) * 81920; OPAQUE(xa[c >> 1][c & 1]); }
  }
  u32x2 fl[16], fh[16];
  auto wadr = [&](int s, int n) -> u32 { const int slot = slot_of(HG, KIND, s); return vb[slot >> 2 & 1][n & 3] + (slot & 3) * 16384 + ((n >> 2) & 1) * 8192 + (n >> 3) * 4096; };
  auto rdx = [&](int s, int i) { const int slot = slot_of(HG, OX, s); xf[4 * s + i] = lds128((slot < 5 ? xa[0][i >> 1] + slot * 16384 : xa[1][i >> 1] + (slot - 5) * 16384) + (i & 1) * 2048); };
  z_wait<zidx(HG, KIND, 0)>(za);
  if (FIRST) { rdx(0, 0); rdx(0, 1); rdx(0, 2); }
#pragma unroll
  for (int n = 0; n < PD; ++n) { fl[n] = tr64(wadr(0, n)); fh[n] = tr64(wadr(0, n) + 512); }
  SB();
  sfor<112>([&](auto gc) {
    constexpr int G = decltype(gc)::v, s = G >> 4, n = G & 15, f = n & 7, n4 = (n + PD) & 15, s4 = n < 16 - PD ? s : s + 1;
    constexpr bool all = HG || FIRST;
    constexpr bool live = all || f < 6, next = s4 < 7 && (all || (n4 & 7) < 6);
    if constexpr (all && (n & 1) == 0 && G < 112 - PD) { __builtin_amdgcn_s_waitcnt(0xC07F | ((2 * PD - 4) << 8)); SB(); }
    if constexpr (live) {
      if constexpr (G < 8) mfma16z(acc[f][0], u32x4{fl[n].x, fl[n].y, fh[n].x, fh[n].y}, xf[4 * s + (n >> 3) * 2 + 0]);
      else mfma16i(acc[f][0], u32x4{fl[n].x, fl[n].y, fh[n].x, fh[n].y}, xf[4 * s + (n >> 3) * 2 + 0]);
      SB();
    }
    if constexpr (n == 16 - PD && s < 6) z_wait_pre<zidx(HG, KIND, s + 1)>(za, pre);
    if constexpr (!FIRST && slot_of(HG, KIND, s) == 8 && n >= 16 - PD && n4 < 4) { vb[0][n4] -= 131072; OPAQUE(vb[0][n4]); }
    if constexpr (next) fl[n4] = tr64(wadr(s4, n4));
    SB();
    if constexpr (live) {
      if constexpr (G < 8) mfma16z(acc[f][1], u32x4{fl[n].x, fl[n].y, fh[n].x, fh[n].y}, xf[4 * s + (n >> 3) * 2 + 1]);
      else mfma16i(acc[f][1], u32x4{fl[n].x, fl[n].y, fh[n].x, fh[n].y}, xf[4 * s + (n >> 3) * 2 + 1]);
      SB();
    }
    if constexpr (next) fh[n4] = tr64(wadr(s4, n4) + 512);
    if constexpr (FIRST) {
      if constexpr (n == 0) rdx(s, 3);
      if constexpr (n >= 16 - PD && n4 < 3 && s < 6) rdx(s + 1, n4);
    }
    if constexpr (n == 12 - PD && s < 6) pre = z_peek<zidx(HG, KIND, s + 1)>(za);
    hook(gc);
    SB();
  });
  asm volatile("s_nop 7" : "+v"(acc[0][0]), "+v"(acc[0][1]), "+v"(acc[1][0]), "+v"(acc[1][1]), "+v"(acc[2][0]), "+v"(acc[2][1]), "+v"(acc[3][0]), "+v"(acc[3][1]),
               "+v"(acc[4][0]), "+v"(acc[4][1]), "+v"(acc[5][0]), "+v"(acc[5][1]));
  if (HG || FIRST) asm volatile("" : "+v"(acc[6][0]), "+v"(acc[6][1]), "+v"(acc[7][0]), "+v"(acc[7][1]));
  SB();
}
DI u32x4 frag16(f32x4 a, f32x4 b) {
  const auto x = __builtin_amdgcn_permlane16_swap(cvtpk(a.x, a.y), cvtpk(b.x, b.y), false, false);
  const auto y = __builtin_amdgcn_permlane16_swap(cvtpk(a.z, a.w), cvtpk(b.z, b.w), false, false);
  return u32x4{x[0], y[0], x[1], y[1]};
}

DI void load_wphi(u32x4 (&wp)[2], u32 base, int lane) {
  const u32 h = lane >> 5, fb = (lane >> 4) & 1, q = (lane & 15) >> 2, pp = lane & 3;
  u32 col = 16 * fb + 4 * pp; if (col == 28) col = 24;
  const u32 a = base + (4 * h + q) * 56 + col * 2;
  wp[0] = trfrag(a, a + 8 * 56);
  u32x2 lo = tr64(a + 16 * 56), hi = tr64(base + (24 + q) * 56 + col * 2);
  wp[1] = u32x4{lo.x, lo.y, h ? 0u : hi.x, h ? 0u : hi.y};
}
DI void load_wphi3(u32x4 (&wp)[3], u32 base, int o, int lane) {
  const u32 h = lane >> 5, fb = (lane >> 4) & 1, q = (lane & 15) >> 2, pp = lane & 3;
  u32 col = 16 * fb + 4 * pp; if (col == 28) col = 24;
#pragma unroll
  for (int i = 0; i < 6; ++i) {
    const u32 d0 = 8 * i + 4 * h - o;
    const bool ok = d0 <= 24;
    const u32x2 v = tr64(base + ((ok ? d0 : 0u) + q) * 56 + col * 2);
    wp[i >> 1][(i & 1) * 2] = ok ? v.x : 0u; wp[i >> 1][(i & 1) * 2 + 1] = ok ? v.y : 0u;
  }
}
DI u32x4 pack8(const f32x16& v, int s) {
  return u32x4{cvtpk(v[8 * s], v[8 * s + 1]), cvtpk(v[8 * s + 2], v[8 * s + 3]), cvtpk(v[8 * s + 4], v[8 * s + 5]), cvtpk(v[8 * s + 6], v[8 * s + 7])};
}
DI float sigmoid(float t) { return __builtin_amdgcn_rcpf(1.f + __builtin_amdgcn_exp2f(-LOG2E * t)); }
DI float vmax(float a, float b) { float r; asm("v_max_f32 %0, %1, %2" : "=v"(r) : "v"(a), "v"(b)); return r; }
DI float vmin(float a, float b) { float r; asm("v_min_f32 %0, %1, %2" : "=v"(r) : "v"(a), "v"(b)); return r; }
DI float max3(float a, float b, float c) { return __builtin_fmaxf(__builtin_fmaxf(a, b), c); }
DI float min3(float a, float b, float c) { return __builtin_fminf(__builtin_fminf(a, b), c); }
template <class Hook> DI void feature_exp(const u32x4 (&t)[3], const u32x4 (&wp)[3], f32x16& ep, f32x16& en, Hook hook) {
  f32x16 pz;
#pragma unroll
  for (int e = 0; e < 16; ++e) pz[e] = 0.f;
  pz = mfma(wp[0], t[0], pz);
  pz = mfma(wp[1], t[1], pz);
  pz = mfma(wp[2], t[2], pz);
  float mx = max3(max3(max3(pz[0], pz[1], pz[2]), max3(pz[3], pz[4], pz[5]), max3(pz[6], pz[7], pz[8])),
                  max3(max3(pz[9], pz[10], pz[11]), max3(pz[12], pz[13], pz[14]), pz[15]), pz[15]);
  float mn = min3(min3(min3(pz[0], pz[1], pz[2]), min3(pz[3], pz[4], pz[5]), min3(pz[6], pz[7], pz[8])),
                  min3(min3(pz[9], pz[10], pz[11]), min3(pz[12], pz[13], pz[14]), pz[15]), pz[15]);
  float a, b;
  both_halves(mx, a, b); mx = __builtin_fmaxf(a, b) * LOG2E;
  both_halves(mn, a, b); mn = __builtin_fminf(a, b) * LOG2E;
  asm volatile("" : "+v"(mx), "+v"(mn));
  hook();
#pragma unroll
  for (int e = 0; e < 16; ++e) ep[e] = __builtin_amdgcn_exp2f(__builtin_fmaf(pz[e], LOG2E, -mx));
  asm volatile("" : "+v"(ep));
  hook();
#pragma unroll
  for (int e = 0; e < 16; ++e) en[e] = __builtin_amdgcn_exp2f(__builtin_fmaf(pz[e], -LOG2E, mn));
  asm volatile("" : "+v"(en));
  hook();
}
DI void reduce_kv(int hd, int pc, srd_t ws, int lane) {
  const u32 h = lane >> 5, r = lane & 31, l16 = lane * 16;
  u32x4 pv[8];
  __builtin_amdgcn_s_sleep(12);
  for (int it = 0; it < SPINS; ++it) {
#pragma unroll
    for (int i = 0; i < 8; ++i) ldg_sc1(pv[i], h * 4096 + r * 16, ws, WS_KVP + (hd * 16 + 2 * i) * 4096 + pc * 512);
    asm volatile("s_waitcnt vmcnt(0)" : "+v"(pv[0]), "+v"(pv[1]), "+v"(pv[2]), "+v"(pv[3]), "+v"(pv[4]), "+v"(pv[5]), "+v"(pv[6]), "+v"(pv[7])::"memory");
    if (arrived(pv)) break;
  }
  u32x4 o;
#pragma unroll
  for (int e = 0; e < 4; ++e) {
    float lo[8], hi[8];
#pragma unroll
    for (int i = 0; i < 8; ++i) { lo[i] = u2f(pv[i][e] << 16); hi[i] = u2f(pv[i][e] & 0xffff0000u); }
    float a, c, d, f;
    both_halves(((lo[0] + lo[1]) + (lo[2] + lo[3])) + ((lo[4] + lo[5]) + (lo[6] + lo[7])), a, c);
    both_halves(((hi[0] + hi[1]) + (hi[2] + hi[3])) + ((hi[4] + hi[5]) + (hi[6] + hi[7])), d, f);
    o[e] = cvtpk(a + c, d + f);
    if (r == 29) o[e] = 0x3f803f80u;
    if (e >= 2 && (pc & 3) == 3) o[e] = 0u;
  }
  stg_sc1(not_ones(o), h ? OOB : l16, ws, WS_KVF + hd * 4096 + pc * 512);
}
template <bool H, class Epi, class Mid>
DI void task_o(int head, int rt32, int w, srd_t ws, u64 tag, int lane, const Sync& y, u64 fq, u32 fqa, u32 fdone, u32x4 own0, u32x4 own1, Epi epi, Mid mid) {
  const int h = lane >> 5;
  const u32 l16 = lane * 16;
  const int ro = (head * 28) & 15;
  u32x4 qf[3], wp[3], kf[4], k1[1];
  auto ldq = [&](u32 vo) {
    const u32 so = WS_Q + (rt32 * 28 + ((head * 7) >> 2)) * 1024;
    ldg_sc1o<0>(qf[0], vo, ws, so); ldg_sc1o<1024>(qf[1], vo, ws, so); ldg_sc1o<2048>(qf[2], ro >= 8 ? vo : OOB, ws, so);
  };
  auto ldk = [&]() { ldg_sc1(k1[0], l16, ws, WS_KVF + head * 4096 + w * 1024); };
  bool early = false, second = false;
  if (H) {
    asm volatile("s_waitcnt vmcnt(0)" : "+v"(fq)::"memory");
    early = __builtin_amdgcn_ballot_w64(fq != tag) == 0;
    ldq(early ? l16 : OOB);
    flag_peek(fq, ws, early ? OOB : fqa);
  }
  epi([&](int c) {
    if (H && c == 2) {
      wait3<3>(qf[0], qf[1], qf[2], fq);
      if (!early && __builtin_amdgcn_ballot_w64(fq != tag) == 0) { early = second = true; ldq(l16); }
    }
  });
  __builtin_amdgcn_s_setprio(H ? 3 : 0);
  u32 done = 0;
  if (H) { if (second) VMWAIT(1); wait3<63>(qf[0], qf[1], qf[2], fq); }
  if (!early) {
    flag_try<true, 0>(done, ws, fdone, tag);
    if (H) {
      flag_wait(ws, fqa, tag);
      ldq(l16);
      asm volatile("s_waitcnt vmcnt(0)" : "+v"(qf[0]), "+v"(qf[1]), "+v"(qf[2])::"memory");
    } else {
      qf[0] = own0; qf[1] = own1; qf[2] = u32x4{0u, 0u, 0u, 0u};
      if ((rt32 & 28) != 28) mid();
    }
  }
  float gate = 0.f;
  if (H) {
    const u32 p1 = lds_peek<C_SYNC>(y), p2 = lds_peek<C_PROG>(y);
    lds_wait_pre<C_SYNC, 1>(y.ra, p1); lds_wait_pre<C_PROG, 1>(y.ra, p2);
    gate = *(LAS float*)(size_t)(L_GW + 256 + (w * 32 + (lane & 31)) * 4);
  }
  load_wphi3(wp, L_WPHI, ro, lane);
  if (H) { gate = sigmoid(gate); OPAQUE(gate); }
  int hk = 0;
  f32x16 ep, en; feature_exp(qf, wp, ep, en, [&]() { if (hk++ == 0) ldk(); flag_try<false, 1>(done, ws, fdone, tag); });
  __builtin_amdgcn_s_setprio(3);
  if (early) { flag_try<true, 1>(done, ws, fdone, tag); VMWAIT(1); } else VMWAIT(0);
  asm volatile("" : "+v"(k1[0])::"memory");
  if (!H && (rt32 & 28) == 28) mid();
  if (!H) {
    lds_wait<C_SYNC, 1>(y); lds_wait<C_PROG, 1>(y);
    gate = sigmoid(*(LAS float*)(size_t)(L_GW + 256 + (w * 32 + (lane & 31)) * 4));
    OPAQUE(gate);
  }
  for (int it = 0; it < SPINS && !arrived(k1); ++it) {
    __builtin_amdgcn_s_sleep(12);
    ldk();
    asm volatile("s_waitcnt vmcnt(0)" : "+v"(k1[0])::"memory");
  }
  sts128(L_KVF + w * 1024 + l16, k1[0]);
  lds_post<C_KVF>(y);
  asm volatile("" : "+v"(ep), "+v"(en));
  u32x4 ef[2][2] = {{pack8(ep, 0), pack8(ep, 1)}, {pack8(en, 0), pack8(en, 1)}};
  asm volatile("" : "+v"(ef[0][0]), "+v"(ef[0][1]), "+v"(ef[1][0]), "+v"(ef[1][1]));
  lds_wait<C_KVF, 1>(y);
#pragma unroll
  for (int i = 0; i < 4; ++i) kf[i] = lds128(L_KVF + i * 1024 + l16);
  f32x16 np, nn;
#pragma unroll
  for (int e = 0; e < 16; ++e) { np[e] = 0.f; nn[e] = 0.f; }
  np = mfma(kf[0], ef[0][0], np); nn = mfma(kf[2], ef[1][0], nn);
  np = mfma(kf[1], ef[0][1], np); nn = mfma(kf[3], ef[1][1], nn);
  const float rp = __builtin_amdgcn_rcpf(np[13]), rn = __builtin_amdgcn_rcpf(nn[13]);
  const float inv = __builtin_amdgcn_rcpf(np[12] * rp + nn[12] * rn + 1e-6f) * gate;
  const float cp = swap_hi(rp * inv, h), cn = swap_hi(rn * inv, h);
  f32x16 o;
#pragma unroll
  for (int e = 0; e < 16; ++e) o[e] = np[e] * cp + nn[e] * cn;
  u32x4 o0 = pack8(o, 0), o1 = pack8(o, 1);
  const float omg = 1.f - gate, oh = u2f(cvtpk(omg, 0.f) << 16);
  o1.z = h ? cvtpk(oh, omg - oh) : o1.z; o1.w = h ? 0u : o1.w;
  stg_sc1(not_ones(o0), l16, ws, WS_O + (rt32 * 32 + head * 2) * 1024);
  stg_sc1(not_ones(o1), l16, ws, WS_O + (rt32 * 32 + head * 2 + 1) * 1024);
}
DI void dump_tile(const f32x16 (&acc)[2][2], u32 base, int lane) {
  const u32 r = lane & 31, a = base + (r >> 3) * 8192 + (r & 7) * 256 + (((lane >> 5) ^ (r & 7)) * 16);
#pragma unroll
  for (int g4 = 0; g4 < 4; ++g4) {
    u32 ag = a ^ (g4 * 32); OPAQUE(ag);
#pragma unroll
    for (int c = 0; c < 2; ++c)
#pragma unroll
      for (int t = 0; t < 2; ++t)
        sts128(ag + t * 32768 + c * 128, __builtin_bit_cast(u32x4, f32x4{acc[c][t][4 * g4], acc[c][t][4 * g4 + 1], acc[c][t][4 * g4 + 2], acc[c][t][4 * g4 + 3]}));
  }
  asm volatile("s_waitcnt lgkmcnt(0)\n\ts_barrier" ::: "memory");
}
DI u32 brow_off(int lane) { const u32 row = lane >> 3; return row * 896 + ((lane & 7) ^ (((row >> 1) & 1) * 4)) * 16; }
struct BPath { u32 wa, ra[2]; };
DI BPath make_bpath(u32 base, int lane) {
  const u32 h = lane >> 5, cb = (lane >> 4) & 1, q = (lane & 15) >> 2, pp = lane & 3;
  BPath t;
  t.wa = base + lane * 16; OPAQUE(t.wa);
#pragma unroll
  for (int c = 0; c < 2; ++c) { t.ra[c] = base + (4 * h + q) * 128 + ((c ^ ((q >> 1) & 1)) * 64) + cb * 32 + pp * 8; OPAQUE(t.ra[c]); }
  return t;
}

template <class Hook> DI void epi_h(const f32x4 (&acc)[8][2], srd_t ws, u32 soff, int lane, Hook hook) {
#pragma unroll
  for (int c = 0; c < 4; ++c) {
    float hv[8];
#pragma unroll
    for (int e = 0; e < 8; ++e) {
      const float x = acc[c][e >> 2][e & 3];
      const float t = __builtin_amdgcn_exp2f(x * __builtin_fmaf(x * x, -0.10294323970f, -2.30220819814f));
      hv[e] = x * __builtin_amdgcn_rcpf(1.f + t) * acc[4 + c][e >> 2][e & 3];
    }
    const u32x4 hp = frag16(f32x4{hv[0], hv[1], hv[2], hv[3]}, f32x4{hv[4], hv[5], hv[6], hv[7]});
    stg_sc1(hp, lane * 16, ws, soff + c * 1024);
    hook(c);
  }
}

template <bool P1, bool isH> DI void body(const Params& p, const int wave) {
  const int tid = threadIdx.x, lane = tid & 63;
  const int w = wave & 3;
  const int b = blockIdx.x, xcd = b & 7, bi = b >> 3;
  const int m = 8 * (xcd >> 2) + (bi & 7), sl = (xcd & 1) * 4 + (bi >> 3), half = (xcd >> 1) & 1;
  const int tl = half * 56 + (xcd & 1) * 32 + bi, rt64 = 16 * (xcd >> 2) + tl / 7, ct2 = tl % 7;
  const int j = 7 * half + sl;
  const int kvhead = 8 * half + sl;
  const int h = lane >> 5;
  const u32 r = lane & 31, l4 = (lane & 3) * 4, l16 = lane * 16;
  const u32 cb = (lane >> 4) & 1, q = (lane & 15) >> 2, pp = lane & 3;
  const srd_t ws = mksrd(p.ws, WS_END), sqkv = mksrd(p.wqkv, D * 3 * D * 2);
  const int rt32 = m * 4 + w;

  if (P1) __builtin_amdgcn_s_setprio(2);
  const u32 zm = L_Z + wave * 512;
  u32 z1 = L_Z + (lane & 3) * 512 + 256, z2 = z1 + 2048, oob = OOB;
  OPAQUE(z1); OPAQUE(z2); OPAQUE(oob);
  Sync y{lane == 0 ? L_CNT + w * 4 : L_DUM + lane * 4, L_CNT + l4, 1u};
  OPAQUE(y.pa); OPAQUE(y.ra); OPAQUE(y.one);
  u32x2 ev;
  asm volatile("buffer_load_dwordx2 %0, off, %1, 0 sc1" : "=v"(ev) : "s"(ws) : "memory");

  if (P1) {
    u32 fvo, sink = 0, tvo2;
    const srd_t st2 = mksrd(w == 0 ? p.wg : w == 1 ? p.wu : w == 2 ? p.wd : p.wo, w < 3 ? D * DF * 2 : D * D * 2);
    {
      const u32 scr = L_K3 + w * 1024;
      const int km = rt32;
      dma_buf(scr, km < 56 ? (lane >> 2) * 2688 + (lane & 3) * 16 : OOB, sqkv, (km >> 1) * 16 * 2688 + (1 + (km & 1)) * 896 + kvhead * 56);
      {
        const srd_t sp = mksrd(p.wphi, 16 * 1568);
        set_m0((w & 1) ? L_K3 + 4096 : L_WPHI);
        dma_off<0>((w & 1) ? OOB : l16, sp, kvhead * 1568);
        dma_off<1024>((w & 1) || lane >= 34 ? OOB : l16, sp, kvhead * 1568);
      }
      u32x4 gwf;
      ldg(gwf, r == 30 ? h * 16 : OOB, mksrd(p.gw, D * 2), (km >> 1) * 32);
      {
        const u32 row = (xcd >> 2) * 224 + bi * 7 + (lane >> 2), c4 = lane & 3;
        const u32 wv = (lane < 28 && !((xcd & 1) && c4 == 3)) ? row * 3584 + (half * 7 + (xcd & 1) * 4 + c4) * 128 : OOB;
        constexpr int SK = 2, NR = 7 - SK;
        const u32 row1 = SK * 64 + ((xcd >> 2) * 32 + bi) * NR + (lane >> 2), xr = lane / NR;
        const u32 tvo1 = w < 2 ? ((lane < 4 * NR && !((xcd & 1) && c4 == 3)) ? row1 * 3584 + (half * 7 + (xcd & 1) * 4 + c4) * 128 : OOB)
                       : w == 2 ? (lane < 8 * NR ? (((xcd * 32 + bi) * 8 + xr) * 7 + SK + (lane - xr * NR)) * 128 : OOB)
                                : ((xcd & 1) && lane < 28 ? row * 2688 + (half * 3 + c4) * 128 : OOB);
        touch(sink, tvo1, w < 2 ? st2 : w == 2 ? mksrd(p.x, S * D * 2) : sqkv);
        tvo2 = w < 2 ? wv + 14 * 128 : w == 2 ? (lane < 49 ? (b * 49 + lane) * 128 : OOB) : (lane < 13 ? (b * 13 + lane) * 128 : OOB);
      }
      const u32x4 ones = {~0u, ~0u, ~0u, ~0u};
      stg_sc1(ones, l16, ws, WS_KVP + ((kvhead * 16 + m) * 4 + w) * 1024);
      __builtin_amdgcn_s_setprio(3);
      asm volatile("s_waitcnt vmcnt(2)" : "+v"(ev), "+v"(gwf)::"memory");
      u32x4 kt;
      if (w & 1) {
        const u32 a = scr + (8 * h + q) * 64 + cb * 32 + 8 * pp;
        kt = trfrag(a, a + 256);
        if (r >= 28) kt = gwf;
      } else {
        u32x4 wp[2]; load_wphi(wp, L_WPHI, lane);
        const u32 a = scr + ((lane & 3) + ((lane >> 1) & 4) + ((lane << 1) & 8)) * 64 + h * 8;
        f32x16 t;
#pragma unroll
        for (int e = 0; e < 16; ++e) t[e] = 0.f;
#pragma unroll
        for (int s = 0; s < 2; ++s) {
          const u32x2 lo = *(LAS u32x2*)(size_t)(a + s * 32), hi = *(LAS u32x2*)(size_t)(a + s * 32 + 16);
          t = mfma(u32x4{lo.x, lo.y, hi.x, hi.y}, wp[s], t);
        }
#pragma unroll
        for (int e = 0; e < 8; ++e) t[e] *= LOG2E;
        kt = pack8(t, 0);
      }
      if (m == 15) kt = ones;
      stg_sc1(kt, m != 14 ? l16 : OOB, ws, m == 15 ? WS_KVF + (kvhead * 4 + w) * 1024 : WS_KT + (kvhead * 56 + km) * 1024);
      fvo = lane == 0 ? WS_FK + (kvhead * 64 + km) * 8 : OOB;
    }
    const u64 tag = (((u64)rfl(ev.y) << 32) | rfl(ev.x)) + 1;
    u32x4 xf[28];
    f32x4 acc[8][2];
    u32 done = 0;
    set_m0(zm);
    unit_gemm<isH, true>(xf, acc, lane, w, z2, [&](auto gc) {
      constexpr int s = decltype(gc)::v >> 4, n = decltype(gc)::v & 15;
      if constexpr (n == 15) z_post<s>(oob, ws);
      if constexpr (s <= 2 && (n == 3 || n == 9)) flag_try<s == 2 && n == 9, s>(done, ws, fvo, tag);
      if constexpr (s == 3 && n == 11) touch(sink, tvo2, st2);
    });
    {
      const u32x4 ones = {~0u, ~0u, ~0u, ~0u};
      stg_sc1(ones, l16, ws, WS_O + (rt32 * 32 + kvhead * 2) * 1024);
      stg_sc1(ones, l16, ws, WS_O + (rt32 * 32 + kvhead * 2 + 1) * 1024);
    }
    if (isH) epi_h(acc, ws, WS_H + (rt32 * 112 + j * 4) * 1024, lane, [](int) {});
    u32x4 qa[8];
    if (!isH) {
#pragma unroll
      for (int c = 0; c < 8; ++c) { qa[c] = frag16(acc[c][0], acc[c][1]); OPAQUE(qa[c]); }
    }
    __builtin_amdgcn_s_setprio(isH ? 1 : 3);
    done = 0;
    fvo = lane >= 16 ? OOB : (isH ? WS_FH + (rt32 * 16 + j) * 128 : WS_FQ + (rt32 * 2 + half) * 128) + lane * 8;
    u64 fk = 0;
    const u32 oh = w * 4 + ((lane >> 3) & 3), ort = rt64 * 2 + h;
    const u32 fka = !isH ? WS_FK + (m * 64 + lane) * 8 : (lane & 4) ? WS_FQ + (rt32 * 2 + half) * 128
                  : (oh & 7) == 7 ? WS_FQ + (ort * 2 + (oh >> 3)) * 128 : WS_FH + (ort * 16 + oh - (oh >> 3)) * 128;
    unit_gemm<isH, false>(xf, acc, lane, w, z2, [&](auto gc) {
      constexpr int s = decltype(gc)::v >> 4, n = decltype(gc)::v & 15;
      if constexpr (isH && s <= 3 && (n == 3 || n == 9)) flag_try<s == 3 && n == 9, 0>(done, ws, fvo, tag);
      if constexpr (s == 4 && n == 15) lds_post<C_PROG>(y);
      if constexpr (isH && s == 6 && n == 2) { __builtin_amdgcn_s_setprio(3); flag_peek(fk, ws, fka); __builtin_amdgcn_s_setprio(1); }
      if constexpr (!isH && s == 0 && n == 15) z_post<7>(oob, ws);
      if constexpr (!isH && s == 0 && n == 1) flag_peek(fk, ws, fka);
      if constexpr (!isH && s == 2 && n == 1) flag_check<1>(fk, ws, fka, tag);
    });
    OPAQUE(sink);
    lds_post<C_PROG>(y);
    __builtin_amdgcn_s_setprio(3);
    u32x4 bo[8][2], own[2] = {};
    if (!isH) { const u32x4 b4 = frag16(acc[4][0], acc[4][1]), b5 = frag16(acc[5][0], acc[5][1]); own[0] = half ? qa[6] : b4; own[1] = half ? qa[7] : b5; }
    task_o<isH>(kvhead, rt32, w, ws, tag, lane, y, fk, fka, fvo + (isH ? 64 * 16 * 128 : 0), own[0], own[1], [&](auto hook) {
      if (isH) epi_h(acc, ws, WS_H + (rt32 * 112 + (14 + j) * 4) * 1024, lane, hook);
      else {
#pragma unroll
        for (int c = 0; c < 8; ++c) stg_sc1(qa[c], l16, ws, WS_Q + (rt32 * 28 + half * 20 + c) * 1024);
#pragma unroll
        for (int c = 0; c < 6; ++c) stg_sc1(frag16(acc[c][0], acc[c][1]), l16, ws, WS_Q + (rt32 * 28 + 8 + c + half * (c < 4 ? 8 : 2)) * 1024);
      }
    }, [&]() { if (!isH) reduce_kv(m, half * 4 + w, ws, lane); });
    if (isH) {
      f32x16 oc[2][2];
#pragma unroll
      for (int i = 0; i < 4; ++i)
#pragma unroll
        for (int e = 0; e < 16; ++e) oc[i >> 1][i & 1][e] = 0.f;
      asm volatile("" : "+v"(oc[0][0]), "+v"(oc[0][1]), "+v"(oc[1][0]), "+v"(oc[1][1]));
      u32x4 ao[16];
      const u32 l16o = l16 + 32 * 1024, oso = WS_O + (rt64 * 2 * 32 + w * 8) * 1024;
#pragma unroll
      for (int i = 0; i < 16; ++i) asm volatile("" : "=v"(ao[i]));
      __builtin_amdgcn_s_sleep(26);
      u32 miss = 0xff, go = 0xff, next = 0;
      auto fetch = [&]() {
        sfor<8>([&](auto ic) {
          constexpr int i = decltype(ic)::v, hh = i >> 1, t = i & 1;
          ldg2_if<i, (hh & 1) * 2048>(ao[4 * hh + t], ao[4 * hh + 2 + t], go, t ? l16o : l16, ws, oso + (hh >> 1) * 4096);
        });
      };
      auto consume = [&](auto hc) {
        constexpr int hh = decltype(hc)::v;
        if ((go >> (2 * hh)) & 3) {
#pragma unroll
          for (int t = 0; t < 2; ++t) {
            const u32x4 two[2] = {ao[4 * hh + t], ao[4 * hh + 2 + t]};
            if (arrived(two)) miss &= ~(go & (1u << (2 * hh + t)));
          }
        }
        if (next == hh && !((miss >> (2 * hh)) & 3)) {
#pragma unroll
          for (int u = 0; u < 8; ++u) oc[(u >> 1) & 1][u & 1] = mfma(bo[2 * hh + (u >> 2)][(u >> 1) & 1], ao[4 * hh + (u >> 2) * 2 + (u & 1)], oc[(u >> 1) & 1][u & 1]);
          next = hh + 1;
        }
      };
      fetch();
      z_wait<18>(L_Z + 2048 + w * 512 + 256);
      {
        const BPath bp = make_bpath(L_R1 + w * 2048, lane);
#pragma unroll
        for (int i = 0; i < 16; ++i) bo[i >> 1][i & 1] = trfrag(bp.ra[i & 1] + (i >> 1) * 8192, bp.ra[i & 1] + (i >> 1) * 8192 + 1024);
      }
      sfor<4>([&](auto hc) { constexpr int hh = decltype(hc)::v; landed4<12 - 4 * hh>(ao[4 * hh], ao[4 * hh + 1], ao[4 * hh + 2], ao[4 * hh + 3]); consume(hc); });
      go = __builtin_popcount(miss) <= 4 ? miss : 0;
      const u32 wv = ((lane >> 3) & 1) * 32768 + (lane >> 4) * 2048 + 1024 + (((lane >> 3) + w) & 7) * 128;
      for (int it = 0; it < SPINS && next < 4; ++it) {
        u32 v;
        fetch();
        asm volatile("buffer_load_dword %0, %1, %2, %3 offen sc1" : "=v"(v) : "v"((miss & ~go) ? wv : OOB), "s"(ws), "s"(oso) : "memory");
        asm volatile("s_waitcnt vmcnt(0)" : "+v"(ao[0]), "+v"(ao[1]), "+v"(ao[2]), "+v"(ao[3]), "+v"(ao[4]), "+v"(ao[5]), "+v"(ao[6]), "+v"(ao[7]), "+v"(ao[8]), "+v"(ao[9]),
                     "+v"(ao[10]), "+v"(ao[11]), "+v"(ao[12]), "+v"(ao[13]), "+v"(ao[14]), "+v"(ao[15]), "+v"(v)::"memory");
        const u64 em = __builtin_amdgcn_ballot_w64(v == 0xffffffffu) & 0x0101010101010101ull;
        const u32 empty = (u32)((em * 0x0102040810204080ull) >> 56);
        sfor<4>(consume);
        go = __builtin_popcount(miss) <= 4 ? miss : miss & ~empty;
      }
#pragma unroll
      for (int t = 0; t < 2; ++t) *(LAS float*)(size_t)(h ? L_GW + (t * 32 + r) * 4 : L_DUM + r * 4) = u2f(ao[2 + t].z << 16) + u2f(ao[2 + t].z & 0xffff0000u);
      dump_tile(oc, L_R1 + w * 2048, lane);
      flag_set(ws, (b | w | lane) ? OOB : WS_E, tag);
    }
  } else {
    using T = TB<isH>;
    const srd_t sx = mksrd(p.x, S * D * 2);
    constexpr u32 wrs = isH ? 3584 : 2688;
    u32 xvo[4], wvo[4];
#pragma unroll
    for (int i = 0; i < 4; ++i) {
      xvo[i] = (lane >> 3) * 896 + (((lane & 7) ^ ((4 * i + (lane >> 4)) & 7)) * 16) + i * (8 * 896 - 1024);
      wvo[i] = (lane >> 3) * wrs + ((((lane >> 1) & 3) ^ (((lane >> 4) & 1) | ((i & 1) << 1))) * 32) + (lane & 1) * 16 + i * (8 * wrs - 1024);
    }
    const srd_t sw = isH ? mksrd(w < 2 ? p.wg : p.wu, D * DF * 2) : sqkv;
    const u32 xso = (m * 128 + w * 32) * 896;
    const u32 wso1 = (w & 1) * 32 * wrs + (isH ? j * 128 : half * 640 + (w >> 1) * 128);
    const u32 wso2 = (w & 1) * 32 * wrs + (isH ? (14 + j) * 128 : half ? 512 - (w >> 1) * 64 : 256 + (w >> 1) * 128);
    const u32 mq = w * 4096, mq2 = w * 2048;
    const u32 kso4 = WS_KT + kvhead * 56 * 1024 + mq, kso2 = WS_KT + kvhead * 56 * 1024 + mq2;
    const u32 l16h = l16 + 112 * 1024;
    const u32 hso = WS_H + (rt64 * 2 * 112 + w * 14) * 1024, bso = w * 14 * 16 * 896 + ct2 * 128;
    const srd_t swd = mksrd(p.wd, D * DF * 2);
    const u32 bvo = brow_off(lane);
    auto fadr = [&](int k) -> u32 {
      return k == 0 ? WS_FK + (kvhead * 64 + lane) * 8 : WS_FH + (((k - 2) * 64 + rt64 * 2 + h) * 16 + ((w * 14) >> 2) + ((lane >> 3) & 3)) * 128;
    };
    u32 so = 0, pre = 0, fa = 0;
    u64 fv = 0, tag = 0;
    u32x4 xf[28], af[28][2], bf[19][2], kvs;
    const srd_t swo = mksrd(p.wo, D * D * 2);
    const u32 bvo1 = bvo + 8 * 896 - 1024, bvop = lane < 32 ? bvo1 : OOB;
    const u32 xb = w * 4096 + r * 128 + ((h ^ ((r >> 1) & 7)) * 16);
    constexpr u32 L_PHI = slot_of(isH, OK, 1) * 16384, L_VF = slot_of(isH, OK, 2) * 16384;
    struct { u32 pw[4], pr[2], vw, vr, lr, lw, so; } ea;
    float keep;
    auto op = [&](auto kc) {
      constexpr int K = decltype(kc)::v;
      constexpr Op o = T::t.v[K];
      if constexpr (T::need(K) >= 0) z_wait_pre<T::need(K)>(z1, pre);
      if constexpr (isH && o.kind == OU && o.idx == 6 && o.i == 0) z_wait<zidx(isH, OU, 3)>(z2);
      constexpr int chk = o.i != 0 ? -1 : (o.kind == OK && o.idx == 0) ? 0 : (o.kind == OA && o.idx == 0) ? 2
                        : (o.kind == OA && o.idx == 14) ? 3 : -1;
      if constexpr (chk >= 0) { flag_check<K - 1 - T::find(OPEEK, chk, 0)>(fv, ws, fa, tag); }
      if constexpr (o.kind <= OK) {
        constexpr u32 sb = slot_of(isH, o.kind, o.idx) * 16384;
        if constexpr (o.i == 0) {
          if constexpr (o.kind == OX) so = dma_open<sb, o.idx * 128>(mq, xso);
          else if constexpr (o.kind == OK) so = dma_open<sb, o.idx * 16384>(o.idx < 3 ? mq : mq2, o.idx < 3 ? kso4 : kso2);
          else so = dma_open<sb, o.idx * 64 * wrs>(mq, o.kind == OW ? wso1 : wso2);
          if constexpr (!isH && o.kind == OU && o.idx == 0) {
#pragma unroll
            for (int i = 0; i < 4; ++i) if (w >= 2 && (((lane >> 2) ^ i) & 1)) wvo[i] = OOB;
          }
        }
        if constexpr (o.kind == OK) dma_off_sc1<o.i * 1024>(l16, ws, so);
        else dma_off<o.i * 1024>(o.kind == OX ? xvo[o.i] : wvo[o.i], o.kind == OX ? sx : sw, so);
      } else if constexpr (o.kind == OZ) { set_m0(zm); z_post<o.idx>(oob, ws); }
      else if constexpr (o.kind == OPEEK) { fa = fadr(o.idx); flag_peek(fv, ws, fa); }
      else if constexpr (o.kind == OB) {
        if constexpr (o.idx < 19) ldg(bf[o.idx][o.i], bvo, swd, bso + ((o.idx >= 14 ? o.idx + 51 : o.idx) * 16 + o.i * 8) * 896);
        else {
          if constexpr (o.idx == 31 && o.i == 0) lds_wait<C_SYNC, 3>(y);
          if constexpr (o.idx == 35 && o.i == 0) lds_wait<C_PROG, 1>(y);
          if constexpr (o.idx == 28 && o.i == 0) lds_wait<C_PROG, 2>(y);
          if constexpr (o.i == 0) {
            if constexpr (o.idx < 28) so = dma_open<L_R2 + (o.idx - 19) * 8192, (o.idx + 37) * 16 * 896>(mq2, bso);
            else so = dma_open<L_R1 + (o.idx - 28) * 8192, (((o.idx - 28) >> 1) * 28 + (o.idx & 1) * 16) * 896>(mq2, w * 112 * 896 + ct2 * 128);
          }
          dma_off<o.i * 1024>(o.i == 0 ? bvo : (o.idx >= 28 && (o.idx & 1)) ? bvop : bvo1, o.idx < 28 ? swd : swo, so);
        }
      }
      else if constexpr (o.kind == OA) {
        constexpr int ks = o.idx >= 14 ? o.idx + 42 : o.idx;
        ldg_sc1o<(ks & 3) * 1024>(af[o.idx][o.i], o.i ? l16h : l16, ws, hso + (ks >> 2) * 4096);
      } else if constexpr (o.kind == OST) stg_sc1(not_ones(kvs), ea.so, ws, WS_KVP + ((kvhead * 16 + m) * 4 + w) * 1024);
      if constexpr (T::need(K + 3) >= 0) pre = z_peek<T::need(K + 3)>(z1);
      constexpr int J = K - LAG;
      if constexpr (J >= 0) {
        constexpr Op oj = T::t.v[J];
        if constexpr (oj.kind == OX && oj.i == 3) {
          if constexpr (J == 3) {
            asm volatile("s_waitcnt vmcnt(%1)" : "+v"(ev) : "n"(LAG) : "memory");
            tag = (((u64)rfl(ev.y) << 32) | rfl(ev.x)) + 1;
          } else VMWAIT(LAG);
#pragma unroll
          for (int kk = 0; kk < 4; ++kk) xf[4 * oj.idx + kk] = lds128(slot_of(isH, OX, oj.idx) * 16384 + (xb ^ (kk * 32)));
        }
      }
      SB();
    };
    auto ops = [&](auto ac, auto bc) { sfor<decltype(bc)::v - decltype(ac)::v>([&](auto kc) { op(IC<decltype(ac)::v + decltype(kc)::v>{}); }); };
#define OPS(a, b) ops(IC<(a)>{}, IC<(b)>{})
#define MK(m) (T::t.mark[m])
#define TICK(M, n, N) OPS(MK(M) + (MK((M) + 1) - MK(M)) * (n) / (N), MK(M) + (MK((M) + 1) - MK(M)) * ((n) + 1) / (N))
#define ETICK(n) OPS(T::etick(n), T::etick((n) + 1))
    f32x16 ka, va;
#pragma unroll
    for (int e = 0; e < 16; ++e) { ka[e] = 0.f; va[e] = r == 28 ? 1.f : 0.f; }
    __builtin_amdgcn_s_setprio(2);
    OPS(0, 36);
    {
      const u32 t0 = 4 * cb + q, ra = L_PHI + (w >> 1) * 2048 + (16 * h + t0) * 64;
#pragma unroll
      for (int g4 = 0; g4 < 4; ++g4) { ea.pw[g4] = L_PHI + w * 4096 + r * 64 + (((2 * g4 + h) ^ ((r >> 1) & 7)) * 8); OPAQUE(ea.pw[g4]); }
      ea.pr[0] = ra + (((4 * (w & 1) + pp) ^ (t0 >> 1)) * 8); ea.pr[1] = ra + 512 + (((4 * (w & 1) + pp) ^ ((t0 >> 1) + 4)) * 8);
      ea.vw = L_VF + w * 2048 + l16; ea.vr = L_VF + h * 1024 + ((lane & 15) + 32 * cb) * 16;
      ea.lr = L_VF + w * 2048 + (r >> 4) * 1024 + (30 + 32 * ((r >> 2) & 1)) * 16 + (((r >> 3) & 1) * 4 + (r & 3)) * 2;
      ea.lw = L_GW + 256 + (w * 32 + r) * 4; ea.so = (h * 16 + (lane & 15) + 32 * cb) * 16;
      keep = h ? 0.f : 1.f;
      OPAQUE(ea.pr[0]); OPAQUE(ea.pr[1]); OPAQUE(ea.vw); OPAQUE(ea.vr); OPAQUE(ea.lr); OPAQUE(ea.lw); OPAQUE(ea.so); OPAQUE(keep);
      asm volatile("" : "+v"(va));
    }
    OPS(36, MK(M_KV0));
    {
      u32 ak[4];
#pragma unroll
      for (int t = 0; t < 4; ++t) { ak[t] = slot_of(isH, OK, t) * 16384 + l16; OPAQUE(ak[t]); }
      u32x4 fk[3], fw[3];
      u32 pk = 0;
      auto reads = [&](int ks) { fk[ks % 3] = lds128(ak[ks >> 3] + (ks & 7) * 2048); fw[ks % 3] = lds128(ak[ks >> 3] + (ks & 7) * 2048 + 1024); };
      z_wait<zidx(isH, OK, 0)>(z2);
      reads(0); reads(1); reads(2);
      SB();
      sfor<28>([&](auto kc) {
        constexpr int ks = decltype(kc)::v, t = ks >> 3;
        ka = mfma(fk[ks % 3], xf[ks], ka);
        SB();
        asm volatile("" : "+v"(ka));
        TICK(M_KV0, ks, 28);
        va = mfma(xf[ks], fw[ks % 3], va);
        SB();
        asm volatile("" : "+v"(va));
        if constexpr ((ks & 7) == 3 && t < 3) pk = z_peek<zidx(isH, OK, t + 1)>(z2);
        if constexpr ((ks & 7) == 5) z_wait_pre<zidx(isH, OK, t + 1)>(z2, pk);
        if constexpr (ks + 3 < 28) reads(ks + 3);
        SB();
      });
    }
    {
      ETICK(0);
      float mx = max3(max3(max3(ka[0], ka[1], ka[2]), max3(ka[3], ka[4], ka[5]), max3(ka[6], ka[7], ka[8])),
                      max3(max3(ka[9], ka[10], ka[11]), max3(ka[12], ka[13], ka[14]), ka[15]), ka[15]);
      asm volatile("" : "+v"(mx));
      ETICK(1);
      float mn = min3(min3(min3(ka[0], ka[1], ka[2]), min3(ka[3], ka[4], ka[5]), min3(ka[6], ka[7], ka[8])),
                      min3(min3(ka[9], ka[10], ka[11]), min3(ka[12], ka[13], ka[14]), ka[15]), ka[15]);
      asm volatile("" : "+v"(mn));
      ETICK(2);
      { float a, c; both_halves(mx, a, c); mx = vmax(a, c); both_halves(mn, a, c); mn = vmin(a, c); }
      asm volatile("" : "+v"(mx), "+v"(mn));
      ETICK(3);
      u32x4 vf[2] = {pack8(va, 0), pack8(va, 1)}, vv[4][2];
      asm volatile("" : "+v"(vf[0]), "+v"(vf[1]));
      ETICK(4);
      u32 lg = 0;
      sfor<2>([&](auto sc) {
        constexpr int sg = decltype(sc)::v, E0 = 5 + 10 * sg;
        f32x16 e;
        float ps[4];
        sfor<4>([&](auto gc) {
          constexpr int g4 = decltype(gc)::v;
#pragma unroll
          for (int i = 4 * g4; i < 4 * g4 + 4; ++i) e[i] = __builtin_amdgcn_exp2f(sg == 0 ? ka[i] - mx : mn - ka[i]);
          if constexpr (g4 > 0) { ps[g4 - 1] = (e[4 * g4 - 4] + e[4 * g4 - 3]) + (e[4 * g4 - 2] + e[4 * g4 - 1]); asm volatile("" : "+v"(ps[g4 - 1])); }
          float t = e[4 * g4 + 3]; asm volatile("" : "+v"(t)); e[4 * g4 + 3] = t;
          if constexpr (g4 == 2) pre = sg == 0 ? z_peek<(isH ? zidx(isH, OU, 6) : 18)>(z2) : lds_peek<C_SYNC>(y);
          ETICK(E0 + g4);
        });
        ps[3] = (e[12] + e[13]) + (e[14] + e[15]);
        float a, c; both_halves(__builtin_fmaf(ps[3], keep, ps[2]) + (ps[0] + ps[1]), a, c);
        float rs = __builtin_amdgcn_rcpf(a + c);
        asm volatile("" : "+v"(rs));
        ETICK(E0 + 4);
        if constexpr (sg == 0) {
          z_wait_pre<(isH ? zidx(isH, OU, 6) : 18)>(z2, pre);
          sts128(ea.vw, vf[0]); sts128(ea.vw + 1024, vf[1]);
          asm volatile("" ::: "memory");
          lg = *(LAS unsigned short*)(size_t)ea.lr;
        } else {
          lds_wait_pre<C_SYNC, 1>(y.ra, pre);
#pragma unroll
          for (int i = 0; i < 8; ++i) vv[i >> 1][i & 1] = lds128(ea.vr + (i >> 1) * 2048 + (i & 1) * 256);
        }
        SB();
        ETICK(E0 + 5);
        sfor<4>([&](auto gc) {
          constexpr int g4 = decltype(gc)::v;
          sts64(ea.pw[g4] + sg * 2048, u32x2{cvtpk(e[4 * g4] * rs, e[4 * g4 + 1] * rs), cvtpk(e[4 * g4 + 2] * rs, e[4 * g4 + 3] * rs)});
          ETICK(E0 + 6 + g4);
        });
        if constexpr (sg == 0) *(LAS u32*)(size_t)ea.lw = lg << 16;
        lds_post<C_SYNC>(y);
      });
      pre = lds_peek<C_SYNC>(y);
      ETICK(25);
      lds_wait_pre<C_SYNC, 2>(y.ra, pre);
      u32x4 pf[4];
#pragma unroll
      for (int u = 0; u < 4; ++u) pf[u] = trfrag(ea.pr[0] + u * 4096, ea.pr[1] + u * 4096);
      lds_post<C_SYNC>(y);
      SB();
      ETICK(26);
      f32x4 kp[2] = {{0.f, 0.f, 0.f, 0.f}, {0.f, 0.f, 0.f, 0.f}};
#pragma unroll
      for (int i = 0; i < 8; ++i)
        kp[i & 1] = __builtin_amdgcn_mfma_f32_16x16x32_bf16(__builtin_bit_cast(bf16x8, pf[i >> 1]), __builtin_bit_cast(bf16x8, vv[i >> 1][i & 1]), kp[i & 1], 0, 0, 0);
      asm volatile("" : "+v"(kp[0]), "+v"(kp[1]));
      ETICK(27);
      const auto sx0 = __builtin_amdgcn_permlane32_swap(cvtpk(kp[0].x, kp[0].y), cvtpk(kp[1].x, kp[1].y), false, false);
      const auto sx1 = __builtin_amdgcn_permlane32_swap(cvtpk(kp[0].z, kp[0].w), cvtpk(kp[1].z, kp[1].w), false, false);
      kvs = u32x4{sx0[0], sx1[0], sx0[1], sx1[1]};
      ETICK(28);
      OPS(MK(M_ST), MK(M_ST) + 1);
    }
    if constexpr (isH) {
      f32x16 acc[2][2];
#pragma unroll
      for (int i = 0; i < 4; ++i)
#pragma unroll
        for (int e = 0; e < 16; ++e) acc[i >> 1][i & 1][e] = 0.f;
      BPath bp = make_bpath(L_K3 + w * 2048, lane);
      u32x4 tf[2][2];
      asm volatile("s_waitcnt vmcnt(%2)" : "+v"(bf[0][0]), "+v"(bf[0][1]) : "n"(MK(M_ST) - T::find(OB, 0, 1)) : "memory");
      sts128(bp.wa, bf[0][0]); sts128(bp.wa + 1024, bf[0][1]);
      tf[0][0] = trfrag(bp.ra[0], bp.ra[0] + 1024); tf[0][1] = trfrag(bp.ra[1], bp.ra[1] + 1024);
      OPS(MK(M_ST) + 1, MK(M_JOB));
      __builtin_amdgcn_s_setprio(1);
      u32x4 lf[14][2];
      u32 lr[2][2];
      sfor<28>([&](auto nc) {
        constexpr int n = decltype(nc)::v, P = MK(M_JOB + n), NL = MK(M_JOB + n + 1) - P, nb = n < 13 ? n + 1 : 13;
        constexpr int k0 = n < CV0 ? 0 : n > 13 ? 9 : (n - CV0) * 9 / (14 - CV0), k1 = n < CV0 ? 0 : n > 13 ? 9 : (n + 1 - CV0) * 9 / (14 - CV0);
        constexpr int need = cmax(cmax(T::find(OA, n, 1), T::find(OB, nb, 1)), k1 > k0 ? T::find(OB, 18 + k1, 1) : 0);
        static_assert(need < P, "stream order");
        if constexpr (n == CV0) {
#pragma unroll
          for (int i = 0; i < 4; ++i) { lr[i >> 1][i & 1] = bp.ra[i & 1] + (L_R2 - L_K3) + (i >> 1) * 4 * 8192; pin1(lr[i >> 1][i & 1]); }
        }
        static_assert(T::find(OB, 18, 1) < T::find(OA, 14, 0), "tiles 14..18 have landed when job 14 starts");
        if constexpr (n < 13) landed4<P - 1 - need>(af[n][0], af[n][1], bf[nb][0], bf[nb][1]);
        else { VMWAIT(P - 1 - need); pin2(af[n][0], af[n][1]); }
        sfor<4>([&](auto uc) {
          constexpr int u = decltype(uc)::v, c = u >> 1, t = u & 1;
          if constexpr (n < 14) acc[c][t] = mfma(tf[n & 1][c], af[n][t], acc[c][t]); else acc[c][t] = mfma(lf[n - 14][c], af[n][t], acc[c][t]);
          SB();
          asm volatile("" : "+v"(acc[c][t]));
          if constexpr (n < 13) {
            if constexpr (u == 0) {
              sts128(bp.wa, bf[nb][0]); sts128(bp.wa + 1024, bf[nb][1]);
              tf[nb & 1][0] = trfrag(bp.ra[0], bp.ra[0] + 1024); tf[nb & 1][1] = trfrag(bp.ra[1], bp.ra[1] + 1024);
            }
            SB();
          }
          if constexpr (n >= 14 && n < 19 && u == 0) {
            pin2(bf[n][0], bf[n][1]);
            sts128(bp.wa, bf[n][0]); sts128(bp.wa + 1024, bf[n][1]);
            lf[n - 5][0] = trfrag(bp.ra[0], bp.ra[0] + 1024); lf[n - 5][1] = trfrag(bp.ra[1], bp.ra[1] + 1024);
            SB();
          }
          if constexpr (u >= 1 && k0 + u - 1 < k1) {
            constexpr int k = k0 + u - 1;
#pragma unroll
            for (int i = 0; i < 2; ++i) lf[k][i] = trfrag(lr[k >> 2 ? 1 : 0][i] + (k < 4 ? k : k - 4) * 8192, lr[k >> 2 ? 1 : 0][i] + (k < 4 ? k : k - 4) * 8192 + 1024);
            SB();
          }
          OPS(P + NL * u / 4, P + NL * (u + 1) / 4);
        });
      });
      dump_tile(acc, L_R2 + w * 2048, lane);
    }
  }
  if (isH) {
    const int tok = tid >> 3, pc = tid & 7;
    const u32 a = (tok >> 3) * 8192 + (tok & 7) * 256 + (((pc * 2) ^ (tok & 7)) * 16);
    f32x4 v[8][2];
#pragma unroll
    for (int u = 0; u < 8; ++u)
#pragma unroll
      for (int i = 0; i < 2; ++i) v[u][i] = __builtin_bit_cast(f32x4, lds128((u < 4 ? L_R2 + u * 2048 : L_R1 + (u - 4) * 2048) + (a ^ (i * 16))));
    const float omg = *(LAS float*)(size_t)(L_GW + tok * 4);
    const f32x4 s0 = ((v[0][0] + v[1][0]) + (v[2][0] + v[3][0])) * omg + ((v[4][0] + v[5][0]) + (v[6][0] + v[7][0]));
    const f32x4 s1 = ((v[0][1] + v[1][1]) + (v[2][1] + v[3][1])) * omg + ((v[4][1] + v[5][1]) + (v[6][1] + v[7][1]));
    const u32x4 res = {cvtpk(s0.x, s0.y), cvtpk(s0.z, s0.w), cvtpk(s1.x, s1.y), cvtpk(s1.z, s1.w)};
    *(u32x4*)(p.out + (rt64 * 64 + tok) * 896 + ct2 * 128 + pc * 16) = res;
  }
}

__global__ __launch_bounds__(512, 2) void fused(Params p) {
  asm volatile(".subsection 2\n.Lhk_text_end:\n\t.fill 256, 4, 0xBF800000\n\t.subsection 0");
  const int wave = rfl(threadIdx.x >> 6);
  if (wave == 1) {
    u64 pc; u32 len;
    asm volatile("s_getpc_b64 %0\n.Lpc%=:\n\ts_mov_b32 %1, .Lhk_text_end-.Lpc%=" : "=s"(pc), "=s"(len));
    const u32 off = ((threadIdx.x & 63) * 32 + (blockIdx.x >> 3)) * 128;
    asm volatile("s_mov_b32 m0, %2\n\ts_nop 0\n\tglobal_load_lds_dword %0, %1" ::"v"(off < len ? off : 0u), "s"(pc), "s"(L_DUM) : "memory");
  }
  *(LAS u32*)(size_t)(L_Z + (threadIdx.x >> 6) * 512 + 256 + (threadIdx.x & 63) * 4) = 1u;
  *(LAS u32*)(size_t)(L_CNT + (threadIdx.x & 15) * 4) = 0u;
  asm volatile("s_waitcnt lgkmcnt(0)\n\ts_barrier" ::: "memory");
  if (!((blockIdx.x & 1) && blockIdx.x >= 192)) {
    if (wave < 4) body<true, true>(p, wave); else body<false, true>(p, wave);
  } else {
    if (wave < 4) body<true, false>(p, wave); else body<false, false>(p, wave);
  }
}
}
#if defined(__HIP_DEVICE_COMPILE__)
#pragma clang attribute pop
#endif

extern "C" void kernel_launch(void* const* d_in, const int* in_sizes, int n_in, void* d_out, int out_size, void* d_ws, size_t ws_size,
                              hipStream_t stream) {
  hk::Params p;
  p.x = (const char*)d_in[0]; p.wqkv = (const char*)d_in[1]; p.wphi = (const char*)d_in[2]; p.wo = (const char*)d_in[3];
  p.wg = (const char*)d_in[5]; p.wu = (const char*)d_in[6]; p.wd = (const char*)d_in[7]; p.gw = (const char*)d_in[8];
  p.out = (char*)d_out; p.ws = (char*)d_ws;
  (void)hipFuncSetAttribute((const void*)hk::fused, hipFuncAttributeMaxDynamicSharedMemorySize, hk::L_TOTAL);
  hk::fused<<<256, 512, hk::L_TOTAL, stream>>>(p);
}
```
